```python
import torch

def _softmax(x, axis=-1):
    return torch.softmax(x, dim=axis)
def _gelu(x):
    return torch.nn.functional.gelu(x, approximate="tanh")
def _silu(x):
    return torch.nn.functional.silu(x)
def _sigmoid(x):
    return torch.sigmoid(x)
def _layernorm(x, axis=-1, eps=1e-5):
    m = x.mean(dim=axis, keepdim=True)
    v = x.var(dim=axis, keepdim=True, unbiased=False)
    return (x - m) / torch.sqrt(v + eps)
def _scatter_add(tgt, idx, src, axis=0):
    return tgt.clone().index_add_(axis, idx, src)
def _segment_sum(src, seg, n):
    out = torch.zeros((n,) + src.shape[1:], dtype=src.dtype)
    return out.index_add_(0, seg, src)
def _pad(x, pw):
    flat = tuple(v for pair in reversed(pw) for v in pair)
    return torch.nn.functional.pad(x, flat)

kernel_name = 'sequential_preLN__grouped_kv_mqa__mixer_s_mlp__prompt'

def setup_inputs():

    _w = (lambda a: (lambda t: t.to(torch.bfloat16) if t.is_floating_point() else t)(torch.as_tensor(a)))
    def _A_setup():
        B, S, D, H = 1, 2048, 512, 32
        Dh = D // H
        return {
            "x": _w((lambda *s: torch.randn(*s, dtype=torch.bfloat16) * 0.05)(B, S, D)),
            "wq": _w((lambda *s: torch.randn(*s, dtype=torch.bfloat16) * 0.05)(D, H * Dh)),
            "wk": _w((lambda *s: torch.randn(*s, dtype=torch.bfloat16) * 0.05)(D, Dh)),
            "wv": _w((lambda *s: torch.randn(*s, dtype=torch.bfloat16) * 0.05)(D, Dh)),
            "wo": _w((lambda *s: torch.randn(*s, dtype=torch.bfloat16) * 0.05)(H * Dh, D)),
        }
    _A_in = _A_setup()

    def _B_setup():
        B, S, D, F = 1, 2048, 512, 1792
        return {
            "x":  _w((lambda *s: torch.randn(*s, dtype=torch.bfloat16) * 0.05)(B, S, D)),
            "w1": _w((lambda *s: torch.randn(*s, dtype=torch.bfloat16) * 3.2)(D, D) * (D ** -0.5)),
            "w2": _w((lambda *s: torch.randn(*s, dtype=torch.bfloat16) * 3.2)(D, D) * (D ** -0.5)),
            "w3": _w((lambda *s: torch.randn(*s, dtype=torch.bfloat16) * 3.2)(D, F) * (D ** -0.5)),
            "w4": _w((lambda *s: torch.randn(*s, dtype=torch.bfloat16) * 1.6)(F, D) * (F ** -0.5)),
        }
    _B_in = _B_setup()

    return {
        "A_x": _A_in["x"],
        "A_wq": _A_in["wq"],
        "A_wk": _A_in["wk"],
        "A_wv": _A_in["wv"],
        "A_wo": _A_in["wo"],
        "B_x": _B_in["x"],
        "B_w1": _B_in["w1"],
        "B_w2": _B_in["w2"],
        "B_w3": _B_in["w3"],
        "B_w4": _B_in["w4"],
    }

def reference(A_x, A_wq, A_wk, A_wv, A_wo, B_x, B_w1, B_w2, B_w3, B_w4):
    def _A_ref(x, wq, wk, wv, wo):
        B, S, D = x.shape
        H = 32
        Dh = D // H
        q = (lambda x, *d: x.permute(*d))((x @ wq).reshape(B, S, H, Dh), 0, 2, 1, 3)
        k = x @ wk
        v = x @ wv
        scores = torch.einsum("bhqd,bkd->bhqk", q, k) * (Dh ** -0.5)
        p = _softmax(scores, axis=-1)
        o = torch.einsum("bhqk,bkd->bhqd", p, v)
        return (lambda x, *d: x.permute(*d))(o, 0, 2, 1, 3).reshape(B, S, H * Dh) @ wo

    def _B_ref(x, w1, w2, w3, w4):
        B, S, D = x.shape
        h = _gelu(_layernorm(x, axis=-1) @ w1)
        c = D // 4
        g0 = _pad(h[:, :, 0*c:1*c], ((0, 0), (1, 0), (0, 0)))[:, :S, :]
        g1 = _pad(h[:, :, 1*c:2*c], ((0, 0), (0, 1), (0, 0)))[:, 1:, :]
        g2 = _pad(h[:, :, 2*c:3*c], ((0, 0), (2, 0), (0, 0)))[:, :S, :]
        g3 = _pad(h[:, :, 3*c:   ], ((0, 0), (0, 2), (0, 0)))[:, 2:, :]
        hs = torch.concatenate([g0, g1, g2, g3], axis=-1) @ w2
        x1 = x + hs
        h2 = _gelu(_layernorm(x1, axis=-1) @ w3) @ w4
        return x1 + h2

    x = A_x
    h = x + _A_ref(_layernorm(x, axis=-1), A_wq, A_wk, A_wv, A_wo)
    h2 = h + _B_ref(_layernorm(h, axis=-1), B_w1, B_w2, B_w3, B_w4)
    return h2
```

```cpp
#include <hip/hip_runtime.h>
#include <hip/hip_bf16.h>
#include <stdint.h>

typedef unsigned short u16;
__device__ __forceinline__ float bf2f(u16 v) { return __uint_as_float(((unsigned)v) << 16); }
__device__ __forceinline__ u16 f2bf(float f) { __hip_bfloat16 b = __float2bfloat16(f); return *(u16*)&b; }

constexpr int S = 2048, D = 512, H = 32, DH = 16, F = 1792;

__global__ void ln_kernel(const float* in, float* out) {
    int r = blockIdx.x; int t = threadIdx.x;
    __shared__ float red[256];
    float a = in[r*D + t], b = in[r*D + 256 + t];
    red[t] = a + b; __syncthreads();
    for (int s = 128; s > 0; s >>= 1) { if (t < s) red[t] += red[t+s]; __syncthreads(); }
    float m = red[0] / D; __syncthreads();
    float da = a - m, db = b - m;
    red[t] = da*da + db*db; __syncthreads();
    for (int s = 128; s > 0; s >>= 1) { if (t < s) red[t] += red[t+s]; __syncthreads(); }
    float rs = rsqrtf(red[0] / D + 1e-5f);
    out[r*D + t] = da * rs; out[r*D + 256 + t] = db * rs;
}
__global__ void cvt_kernel(const u16* in, float* out, int n) { int i = blockIdx.x*256 + threadIdx.x; if (i < n) out[i] = bf2f(in[i]); }

__device__ __forceinline__ float gelu(float x) { float u = 0.7978845608f * (x + 0.044715f*x*x*x); return 0.5f*x*(1.f + tanhf(u)); }
__global__ void gemm_kernel(const float* A, const u16* W, float* C, int K, int N, int mode, const float* add1, const float* add2, int shift) {
    __shared__ float As[16][64+1]; __shared__ float Ws[16][64+1];
    int r0 = blockIdx.y*64, c0 = blockIdx.x*64; int tx = threadIdx.x & 15, ty = threadIdx.x >> 4;
    float acc[4][4] = {};
    for (int k0 = 0; k0 < K; k0 += 16) {
        for (int i = threadIdx.x; i < 1024; i += 256) {
            int rr = i >> 4, kk = i & 15; int row = r0 + rr; int k = k0 + kk; float v;
            if (shift) { int g = k / 128; int sh = (g==0) ? 1 : (g==1) ? -1 : (g==2) ? 2 : -2; int sr = row - sh; v = (sr >= 0 && sr < S) ? A[sr*K + k] : 0.f; }
            else v = A[row*K + k];
            As[kk][rr] = v;
            int kk2 = i >> 6, cc = i & 63; Ws[kk2][cc] = (c0 + cc < N) ? bf2f(W[(k0+kk2)*N + c0 + cc]) : 0.f;
        }
        __syncthreads();
        for (int kk = 0; kk < 16; ++kk) for (int i = 0; i < 4; ++i) for (int j = 0; j < 4; ++j) acc[i][j] += As[kk][ty*4+i] * Ws[kk][tx*4+j];
        __syncthreads();
    }
    for (int i = 0; i < 4; ++i) for (int j = 0; j < 4; ++j) {
        int row = r0 + ty*4+i, col = c0 + tx*4+j; if (col >= N) continue;
        float v = acc[i][j]; if (mode == 1) v = gelu(v);
        if (add1) v += add1[row*N + col]; if (add2) v += add2[row*N + col];
        C[row*N + col] = v;
    }
}
__global__ void attn_kernel(const float* q, const float* k, const float* v, float* o) {
    int idx = blockIdx.x*256 + threadIdx.x; int row = idx >> 5, h = idx & 31;
    float qq[16]; for (int d = 0; d < 16; ++d) qq[d] = q[row*D + h*16 + d] * 0.25f;
    float m = -1e30f, l = 0.f, acc[16] = {};
    for (int j = 0; j < S; ++j) {
        float s = 0; for (int d = 0; d < 16; ++d) s += qq[d] * k[j*16 + d];
        float mn = fmaxf(m, s); float al = __expf(m - mn); float p = __expf(s - mn);
        l = l*al + p; for (int d = 0; d < 16; ++d) acc[d] = acc[d]*al + p * v[j*16 + d];
        m = mn;
    }
    for (int d = 0; d < 16; ++d) o[row*D + h*16 + d] = acc[d] / l;
}
__global__ void out_kernel(const float* in, u16* out, int n) { int i = blockIdx.x*256 + threadIdx.x; if (i < n) out[i] = f2bf(in[i]); }

extern "C" void kernel_launch(void* const* d_in, const int* in_sizes, int n_in, void* d_out, int out_size, void* d_ws, size_t ws_size, hipStream_t stream) {
    const u16* x = (const u16*)d_in[0]; const u16* wq = (const u16*)d_in[1]; const u16* wk = (const u16*)d_in[2]; const u16* wv = (const u16*)d_in[3]; const u16* wo = (const u16*)d_in[4];
    const u16* w1 = (const u16*)d_in[6]; const u16* w2 = (const u16*)d_in[7]; const u16* w3 = (const u16*)d_in[8]; const u16* w4 = (const u16*)d_in[9];
    float* ws = (float*)d_ws; const int SD = S*D;
    float *xf = ws, *xn = ws + SD, *q = ws + 2*SD, *k = ws + 3*SD, *v = ws + 4*SD, *o = ws + 5*SD, *h = ws + 6*SD, *xp = ws + 7*SD, *y = ws + 8*SD, *t = ws + 9*SD, *x1 = ws + 10*SD, *z = ws + 11*SD, *u = ws + 12*SD, *res = ws + 16*SD;
    cvt_kernel<<<SD/256, 256, 0, stream>>>(x, xf, SD);
    ln_kernel<<<S, 256, 0, stream>>>(xf, xn);
    gemm_kernel<<<dim3(8, 32), 256, 0, stream>>>(xn, wq, q, D, D, 0, nullptr, nullptr, 0);
    gemm_kernel<<<dim3(1, 32), 256, 0, stream>>>(xn, wk, k, D, 16, 0, nullptr, nullptr, 0);
    gemm_kernel<<<dim3(1, 32), 256, 0, stream>>>(xn, wv, v, D, 16, 0, nullptr, nullptr, 0);
    attn_kernel<<<S*H/256, 256, 0, stream>>>(q, k, v, o);
    gemm_kernel<<<dim3(8, 32), 256, 0, stream>>>(o, wo, h, D, D, 0, xf, nullptr, 0);
    ln_kernel<<<S, 256, 0, stream>>>(h, xp);
    ln_kernel<<<S, 256, 0, stream>>>(xp, y);
    gemm_kernel<<<dim3(8, 32), 256, 0, stream>>>(y, w1, t, D, D, 1, nullptr, nullptr, 0);
    gemm_kernel<<<dim3(8, 32), 256, 0, stream>>>(t, w2, x1, D, D, 0, xp, nullptr, 1);
    ln_kernel<<<S, 256, 0, stream>>>(x1, z);
    gemm_kernel<<<dim3(28, 32), 256, 0, stream>>>(z, w3, u, D, F, 1, nullptr, nullptr, 0);
    gemm_kernel<<<dim3(8, 32), 256, 0, stream>>>(u, w4, res, F, D, 0, x1, h, 0);
    out_kernel<<<SD/256, 256, 0, stream>>>(res, (u16*)d_out, SD);
}
```

```cpp
#include <hip/hip_runtime.h>
#include <stdint.h>

typedef unsigned short u16;
typedef unsigned int u32;
typedef unsigned long long u64;
typedef short bf16x8 __attribute__((ext_vector_type(8)));
typedef short s16x4 __attribute__((ext_vector_type(4)));
typedef float f32x2 __attribute__((ext_vector_type(2)));
typedef float f32x4 __attribute__((ext_vector_type(4)));
typedef float f32x16 __attribute__((ext_vector_type(16)));
typedef u32 u32x2 __attribute__((ext_vector_type(2)));
typedef u32 u32x4 __attribute__((ext_vector_type(4)));
typedef __bf16 bf16x2_t __attribute__((ext_vector_type(2)));
#define LAS __attribute__((address_space(3)))
#define GAS __attribute__((address_space(1)))
#define DI __device__ __forceinline__

constexpr u32 WS_EPOCH = 0x0;
constexpr u32 WS_FKV = 0x1000;
constexpr u32 WS_FW = 0x5000;
constexpr u32 WS_K = 0x10000;
constexpr u32 WS_V = 0x20000;
constexpr u32 WS_CS1 = 0x30000;
constexpr u32 WS_CS3 = 0x31000;
constexpr u32 WS_DBG = 0x1E00000;
constexpr u32 WS_WO = 0x100000, WS_W1 = 0x180000, WS_W2 = 0x200000, WS_W3 = 0x280000, WS_W4 = 0x440000;
constexpr u32 WS_OX = 0x600000, WS_HX = 0x800000, WS_TS = 0xA00000, WS_X1 = 0xC00000, WS_UX = 0xE00000;
constexpr u32 WS_STAMP = 0x2000000;
constexpr u32 WS_END = 0x2100000;
constexpr u32 OOB = 0x80000000u;

constexpr u32 L_XIMG = 0;
constexpr u32 L_WQ = 65536;
constexpr u32 L_WKV = 131072;
constexpr u32 L_S1 = 147456;
constexpr u32 L_K = 0, L_V = 65536, L_ONES = 131072;
constexpr u32 L_BIMG = 0;
constexpr u32 L_RED = 0;
constexpr u32 L_PART = 98304;
constexpr u32 L_STAT = 106496;
constexpr u32 L_RES = 110592;
constexpr u32 L_CS = 131072 + 8192;
constexpr u32 L_DUMMY = 143360;
constexpr u32 L_STAMP = 161792;
constexpr u32 LDS_BYTES = 163840;

__shared__ __attribute__((aligned(16))) char smem[LDS_BYTES];

constexpr u32 SENT = 0xFFFFFFFFu;
constexpr float LN_EPS = 1e-5f;
constexpr float QSCALE = 0.25f * 1.4426950408889634f;

DI f32x16 mfma32(bf16x8 a, bf16x8 b, f32x16 c) { return __builtin_amdgcn_mfma_f32_32x32x16_bf16(a, b, c, 0, 0, 0); }
DI f32x4 mfma16(bf16x8 a, bf16x8 b, f32x4 c) { return __builtin_amdgcn_mfma_f32_16x16x32_bf16(a, b, c, 0, 0, 0); }
DI u32 cvtpk(float lo, float hi) { f32x2 v = {lo, hi}; bf16x2_t b = __builtin_convertvector(v, bf16x2_t); return __builtin_bit_cast(u32, b); }
DI u32 canon(u32 d) { return d < 0xFFFFFFFEu ? d : 0xFFFFFFFEu; }
DI float bflo(u32 d) { return __uint_as_float(d << 16); }
DI float bfhi(u32 d) { return __uint_as_float(d & 0xFFFF0000u); }
DI float gelu(float x) {
  const float c1 = -2.0f * 0.7978845608028654f * 1.4426950408889634f, c2 = c1 * 0.044715f;
  const float e = __builtin_amdgcn_exp2f(x * (c1 + c2 * x * x));
  return x * __builtin_amdgcn_rcpf(1.0f + e);
}

#define BAR() asm volatile("s_waitcnt lgkmcnt(0)\n\ts_barrier" ::: "memory")
#define WAITV0() asm volatile("s_waitcnt vmcnt(0)" ::: "memory")
#define FENCE() asm volatile("" ::: "memory")

template <class T> DI T ldsr(u32 off) { return *(const LAS T*)((LAS char*)smem + off); }
template <class T> DI void ldsw(u32 off, T v) { *(LAS T*)((LAS char*)smem + off) = v; }
DI s16x4 ldstr(u32 off) {
  typedef short v4i16_t __attribute__((ext_vector_type(4)));
  return __builtin_bit_cast(s16x4, __builtin_amdgcn_ds_read_tr16_b64_v4i16((LAS v4i16_t*)((LAS char*)smem + off)));
}
DI bf16x8 cat(s16x4 a, s16x4 b) { return __builtin_shufflevector(a, b, 0, 1, 2, 3, 4, 5, 6, 7); }
DI u32 lds_base() { return (u32)(uintptr_t)(LAS char*)smem; }

DI void dma1(const void* sbase, u32 voff, u32 ldsdst) {
  u32 keep;
  asm volatile("s_mov_b32 %0, m0\n\ts_mov_b32 m0, %3\n\ts_nop 2\n\tglobal_load_lds_dwordx4 %1, %2\n\ts_mov_b32 m0, %0"
               : "=&s"(keep) : "v"(voff), "s"(sbase), "s"(ldsdst) : "memory");
}
DI void dma4_sc1(const void* sbase, u32 voff, u32 ldsdst) {
  u32 keep;
  asm volatile("s_mov_b32 %0, m0\n\ts_mov_b32 m0, %3\n\ts_nop 2\n\t"
               "global_load_lds_dwordx4 %1, %2 sc1\n\tglobal_load_lds_dwordx4 %1, %2 offset:1024 sc1\n\t"
               "global_load_lds_dwordx4 %1, %2 offset:2048 sc1\n\tglobal_load_lds_dwordx4 %1, %2 offset:3072 sc1\n\ts_mov_b32 m0, %0"
               : "=&s"(keep) : "v"(voff), "s"(sbase), "s"(ldsdst) : "memory");
}

DI void dma1_sc1(const void* sbase, u32 voff, u32 ldsdst) {
  u32 keep;
  asm volatile("s_mov_b32 %0, m0\n\ts_mov_b32 m0, %3\n\ts_nop 2\n\tglobal_load_lds_dwordx4 %1, %2 sc1\n\ts_mov_b32 m0, %0"
               : "=&s"(keep) : "v"(voff), "s"(sbase), "s"(ldsdst) : "memory");
}

typedef __amdgpu_buffer_rsrc_t rsrc_t;
DI rsrc_t mkrsrc(const void* p, u32 bytes) { return __builtin_amdgcn_make_buffer_rsrc((void*)p, 0, bytes, 0x00020000); }
DI u32x4 ld16(rsrc_t r, u32 voff, u32 soff) { return __builtin_amdgcn_raw_buffer_load_b128(r, voff, soff, 16); }
DI void st16(rsrc_t r, u32 voff, u32 soff, u32x4 v) { __builtin_amdgcn_raw_buffer_store_b128(v, r, voff, soff, 16); }
DI void st8(rsrc_t r, u32 voff, u32 soff, u32x2 v) { __builtin_amdgcn_raw_buffer_store_b64(v, r, voff, soff, 16); }
DI void st4(rsrc_t r, u32 voff, u32 soff, u32 v) { __builtin_amdgcn_raw_buffer_store_b32(v, r, voff, soff, 16); }
DI u32 ld4(rsrc_t r, u32 voff, u32 soff) { return __builtin_amdgcn_raw_buffer_load_b32(r, voff, soff, 16); }
DI bf16x8 asbf(u32x4 v) { return __builtin_bit_cast(bf16x8, v); }
DI u32x4 asu(bf16x8 v) { return __builtin_bit_cast(u32x4, v); }
DI u32 max4(u32x4 v) { u32 a = v[0] > v[1] ? v[0] : v[1]; u32 b = v[2] > v[3] ? v[2] : v[3]; return a > b ? a : b; }
DI u32 umax(u32 a, u32 b) { return a > b ? a : b; }

constexpr u64 SPIN_TICKS = 400000ull;
DI bool timed_out(u64 t0) { return __builtin_amdgcn_s_memrealtime() - t0 > SPIN_TICKS; }

#define STAMP(i) do { } while (0)
#define MFMA_GAP16(v) asm volatile("s_nop 7\n\ts_nop 3" : "+v"(v))

DI void wait_flags(rsrc_t wsr, u32 base, u32 taglo, u32 taghi, int lane, u64 t0) {
  for (;;) {
    const u32x4 a = ld16(wsr, lane * 32, base), b = ld16(wsr, lane * 32 + 16, base);
    const bool ok = a[0] == taglo && a[1] == taghi && a[2] == taglo && a[3] == taghi && b[0] == taglo && b[1] == taghi && b[2] == taglo && b[3] == taghi;
    if (__all(ok)) break;
    if (timed_out(t0)) break;
    __builtin_amdgcn_s_sleep(2);
    FENCE();
  }
}

template <u32 R1, u32 R2, u32 R3, int YOUNGER>
DI void gather_finish(const char* src, u32 ldsimg, int w, int lane, u32 roff, bf16x8 (&f)[4], u64 t0) {
  asm volatile("s_waitcnt vmcnt(%0)" :: "n"(YOUNGER) : "memory");
  for (;;) {
    f[0] = ldsr<bf16x8>(roff); f[1] = ldsr<bf16x8>(roff + R1); f[2] = ldsr<bf16x8>(roff + R2); f[3] = ldsr<bf16x8>(roff + R3);
    const u32 m = umax(umax(max4(asu(f[0])), max4(asu(f[1]))), umax(max4(asu(f[2])), max4(asu(f[3]))));
    if (!__any(m == SENT)) break;
    if (timed_out(t0)) break;
    __builtin_amdgcn_s_sleep(2);
    dma4_sc1(src, (u32)(w * 4096 + lane * 16), lds_base() + ldsimg + w * 4096);
    WAITV0();
  }
}

DI bf16x8 ones_frag() { return asbf((u32x4){0x3F803F80u, 0x3F803F80u, 0x3F803F80u, 0x3F803F80u}); }
DI float diag_pick(f32x4 g, int lane) { const int r = lane & 3; float a = (r & 1) ? g[1] : g[0], b = (r & 1) ? g[3] : g[2]; return (r & 2) ? b : a; }

struct RowStat { float mu, rstd, var; };
DI RowStat finish_stat(float s, float q) {
  RowStat r; r.mu = s * (1.0f / 512.0f);
  r.var = fmaxf(q * (1.0f / 512.0f) - r.mu * r.mu, 0.0f);
  r.rstd = __builtin_amdgcn_rsqf(r.var + LN_EPS);
  return r;
}

#define A3_N0 12
#define A3_N1 24
#define SL0 0
#define SL1 0
#define SLEEP0() do { if (SL0) __builtin_amdgcn_s_sleep(SL0); } while (0)
#define SLEEP1() do { if (SL1) __builtin_amdgcn_s_sleep(SL1); } while (0)

__global__ __launch_bounds__(512, 2) void fused_block_kernel(const u16* __restrict__ x, const u16* __restrict__ wq, const u16* __restrict__ wk,
                                                             const u16* __restrict__ wv, const u16* __restrict__ wo, const u16* __restrict__ w1,
                                                             const u16* __restrict__ w2, const u16* __restrict__ w3, const u16* __restrict__ w4,
                                                             u16* __restrict__ out, char* ws) {
  const int tid = threadIdx.x, lane = tid & 63, w = __builtin_amdgcn_readfirstlane(tid >> 6);
  const int b = blockIdx.x;
  const int cl = (b & 7) * 4 + (b >> 6), j = (b >> 3) & 7;
  const u64 t0 = __builtin_amdgcn_s_memrealtime();
  const rsrc_t wsr = mkrsrc(ws, WS_END);
  const u64 tag = *(const GAS u64*)(ws + WS_EPOCH) + 1ull;
  const u32 taglo = (u32)tag, taghi = (u32)(tag >> 32);
  const u32 lb = lds_base();
  const bf16x8 one = ones_frag();
  const int i16 = lane & 15, g4 = lane >> 4, r32 = lane & 31, h2 = lane >> 5;
  STAMP(0);

  const int kvrt = j >> 1, kvmat = j & 1;
  auto grow = [&](int rho) { return (rho < 32 ? 0 : 1024 - 32) + 32 * cl + rho; };
  u32 pe[4][8];
  {
#pragma unroll
    for (int i = 0; i < 2; ++i) {
      const int rho = 16 * kvrt + 2 * w + i;
      dma1(x, (u32)(grow(rho) * 1024 + ((lane ^ (rho & 15)) * 16)), lb + L_XIMG + rho * 1024);
    }
    const u16* wkv = kvmat ? wv : wk;
#pragma unroll
    for (int i = 0; i < 2; ++i) dma1(wkv, (u32)((2 * w + i) * 1024 + lane * 16), lb + L_WKV + (2 * w + i) * 1024);
    if (w > 0) {
#pragma unroll
      for (int i = 0; i < 16; ++i) {
        const int t = (w - 1) * 16 + i;
        if (t < 48) {
          const int rho = t < 16 * kvrt ? t : t + 16;
          dma1(x, (u32)(grow(rho) * 1024 + ((lane ^ (rho & 15)) * 16)), lb + L_XIMG + rho * 1024);
        } else {
          const int g = t - 48, row = 8 * g + (lane >> 3), c = (lane & 7) ^ (4 * ((row >> 1) & 1));
          dma1(wq, (u32)(row * 1024 + j * 128 + c * 16), lb + L_WQ + g * 1024);
        }
      }
    }
    {
      const u16* psrc = b < 56 ? w3 : b < 72 ? w1 : b < 104 ? wo : b < 136 ? w2 : w4;
      const u32 prow = b < 56 ? 3584u : 1024u;
      const u32 pbytes = (b < 56 || b >= 136) ? 512u * 1792u * 2u : 512u * 512u * 2u;
      const bool perm = (b >= 72 && b < 104) || b >= 136;
      u32 pv[4] = {OOB, OOB, OOB, OOB};
      if (b < 56) {
#pragma unroll
        for (int t = 0; t < 4; ++t) pv[t] = (u32)(((16 * (4 * w + t) + 8 * h2) * 1792 + 32 * b + r32) * 2);
      } else if (b < 72) {
#pragma unroll
        for (int t = 0; t < 4; ++t) pv[t] = (u32)(((32 * (2 * w + (t & 1)) + 8 * g4) * 512 + 16 * (2 * (b - 56) + (t >> 1)) + i16) * 2);
      } else if (b < 104) {
#pragma unroll
        for (int t = 0; t < 2; ++t) { const int f = (b - 72) * 16 + 2 * w + t; pv[t] = (u32)(((32 * (f & 15) + 16 * (g4 >> 1) + 4 * (g4 & 1)) * 512 + 16 * (f >> 4) + i16) * 2); }
      } else if (b < 136) {
#pragma unroll
        for (int t = 0; t < 2; ++t) { const int f = (b - 104) * 16 + 2 * w + t; pv[t] = (u32)(((32 * (f & 15) + 8 * g4) * 512 + 16 * (f >> 4) + i16) * 2); }
      } else if (b < 248) {
#pragma unroll
        for (int t = 0; t < 2; ++t) {
          const int f = (b - 136) * 16 + 2 * w + t;
          const int ks = f % 14, ft = f / 14, T = ft & 1, wc = (ft >> 1) & 7, jj = ft >> 4;
          const int a = r32 >> 3, hh = (r32 >> 2) & 1, bb = r32 & 3, c = 16 * (a >> 1) + 8 * hh + 4 * (a & 1) + bb;
          pv[t] = (u32)(((224 * jj + 16 * ks + 4 * h2) * 512 + 64 * wc + 32 * T + c) * 2);
        }
      }
      const rsrc_t pr = mkrsrc(psrc, pbytes);
#pragma unroll
      for (int t = 0; t < 4; ++t)
#pragma unroll
        for (int jj = 0; jj < 8; ++jj) {
          const u32 dk = perm ? (u32)(8 * (jj >> 2) + (jj & 3)) : (u32)jj;
          pe[t][jj] = (u16)__builtin_amdgcn_raw_buffer_load_b16(pr, pv[t], dk * prow, 0);
        }
    }
    if (w > 0) asm volatile("s_waitcnt vmcnt(48)" ::: "memory"); else asm volatile("s_waitcnt vmcnt(32)" ::: "memory");
    BAR();
  }
  STAMP(1);

  constexpr u32 S_PM = L_S1, S_PC = L_S1 + 8192, S_PS = L_S1 + 8704, S_PQ = L_S1 + 9216, S_FIN = L_S1 + 9728;
  constexpr u32 S_QS = L_S1 + 10240, S_QC = L_S1 + 11264, S_CSP = L_S1 + 12288;
  {
    f32x4 am = {0, 0, 0, 0}, ac = {0, 0, 0, 0}, as = {0, 0, 0, 0}, aq = {0, 0, 0, 0};
    bf16x8 xf[2], wf[2];
#pragma unroll
    for (int t = 0; t < 2; ++t) {
      const int ks = 2 * w + t;
      xf[t] = ldsr<bf16x8>(L_XIMG + (16 * kvrt + i16) * 1024 + (((4 * ks + g4) ^ i16) * 16));
      const u32 wa = L_WKV + (32 * ks + 8 * g4 + (i16 >> 2)) * 32 + (i16 & 3) * 8;
      wf[t] = cat(ldstr(wa), ldstr(wa + 128));
    }
#pragma unroll
    for (int t = 0; t < 2; ++t) {
      const bf16x8 pa = kvmat ? xf[t] : wf[t], pb = kvmat ? wf[t] : xf[t];
      const bf16x8 ca = kvmat ? one : wf[t], cb = kvmat ? wf[t] : one;
      am = mfma16(pa, pb, am); ac = mfma16(ca, cb, ac);
      as = mfma16(one, xf[t], as); aq = mfma16(xf[t], xf[t], aq);
    }
    MFMA_GAP16(am); MFMA_GAP16(aq);
    ldsw<f32x4>(S_PM + w * 1024 + lane * 16, am);
    if (kvmat) { if (lane < 16) ldsw<float>(S_PC + (w * 16 + lane) * 4, ac[0]); }
    else       { if (i16 == 0) ldsw<f32x4>(S_PC + (w * 16 + 4 * g4) * 4, ac); }
    if (lane < 16) ldsw<float>(S_PS + (lane * 8 + w) * 4, as[0]);
    if ((i16 >> 2) == g4) ldsw<float>(S_PQ + (i16 * 8 + w) * 4, diag_pick(aq, lane));
    BAR();
  }
  if (w == 0) {
    if (lane < 16) {
      const f32x4 s0 = ldsr<f32x4>(S_PS + lane * 32), s1 = ldsr<f32x4>(S_PS + lane * 32 + 16);
      const f32x4 q0 = ldsr<f32x4>(S_PQ + lane * 32), q1 = ldsr<f32x4>(S_PQ + lane * 32 + 16);
      const f32x4 s = s0 + s1, q = q0 + q1;
      const RowStat st = finish_stat((s[0] + s[1]) + (s[2] + s[3]), (q[0] + q[1]) + (q[2] + q[3]));
      ldsw<float>(S_FIN + lane * 4, st.mu); ldsw<float>(S_FIN + 64 + lane * 4, st.rstd);
    }
    f32x4 m = {0, 0, 0, 0}, c4 = {0, 0, 0, 0}; float c1 = 0.f;
#pragma unroll
    for (int ww = 0; ww < 8; ++ww) {
      m += ldsr<f32x4>(S_PM + ww * 1024 + lane * 16);
      if (kvmat) c1 += ldsr<float>(S_PC + (ww * 16 + i16) * 4); else c4 += ldsr<f32x4>(S_PC + (ww * 16 + 4 * g4) * 4);
    }
    asm volatile("s_waitcnt lgkmcnt(0)" ::: "memory");
    f32x4 v;
    const int R0 = grow(16 * kvrt);
    u32 off;
    if (kvmat) {
      const f32x4 mu = ldsr<f32x4>(S_FIN + g4 * 16), rs = ldsr<f32x4>(S_FIN + 64 + g4 * 16);
      v = rs * (m - mu * c1);
      off = WS_V + (R0 >> 5) * 1024 + ((R0 >> 4) & 1) * 512 + (g4 & 1) * 256 + i16 * 16 + (g4 >> 1) * 8;
    } else {
      const float mu = ldsr<float>(S_FIN + i16 * 4), rs = ldsr<float>(S_FIN + 64 + i16 * 4);
      v = rs * (m - mu * c4);
      off = WS_K + (R0 >> 5) * 1024 + (g4 & 1) * 512 + ((R0 & 31) + i16) * 16 + (g4 >> 1) * 8;
    }
    st8(wsr, off, 0, (u32x2){cvtpk(v[0], v[1]), cvtpk(v[2], v[3])});
    WAITV0();
    if (lane < 8) __hip_atomic_store((GAS u64*)(ws + WS_FKV + lane * 2048 + b * 8), tag, __ATOMIC_RELAXED, __HIP_MEMORY_SCOPE_AGENT);
  } else {
    asm volatile("s_waitcnt vmcnt(32)" ::: "memory");
  }
  STAMP(2);
  const int q_hp = w & 1, q_rt = (w >> 1) & 1, q_kh = w >> 2;
  const int head = 2 * q_hp + q_kh;
  bf16x8 qf;
  {
    BAR();
    STAMP(3);
    {
      f32x4 as = {0, 0, 0, 0}, aq = {0, 0, 0, 0};
      const int rt = w & 3, kh = w >> 2;
#pragma unroll
      for (int t = 0; t < 8; ++t) {
        const bf16x8 xf = ldsr<bf16x8>(L_XIMG + (16 * rt + i16) * 1024 + (((4 * (8 * kh + t) + g4) ^ i16) * 16));
        as = mfma16(one, xf, as); aq = mfma16(xf, xf, aq);
      }
      MFMA_GAP16(as); MFMA_GAP16(aq);
      if (lane < 16) ldsw<float>(S_QS + ((16 * rt + lane) * 2 + kh) * 4, as[0]);
      if ((i16 >> 2) == g4) ldsw<float>(S_QS + 512 + ((16 * rt + i16) * 2 + kh) * 4, diag_pick(aq, lane));
    }
    f32x16 acc = {}, accc = {};
    const int q = i16 >> 2, p = i16 & 3, cb = g4 & 1;
    const u32 wa = L_WQ + (16 * 16 * q_kh + 8 * h2 + q) * 128 + (((4 * q_hp + 2 * cb + (p >> 1)) ^ (4 * ((q >> 1) & 1))) * 16) + (p & 1) * 8;
    const u32 xa = L_XIMG + (32 * q_rt + r32) * 1024 + 32 * q_kh * 16;
#pragma unroll
    for (int t = 0; t < 16; ++t) {
      const bf16x8 af = cat(ldstr(wa + t * 2048), ldstr(wa + t * 2048 + 512));
      const bf16x8 bfr = ldsr<bf16x8>(xa + (t >> 3) * 256 + (((2 * (t & 7) + h2) ^ (r32 & 15)) * 16));
      acc = mfma32(af, bfr, acc);
    }
#pragma unroll
    for (int t = 0; t < 8; ++t) {
      const u32 wb = wa + (8 * q_rt + t) * 2048;
      accc = mfma32(cat(ldstr(wb), ldstr(wb + 512)), one, accc);
    }
    f32x4 k0, k1, s0, s1;
#pragma unroll
    for (int e = 0; e < 4; ++e) {
      k0[e] = q_kh ? acc[8 + e] : acc[e]; k1[e] = q_kh ? acc[12 + e] : acc[4 + e];
      s0[e] = q_kh ? acc[e] : acc[8 + e]; s1[e] = q_kh ? acc[4 + e] : acc[12 + e];
    }
    ldsw<f32x4>(L_WKV + w * 2048 + lane * 16, s0); ldsw<f32x4>(L_WKV + w * 2048 + 1024 + lane * 16, s1);
    asm volatile("s_nop 7\n\ts_nop 3" : "+v"(accc));
    if (r32 == 0) {
#pragma unroll
      for (int a = 0; a < 4; ++a) ldsw<f32x4>(S_QC + (((q_hp * 2 + q_rt) * 2 + q_kh) * 32 + 8 * a + 4 * h2) * 4, (f32x4){accc[4 * a], accc[4 * a + 1], accc[4 * a + 2], accc[4 * a + 3]});
    }
    BAR();
    k0 += ldsr<f32x4>(L_WKV + (w ^ 4) * 2048 + lane * 16); k1 += ldsr<f32x4>(L_WKV + (w ^ 4) * 2048 + 1024 + lane * 16);
    f32x4 c0 = {0, 0, 0, 0}, c1 = {0, 0, 0, 0};
#pragma unroll
    for (int s = 0; s < 4; ++s) {
      c0 += ldsr<f32x4>(S_QC + ((q_hp * 4 + s) * 32 + 16 * q_kh + 4 * h2) * 4);
      c1 += ldsr<f32x4>(S_QC + ((q_hp * 4 + s) * 32 + 16 * q_kh + 8 + 4 * h2) * 4);
    }
    const f32x2 sp = ldsr<f32x2>(S_QS + (32 * q_rt + r32) * 8), qp = ldsr<f32x2>(S_QS + 512 + (32 * q_rt + r32) * 8);
    const RowStat st = finish_stat(sp[0] + sp[1], qp[0] + qp[1]);
    const float sc = st.rstd * QSCALE;
    k0 = (k0 - st.mu * c0) * sc; k1 = (k1 - st.mu * c1) * sc;
    qf = asbf((u32x4){cvtpk(k0[0], k0[1]), cvtpk(k0[2], k0[3]), cvtpk(k1[0], k1[1]), cvtpk(k1[2], k1[3])});
  }
  STAMP(4);
  bf16x8 pt[4];
#pragma unroll
  for (int t = 0; t < 4; ++t)
    pt[t] = asbf((u32x4){pe[t][0] | (pe[t][1] << 16), pe[t][2] | (pe[t][3] << 16), pe[t][4] | (pe[t][5] << 16), pe[t][6] | (pe[t][7] << 16)});
  if (b < 56) {
    f32x16 cc = {};
#pragma unroll
    for (int t = 0; t < 4; ++t) cc = mfma32(pt[t], one, cc);
    asm volatile("s_nop 7\n\ts_nop 3" : "+v"(cc));
    if (r32 == 0) {
#pragma unroll
      for (int a = 0; a < 4; ++a) ldsw<f32x4>(S_CSP + (w * 32 + 8 * a + 4 * h2) * 4, (f32x4){cc[4 * a], cc[4 * a + 1], cc[4 * a + 2], cc[4 * a + 3]});
    }
  } else if (b < 72) {
    f32x4 c0 = {0, 0, 0, 0}, c1 = {0, 0, 0, 0};
    c0 = mfma16(pt[0], one, c0); c1 = mfma16(pt[2], one, c1); c0 = mfma16(pt[1], one, c0); c1 = mfma16(pt[3], one, c1);
    MFMA_GAP16(c0); MFMA_GAP16(c1);
    if (i16 == 0) { ldsw<f32x4>(S_CSP + (w * 32 + 4 * g4) * 4, c0); ldsw<f32x4>(S_CSP + (w * 32 + 16 + 4 * g4) * 4, c1); }
  }
  const int ct = w & 3, kh = w >> 2;
  const int dlt = (j >> 1) == 0 ? 1 : (j >> 1) == 1 ? -1 : (j >> 1) == 2 ? 2 : -2;
  auto blk_of = [&](int half) { return half * 32 + cl; };
  auto ox_off = [&]() { const int H = 4 * j + head; return WS_OX + blk_of(q_rt) * 32768 + ((r32 >> 4) * 16 + (H >> 1)) * 1024 + (2 * (H & 1) + h2) * 256 + i16 * 16; };
  auto bx_in = [&](int rt, int i) { return (u32)((rt * 16 + 2 * j + (ct >> 1)) * 1024 + (2 * (ct & 1) + (g4 >> 1)) * 256 + i * 16 + (g4 & 1) * 8); };
  auto hx_off = [&](int half) { return WS_HX + blk_of(half) * 32768 + bx_in(kh, i16); };
  auto ts_row = [&](int half) { return blk_of(half) * 32 + 16 * kh + i16 + dlt; };
  auto ts_off = [&](int half) { const int R = ts_row(half) & 2047; return WS_TS + (R >> 5) * 32768 + bx_in((R >> 4) & 1, R & 15); };
  auto x1_off = [&](int half) { return WS_X1 + cl * 65536 + (half * 32 + 4 * j + ct) * 1024 + (g4 >> 1) * 512 + (16 * kh + i16) * 16 + (g4 & 1) * 8; };
  auto ux_off = [&](int half, int s) { return w < 7 ? WS_UX + ((blk_of(half) * 8 + j) * 14 + 2 * w + s) * 1024 + lane * 16 : OOB; };
  {
    if (w == 1) {
      wait_flags(wsr, WS_FKV + (b & 7) * 2048, taglo, taghi, lane, t0);
      if (b == 0 && lane == 0) __hip_atomic_store((GAS u64*)(ws + WS_EPOCH), tag, __ATOMIC_RELAXED, __HIP_MEMORY_SCOPE_AGENT);
    }
    BAR();
    STAMP(5);
    const u32 vo = (u32)(w * 1024 + lane * 16), ld = lb + w * 1024;
#pragma unroll
    for (int i = 0; i < 3; ++i) dma1_sc1(ws + WS_K + i * 8192, vo, ld + L_K + i * 8192);
#pragma unroll
    for (int i = 0; i < 2; ++i) dma1_sc1(ws + WS_V + i * 8192, vo, ld + L_V + i * 8192);
#pragma unroll
    for (int i = 3; i < 8; ++i) dma1_sc1(ws + WS_K + i * 8192, vo, ld + L_K + i * 8192);
#pragma unroll
    for (int i = 2; i < 8; ++i) dma1_sc1(ws + WS_V + i * 8192, vo, ld + L_V + i * 8192);
    if (tid < 272) ldsw<u32x4>(L_ONES + tid * 16, (u32x4){0x3F803F80u, 0x3F803F80u, 0x3F803F80u, 0x3F803F80u});
    float cs = 0.f;
    if (b < 72 && tid < 32) {
#pragma unroll
      for (int ww = 0; ww < 8; ++ww) cs += ldsr<float>(S_CSP + (ww * 32 + tid) * 4);
    }
    asm volatile("s_waitcnt vmcnt(11)" ::: "memory");
    BAR();
    STAMP(6);
    {
      const u32 pb = b < 56 ? WS_W3 + (b * 32 + 4 * w) * 1024 : b < 72 ? WS_W1 + (2 * (b - 56) * 16 + 2 * w) * 1024
                   : b < 104 ? WS_WO + (b - 72) * 16384 + 2 * w * 1024 : b < 136 ? WS_W2 + (b - 104) * 16384 + 2 * w * 1024 : WS_W4 + (b - 136) * 16384 + 2 * w * 1024;
      const u32 p23 = b < 56 ? 2048u : 16384u;
      const u32 v01 = b < 248 ? pb + lane * 16 : OOB, v23 = b < 72 ? pb + p23 + lane * 16 : OOB;
      st16(wsr, v01, 0, asu(pt[0])); st16(wsr, v01, 1024, asu(pt[1])); st16(wsr, v23, 0, asu(pt[2])); st16(wsr, v23, 1024, asu(pt[3]));
      st4(wsr, (b < 72 && tid < 32) ? (b < 56 ? WS_CS3 + b * 128 : WS_CS1 + (b - 56) * 128) + tid * 4 : OOB, 0, __float_as_uint(cs));
      const u32x4 s4 = {SENT, SENT, SENT, SENT}; const u32x2 s2 = {SENT, SENT}, z2 = {0u, 0u};
      st16(wsr, ox_off(), 0, s4);
#pragma unroll
      for (int half = 0; half < 2; ++half) st8(wsr, ts_off(half), 0, (u32)ts_row(half) < 2048u ? s2 : z2);
    }
  }
  u32 opk[4];
  bf16x8 a_o[8], a_1[8], a_2[8], a3[32];
  f32x4 cs1; u32x4 cs3v; u32x2 xres[2];
  {
    u32 ka = L_K + lane * 16;
    const bool isv = r32 < 16;
    u32 va = isv ? L_V + h2 * 256 + r32 * 16 : L_ONES + ((r32 >= 20 && r32 < 28) ? 64 : 0);
    const u32 vinc = isv ? 4096u : 0u;
    f32x16 o = {};
    f32x16 negm;
    {
      const bf16x8 kf = ldsr<bf16x8>(ka);
      f32x16 z = {};
      const f32x16 s = mfma32(kf, qf, z);
      float m = fmaxf(fmaxf(s[0], s[1]), fmaxf(s[2], s[3]));
#pragma unroll
      for (int i = 4; i < 16; i += 2) m = fmaxf(m, fmaxf(s[i], s[i + 1]));
      const auto rr = __builtin_amdgcn_permlane32_swap(__float_as_uint(m), __float_as_uint(m), false, false);
      m = fmaxf(__uint_as_float(rr[0]), __uint_as_float(rr[1]));
#pragma unroll
      for (int i = 0; i < 16; ++i) negm[i] = -m;
    }
    f32x16 s = mfma32(ldsr<bf16x8>(ka), qf, negm);
    auto seg = [&]() {
      for (int it = 0; it < 4; ++it) {
#pragma unroll
        for (int u = 0; u < 4; ++u) {
          const bf16x8 v0 = ldsr<bf16x8>(va + u * 1024), v1 = ldsr<bf16x8>(va + u * 1024 + 512);
          const bf16x8 kn = ldsr<bf16x8>(ka + (u + 1) * 1024);
          const f32x16 sn = mfma32(kn, qf, negm);
          u32 pk[8];
#pragma unroll
          for (int i = 0; i < 8; ++i) pk[i] = cvtpk(__builtin_amdgcn_exp2f(s[2 * i]), __builtin_amdgcn_exp2f(s[2 * i + 1]));
          o = mfma32(v0, asbf((u32x4){pk[0], pk[1], pk[2], pk[3]}), o);
          o = mfma32(v1, asbf((u32x4){pk[4], pk[5], pk[6], pk[7]}), o);
          s = sn;
        }
        va += vinc; ka += 4096;
      }
    };
    seg();
    STAMP(7);
    WAITV0();
    BAR();
    if (w == 0 && lane < 8) __hip_atomic_store((GAS u64*)(ws + WS_FW + lane * 2048 + b * 8), tag, __ATOMIC_RELAXED, __HIP_MEMORY_SCOPE_AGENT);
    {
      const u32x4 s4 = {SENT, SENT, SENT, SENT}; const u32x2 s2 = {SENT, SENT};
#pragma unroll
      for (int half = 0; half < 2; ++half) { st8(wsr, hx_off(half), 0, s2); st8(wsr, x1_off(half), 0, s2); st16(wsr, ux_off(half, 0), 0, s4); st16(wsr, ux_off(half, 1), 0, s4); }
    }
    seg();
    STAMP(8);
    if (w == 1) wait_flags(wsr, WS_FW + (b & 7) * 2048, taglo, taghi, lane, t0);
    BAR();
    STAMP(9);
#pragma unroll
    for (int t = 0; t < 8; ++t) {
      const u32 fo = (((4 * j + ct) * 16) + 8 * kh + t) * 1024 + lane * 16;
      a_o[t] = asbf(ld16(wsr, fo, WS_WO)); a_1[t] = asbf(ld16(wsr, fo, WS_W1)); a_2[t] = asbf(ld16(wsr, fo, WS_W2));
    }
    cs1 = __builtin_bit_cast(f32x4, ld16(wsr, (64 * j + 16 * ct + 4 * g4) * 4, WS_CS1));
    cs3v = ld16(wsr, tid < 56 ? (224 * j) * 4 + tid * 16 : OOB, WS_CS3);
#pragma unroll
    for (int half = 0; half < 2; ++half) xres[half] = *(const GAS u32x2*)(x + (size_t)(blk_of(half) * 32 + 16 * kh + i16) * 512 + 64 * j + 16 * ct + 4 * g4);
    FENCE();
    seg();
    {
      const u32 qd = (u32)(b >> 6) & 3u;
#pragma unroll
      for (int i = 0; i < 7; ++i) dma1(ws + WS_W3, (u32)(j * 229376 + (qd * 56 + w * 7 + i) * 1024 + lane * 16), lb + L_DUMMY + w * 1024);
#pragma unroll
      for (int i = 0; i < 7; ++i) {
        const u32 f = qd * 56 + w * 7 + i;
        dma1(ws + WS_W4, (u32)((((f / 28) * 8 + j) * 28 + f % 28) * 1024 + lane * 16), lb + L_DUMMY + w * 1024);
      }
    }
    seg();
    STAMP(10);
    ka = L_K + lane * 16;
    float l = o[8];
    if (!__all(l <= 1.8446744e19f)) {
      float m = -INFINITY;
      o = (f32x16){};
      u32 vb = isv ? L_V + h2 * 256 + r32 * 16 : L_ONES;
      for (int t = 0; t < 64; ++t) {
        f32x16 z = {};
        const f32x16 sx = mfma32(ldsr<bf16x8>(ka + t * 1024), qf, z);
        float mt = sx[0];
#pragma unroll
        for (int i = 1; i < 16; ++i) mt = fmaxf(mt, sx[i]);
        const auto rr = __builtin_amdgcn_permlane32_swap(__float_as_uint(mt), __float_as_uint(mt), false, false);
        mt = fmaxf(fmaxf(__uint_as_float(rr[0]), __uint_as_float(rr[1])), m);
        const float f = __builtin_amdgcn_exp2f(m - mt);
        m = mt;
        u32 pk[8];
#pragma unroll
        for (int i = 0; i < 8; ++i) pk[i] = cvtpk(__builtin_amdgcn_exp2f(sx[2 * i] - m), __builtin_amdgcn_exp2f(sx[2 * i + 1] - m));
#pragma unroll
        for (int i = 0; i < 16; ++i) o[i] *= f;
        o = mfma32(ldsr<bf16x8>(vb), asbf((u32x4){pk[0], pk[1], pk[2], pk[3]}), o);
        o = mfma32(ldsr<bf16x8>(vb + 512), asbf((u32x4){pk[4], pk[5], pk[6], pk[7]}), o);
        vb += isv ? 1024u : 0u;
      }
      asm volatile("s_nop 7\n\ts_nop 3" : "+v"(o));
      l = o[8];
    }
    const float rl = __builtin_amdgcn_rcpf(l);
#pragma unroll
    for (int i = 0; i < 4; ++i) opk[i] = canon(cvtpk(o[2 * i] * rl, o[2 * i + 1] * rl));
  }
  WAITV0();
  st16(wsr, ox_off(), 0, (u32x4){opk[0], opk[1], opk[2], opk[3]});
  BAR();
  STAMP(11);
  if (tid < 56) ldsw<u32x4>(L_CS + tid * 16, cs3v);
  const u32 a3off = w < 7 ? ((7 * j + w) * 32) * 1024 + lane * 16 : OOB;
#pragma unroll
  for (int t = 0; t < A3_N0; ++t) a3[t] = asbf(ld16(wsr, a3off, WS_W3 + t * 1024));
  FENCE();

  auto g_issue = [&](u32 src, int half) { dma4_sc1(ws + src, (u32)(w * 4096 + lane * 16), lb + L_BIMG + half * 32768 + w * 4096); };
  auto gemm64 = [&](int half, const bf16x8 (&a)[8], const bf16x8 (&f)[4], bool stats) -> f32x4 {
    const u32 img = L_BIMG + half * 32768;
    if (stats) {
      f32x4 as = {0, 0, 0, 0}, aq = {0, 0, 0, 0};
#pragma unroll
      for (int t = 0; t < 4; ++t) { as = mfma16(one, f[t], as); aq = mfma16(f[t], f[t], aq); }
      MFMA_GAP16(as); MFMA_GAP16(aq);
      const u32 sb = L_STAT + half * 2048 + (16 * (w >> 2)) * 32 + (w & 3) * 4;
      if (lane < 16) ldsw<float>(sb + lane * 32, as[0]);
      if ((i16 >> 2) == g4) ldsw<float>(sb + 1024 + i16 * 32, diag_pick(aq, lane));
    }
    BAR();
    f32x4 acc0 = {0, 0, 0, 0}, acc1 = {0, 0, 0, 0};
#pragma unroll
    for (int t = 0; t < 8; ++t) {
      const bf16x8 b0 = ldsr<bf16x8>(img + (8 * kh + t) * 1024 + lane * 16), b1 = ldsr<bf16x8>(img + (16 + 8 * kh + t) * 1024 + lane * 16);
      acc0 = mfma16(a[t], b0, acc0); acc1 = mfma16(a[t], b1, acc1);
    }
    const f32x4 give = kh ? acc0 : acc1, keep = kh ? acc1 : acc0;
    ldsw<f32x4>(L_PART + (ct * 2 + kh) * 1024 + lane * 16, give);
    BAR();
    return keep + ldsr<f32x4>(L_PART + (ct * 2 + (kh ^ 1)) * 1024 + lane * 16);
  };
  auto src_of = [&](u32 base, int half) { return base + blk_of(half) * 32768; };

  f32x4 hres[2], xp[2];
  bf16x8 f[4];
  {
    SLEEP0();
    g_issue(src_of(WS_OX, 0), 0);
    gather_finish<1024, 2048, 3072, 0>(ws + src_of(WS_OX, 0), L_BIMG, w, lane, L_BIMG + w * 4096 + lane * 16, f, t0);
    STAMP(12);
    g_issue(src_of(WS_OX, 1), 1);
#pragma unroll
    for (int half = 0; half < 2; ++half) {
      if (half == 1) gather_finish<1024, 2048, 3072, 1>(ws + src_of(WS_OX, 1), L_BIMG + 32768, w, lane, L_BIMG + 32768 + w * 4096 + lane * 16, f, t0);
      const f32x4 acc = gemm64(half, a_o, f, false);
      hres[half] = acc + (f32x4){bflo(xres[half][0]), bfhi(xres[half][0]), bflo(xres[half][1]), bfhi(xres[half][1])};
      st8(wsr, hx_off(half), 0, (u32x2){canon(cvtpk(hres[half][0], hres[half][1])), canon(cvtpk(hres[half][2], hres[half][3]))});
    }
  }
  STAMP(13);
#pragma unroll
  for (int t = A3_N0; t < A3_N1; ++t) a3[t] = asbf(ld16(wsr, a3off, WS_W3 + t * 1024));
  FENCE();
  {
    SLEEP1();
    g_issue(src_of(WS_HX, 0), 0);
    gather_finish<1024, 2048, 3072, 0>(ws + src_of(WS_HX, 0), L_BIMG, w, lane, L_BIMG + w * 4096 + lane * 16, f, t0);
    STAMP(14);
    g_issue(src_of(WS_HX, 1), 1);
#pragma unroll
    for (int half = 0; half < 2; ++half) {
      if (half == 1) gather_finish<1024, 2048, 3072, 1>(ws + src_of(WS_HX, 1), L_BIMG + 32768, w, lane, L_BIMG + 32768 + w * 4096 + lane * 16, f, t0);
      const f32x4 acc = gemm64(half, a_1, f, true);
      const u32 sb = L_STAT + half * 2048 + (16 * kh + i16) * 32;
      const f32x4 s = ldsr<f32x4>(sb), q = ldsr<f32x4>(sb + 1024);
      const RowStat st = finish_stat((s[0] + s[1]) + (s[2] + s[3]), (q[0] + q[1]) + (q[2] + q[3]));
      const float r2 = __builtin_amdgcn_rsqf(st.var * st.rstd * st.rstd + LN_EPS), rr = st.rstd * r2;
      xp[half] = (hres[half] - st.mu) * st.rstd;
      const f32x4 pre = (acc - st.mu * cs1) * rr;
      const u32x2 tv = {canon(cvtpk(gelu(pre[0]), gelu(pre[1]))), canon(cvtpk(gelu(pre[2]), gelu(pre[3])))};
      st8(wsr, (u32)ts_row(half) < 2048u ? ts_off(half) : OOB, 0, tv);
    }
  }
  STAMP(15);
#pragma unroll
  for (int t = A3_N1; t < 32; ++t) a3[t] = asbf(ld16(wsr, a3off, WS_W3 + t * 1024));
  FENCE();
  {
    SLEEP1();
    g_issue(src_of(WS_TS, 0), 0);
    gather_finish<1024, 2048, 3072, 0>(ws + src_of(WS_TS, 0), L_BIMG, w, lane, L_BIMG + w * 4096 + lane * 16, f, t0);
    STAMP(16);
    g_issue(src_of(WS_TS, 1), 1);
#pragma unroll
    for (int half = 0; half < 2; ++half) {
      if (half == 1) gather_finish<1024, 2048, 3072, 1>(ws + src_of(WS_TS, 1), L_BIMG + 32768, w, lane, L_BIMG + 32768 + w * 4096 + lane * 16, f, t0);
      const f32x4 acc = gemm64(half, a_2, f, false);
      const f32x4 x1 = xp[half] + acc;
      st8(wsr, x1_off(half), 0, (u32x2){canon(cvtpk(x1[0], x1[1])), canon(cvtpk(x1[2], x1[3]))});
      ldsw<f32x4>(L_RES + ((32 * half + 16 * kh + i16) * 68 + 16 * ct + 4 * g4) * 4, hres[half] + x1);
    }
  }
  STAMP(17);
  bf16x8 a4[2][14];
  const u32 a4off = ((w * 8 + j) * 28) * 1024 + lane * 16;
#pragma unroll
  for (int t = 0; t < 14; ++t) a4[0][t] = asbf(ld16(wsr, a4off, WS_W4 + t * 1024));
  FENCE();
  {
    const u32 x1src = WS_X1 + cl * 65536;
    const u32 rb = w * 4096 + (g4 >> 1) * 1024 + (g4 & 1) * 512 + i16 * 16;
    auto x1_stats = [&](int half) {
      f32x4 as0 = {0, 0, 0, 0}, aq0 = {0, 0, 0, 0}, as1 = {0, 0, 0, 0}, aq1 = {0, 0, 0, 0};
      as0 = mfma16(one, f[0], as0); aq0 = mfma16(f[0], f[0], aq0); as1 = mfma16(one, f[2], as1); aq1 = mfma16(f[2], f[2], aq1);
      as0 = mfma16(one, f[1], as0); aq0 = mfma16(f[1], f[1], aq0); as1 = mfma16(one, f[3], as1); aq1 = mfma16(f[3], f[3], aq1);
      MFMA_GAP16(as0); MFMA_GAP16(aq0); MFMA_GAP16(as1); MFMA_GAP16(aq1);
      const u32 sb = L_STAT + half * 2048 + w * 4;
      if (lane < 16) { ldsw<float>(sb + lane * 32, as0[0]); ldsw<float>(sb + (16 + lane) * 32, as1[0]); }
      if ((i16 >> 2) == g4) { ldsw<float>(sb + 1024 + i16 * 32, diag_pick(aq0, lane)); ldsw<float>(sb + 1024 + (16 + i16) * 32, diag_pick(aq1, lane)); }
    };
    auto u_out = [&](int half, const f32x16& acc) {
      const u32 sr = L_STAT + half * 2048 + r32 * 32;
      const f32x4 s0 = ldsr<f32x4>(sr), s1 = ldsr<f32x4>(sr + 16), q0 = ldsr<f32x4>(sr + 1024), q1 = ldsr<f32x4>(sr + 1040);
      const f32x4 s = s0 + s1, q = q0 + q1;
      const RowStat st = finish_stat((s[0] + s[1]) + (s[2] + s[3]), (q[0] + q[1]) + (q[2] + q[3]));
      u32 up[8];
#pragma unroll
      for (int a = 0; a < 4; ++a) {
        const f32x4 cs = ldsr<f32x4>(L_CS + (32 * w + 8 * a + 4 * h2) * 4);
        float gv[4];
#pragma unroll
        for (int e = 0; e < 4; ++e) gv[e] = gelu((acc[4 * a + e] - st.mu * cs[e]) * st.rstd);
        up[2 * a] = canon(cvtpk(gv[0], gv[1])); up[2 * a + 1] = canon(cvtpk(gv[2], gv[3]));
      }
      st16(wsr, ux_off(half, 0), 0, (u32x4){up[0], up[1], up[2], up[3]});
      st16(wsr, ux_off(half, 1), 0, (u32x4){up[4], up[5], up[6], up[7]});
    };
    SLEEP1();
    g_issue(x1src, 0);
    gather_finish<2048, 256, 2304, 0>(ws + x1src, L_BIMG, w, lane, L_BIMG + rb, f, t0);
    STAMP(18);
    g_issue(x1src + 32768, 1);
    x1_stats(0);
    BAR();
    f32x16 accA = {}, accB = {};
#pragma unroll
    for (int t = 0; t < 32; ++t) accA = mfma32(a3[t], ldsr<bf16x8>(L_BIMG + t * 1024 + lane * 16), accA);
    STAMP(19);
    gather_finish<2048, 256, 2304, 0>(ws + x1src + 32768, L_BIMG + 32768, w, lane, L_BIMG + 32768 + rb, f, t0);
    x1_stats(1);
    BAR();
    STAMP(20);
#pragma unroll
    for (int t = 0; t < 32; ++t) {
      accB = mfma32(a3[t], ldsr<bf16x8>(L_BIMG + 32768 + t * 1024 + lane * 16), accB);
      if (t >= 4 && t < 32 && (t & 1) == 0) a4[1][(t - 4) >> 1] = asbf(ld16(wsr, a4off, WS_W4 + (14 + ((t - 4) >> 1)) * 1024));
    }
    u_out(0, accA);
    STAMP(21);
    u_out(1, accB);
    STAMP(22);
  }
#pragma unroll
  for (int half = 0; half < 2; ++half) {
    const u32 ub = WS_UX + ((blk_of(half) * 8 + w) * 14) * 1024;
    u32x4 uf[14];
    for (;;) {
#pragma unroll
      for (int t = 0; t < 14; ++t) uf[t] = ld16(wsr, lane * 16, ub + t * 1024);
      u32 m = 0;
#pragma unroll
      for (int t = 0; t < 14; ++t) m = umax(m, max4(uf[t]));
      if (!__any(m == SENT)) break;
      if (timed_out(t0)) break;
      __builtin_amdgcn_s_sleep(4);
      FENCE();
    }
    if (half == 0) STAMP(23); else STAMP(25);
    f32x16 acc0 = {}, acc1 = {};
#pragma unroll
    for (int t = 0; t < 14; ++t) { acc0 = mfma32(a4[0][t], asbf(uf[t]), acc0); acc1 = mfma32(a4[1][t], asbf(uf[t]), acc1); }
    if (half == 0) BAR();
#pragma unroll
    for (int s = 0; s < 2; ++s) {
      ldsw<u32x4>(L_RED + half * 32768 + (w * 4 + s) * 1024 + lane * 16, (u32x4){cvtpk(acc0[8 * s], acc0[8 * s + 1]), cvtpk(acc0[8 * s + 2], acc0[8 * s + 3]),
                                                                                 cvtpk(acc0[8 * s + 4], acc0[8 * s + 5]), cvtpk(acc0[8 * s + 6], acc0[8 * s + 7])});
      ldsw<u32x4>(L_RED + half * 32768 + (w * 4 + 2 + s) * 1024 + lane * 16, (u32x4){cvtpk(acc1[8 * s], acc1[8 * s + 1]), cvtpk(acc1[8 * s + 2], acc1[8 * s + 3]),
                                                                                     cvtpk(acc1[8 * s + 4], acc1[8 * s + 5]), cvtpk(acc1[8 * s + 6], acc1[8 * s + 7])});
    }
    if (half == 0) STAMP(24); else STAMP(26);
  }
  BAR();
  {
    const int half = w >> 2, v = w & 3;
    const int col = 32 * (v >> 1) + 16 * (v & 1) + 8 * h2;
    f32x4 r0 = ldsr<f32x4>(L_RES + ((32 * half + r32) * 68 + col) * 4), r1 = ldsr<f32x4>(L_RES + ((32 * half + r32) * 68 + col + 4) * 4);
#pragma unroll
    for (int s = 0; s < 8; ++s) {
      const u32x4 p = ldsr<u32x4>(L_RED + half * 32768 + (s * 4 + v) * 1024 + lane * 16);
      r0 += (f32x4){bflo(p[0]), bfhi(p[0]), bflo(p[1]), bfhi(p[1])};
      r1 += (f32x4){bflo(p[2]), bfhi(p[2]), bflo(p[3]), bfhi(p[3])};
    }
    *(GAS u32x4*)(out + (size_t)(blk_of(half) * 32 + r32) * 512 + 64 * j + col) = (u32x4){cvtpk(r0[0], r0[1]), cvtpk(r0[2], r0[3]), cvtpk(r1[0], r1[1]), cvtpk(r1[2], r1[3])};
  }
  STAMP(27);
}

extern "C" void kernel_launch(void* const* d_in, const int* in_sizes, int n_in, void* d_out, int out_size, void* d_ws, size_t ws_size, hipStream_t stream) {
  (void)in_sizes; (void)n_in; (void)out_size; (void)ws_size;
  fused_block_kernel<<<256, 512, 0, stream>>>((const u16*)d_in[0], (const u16*)d_in[1], (const u16*)d_in[2], (const u16*)d_in[3], (const u16*)d_in[4],
                                              (const u16*)d_in[6], (const u16*)d_in[7], (const u16*)d_in[8], (const u16*)d_in[9], (u16*)d_out, (char*)d_ws);
}
```

```cpp
#include <hip/hip_runtime.h>
#include <stdint.h>
#include <type_traits>

typedef unsigned short u16;
typedef unsigned int u32;
typedef unsigned long long u64;
typedef short bf16x8 __attribute__((ext_vector_type(8)));
typedef short s16x4 __attribute__((ext_vector_type(4)));
typedef float f32x2 __attribute__((ext_vector_type(2)));
typedef float f32x4 __attribute__((ext_vector_type(4)));
typedef float f32x16 __attribute__((ext_vector_type(16)));
typedef u32 u32x2 __attribute__((ext_vector_type(2)));
typedef u32 u32x4 __attribute__((ext_vector_type(4)));
typedef __bf16 bf16x2_t __attribute__((ext_vector_type(2)));
#define LAS __attribute__((address_space(3)))
#define GAS __attribute__((address_space(1)))
#define DI __device__ __forceinline__

constexpr u32 WS_EPOCH = 0x0;
constexpr u32 WS_FKV = 0x1000;
constexpr u32 WS_FW = 0x5000;
constexpr u32 WS_K = 0x10000;
constexpr u32 WS_V = 0x20000;
constexpr u32 WS_DBG = 0x1E00000;
constexpr u32 WS_WO = 0x100000, WS_W1 = 0x180000, WS_W2 = 0x200000, WS_W3 = 0x280000, WS_W4 = 0x440000;
constexpr u32 WS_OX = 0x600000, WS_HX = 0x800000, WS_TS = 0xA00000, WS_X1 = 0xC00000, WS_UX = 0xE00000;
constexpr u32 WS_STAMP = 0x2000000;
constexpr u32 WS_END = 0x2100000;
constexpr u32 OOB = 0x80000000u;

constexpr u32 XP = 1040;
constexpr u32 L_XIMG = 0;
constexpr u32 L_WQ = 66560;
constexpr u32 L_WKV = 132096;
constexpr u32 S_PM = 148480, S_PC = 156672, S_PS = 157184, S_PQ = 157696, S_FIN = 158208, S_QS = 158336, S_QC = 159360;
constexpr u32 L_DUM1 = 160384;
constexpr u32 L_CNT = 161408;
constexpr u32 L_K = 0, L_V = 65536, L_ONES = 131072;
constexpr u32 L_STG = 135424;
constexpr u32 L_CS1P = 155904;
constexpr u32 L_P = 0;
constexpr u32 L_STAT = 65536;
constexpr u32 STP = 80, STH = 2560;
constexpr u32 L_CS = 71680;
constexpr u32 L_RES = 73728;
constexpr u32 L_A4 = 90112;
constexpr u32 L_STAMP = 161792;
constexpr u32 LDS_BYTES = 163840;

__shared__ __attribute__((aligned(16))) char smem[LDS_BYTES];

constexpr u32 SENT = 0xFFFFFFFFu;
constexpr float LN_EPS = 1e-5f;
constexpr float QSCALE = 0.25f * 1.4426950408889634f;
#define CODE_KB 43

DI f32x16 mfma32(bf16x8 a, bf16x8 b, f32x16 c) { return __builtin_amdgcn_mfma_f32_32x32x16_bf16(a, b, c, 0, 0, 0); }
DI f32x4 mfma16(bf16x8 a, bf16x8 b, f32x4 c) { return __builtin_amdgcn_mfma_f32_16x16x32_bf16(a, b, c, 0, 0, 0); }
DI u32 cvtpk(float lo, float hi) { f32x2 v = {lo, hi}; bf16x2_t b = __builtin_convertvector(v, bf16x2_t); return __builtin_bit_cast(u32, b); }
DI u32 canon(u32 d) { return d < 0xFFFFFFFEu ? d : 0xFFFFFFFEu; }
DI float bflo(u32 d) { return __uint_as_float(d << 16); }
DI float bfhi(u32 d) { return __uint_as_float(d & 0xFFFF0000u); }
DI float dot2(u32 a, u32 b, float c) { return __builtin_amdgcn_fdot2_f32_bf16(__builtin_bit_cast(bf16x2_t, a), __builtin_bit_cast(bf16x2_t, b), c, false); }
DI float gelu(float x) {
  const float c1 = -2.0f * 0.7978845608028654f * 1.4426950408889634f, c2 = c1 * 0.044715f;
  const float e = __builtin_amdgcn_exp2f(x * (c1 + c2 * x * x));
  return x * __builtin_amdgcn_rcpf(1.0f + e);
}
DI float swap32_add(float v) {
  const auto r = __builtin_amdgcn_permlane32_swap(__float_as_uint(v), __float_as_uint(v), false, false);
  return __uint_as_float(r[0]) + __uint_as_float(r[1]);
}
DI float xor1_add(float v) { return v + __int_as_float(__builtin_amdgcn_mov_dpp(__float_as_int(v), 0xB1, 0xF, 0xF, true)); }

#define BAR() asm volatile("s_waitcnt lgkmcnt(0)\n\ts_barrier" ::: "memory")
#define WAITV(n) asm volatile("s_waitcnt vmcnt(" #n ")" ::: "memory")
#define FENCE() asm volatile("" ::: "memory")
template <class T> DI T opaque(T v) { asm volatile("" : "+v"(v)); return v; }

template <class T> DI T ldsr(u32 off) { return *(const LAS T*)((LAS char*)smem + off); }
template <class T> DI void ldsw(u32 off, T v) { *(LAS T*)((LAS char*)smem + off) = v; }
DI s16x4 ldstr(u32 off) {
  typedef short v4i16_t __attribute__((ext_vector_type(4)));
  return __builtin_bit_cast(s16x4, __builtin_amdgcn_ds_read_tr16_b64_v4i16((LAS v4i16_t*)((LAS char*)smem + off)));
}
DI bf16x8 cat(s16x4 a, s16x4 b) { return __builtin_shufflevector(a, b, 0, 1, 2, 3, 4, 5, 6, 7); }
DI u32 lds_base() { return (u32)(uintptr_t)(LAS char*)smem; }

#define DMA_HEAD "s_mov_b32 %[k], m0\n\ts_mov_b32 m0, %[lds]\n\ts_nop 0\n\t"
#define DMA_TAIL "s_mov_b32 m0, %[k]"
DI void dma1(const void* sbase, u32 voff, u32 ldsdst) {
  u32 keep;
  asm volatile(DMA_HEAD "global_load_lds_dwordx4 %[v], %[s]\n\t" DMA_TAIL : [k] "=&s"(keep) : [v] "v"(voff), [s] "s"(sbase), [lds] "s"(ldsdst) : "memory");
}
DI void dma2(const void* sbase, u32 voff, u32 ldsdst) {
  u32 keep;
  asm volatile(DMA_HEAD "global_load_lds_dwordx4 %[v], %[s]\n\tglobal_load_lds_dwordx4 %[v], %[s] offset:1024\n\t" DMA_TAIL
               : [k] "=&s"(keep) : [v] "v"(voff), [s] "s"(sbase), [lds] "s"(ldsdst) : "memory");
}
DI void dma4_rows(const void* sbase, u32 v0, u32 v1, u32 v2, u32 v3, u32 ldsdst) {
  u32 keep;
  asm volatile(DMA_HEAD "global_load_lds_dwordx4 %[a], %[s]\n\tglobal_load_lds_dwordx4 %[b], %[s] offset:1040\n\t"
               "global_load_lds_dwordx4 %[c], %[s] offset:2080\n\tglobal_load_lds_dwordx4 %[d], %[s] offset:3120\n\t" DMA_TAIL
               : [k] "=&s"(keep) : [a] "v"(v0), [b] "v"(v1), [c] "v"(v2), [d] "v"(v3), [s] "s"(sbase), [lds] "s"(ldsdst) : "memory");
}
DI void dma4_sc1(const void* sbase, u32 voff, u32 ldsdst) {
  u32 keep;
  asm volatile(DMA_HEAD "global_load_lds_dwordx4 %[v], %[s] sc1\n\tglobal_load_lds_dwordx4 %[v], %[s] offset:1024 sc1\n\t"
               "global_load_lds_dwordx4 %[v], %[s] offset:2048 sc1\n\tglobal_load_lds_dwordx4 %[v], %[s] offset:3072 sc1\n\t" DMA_TAIL
               : [k] "=&s"(keep) : [v] "v"(voff), [s] "s"(sbase), [lds] "s"(ldsdst) : "memory");
}
#define DMA_STEP(SC) "global_load_lds_dwordx4 %[v], %[s]" SC "\n\ts_add_u32 m0, m0, %[ls]\n\tv_add_u32 %[v], %[gs], %[v]\n\t"
template <int N, bool SC1, u32 GS>
DI void dma_strided(const void* sbase, u32 voff, u32 ldsdst, u32 lstep) {
  u32 keep;
  static_assert(N == 2 || N == 3 || N == 4 || N == 8, "");
#define DMA_RUN(BODY) asm volatile(DMA_HEAD BODY DMA_TAIL : [k] "=&s"(keep), [v] "+v"(voff) : [s] "s"(sbase), [lds] "s"(ldsdst), [ls] "s"(lstep), [gs] "n"(GS) : "memory", "scc")
  if constexpr (SC1) {
    if constexpr (N == 2) DMA_RUN(DMA_STEP(" sc1") DMA_STEP(" sc1"));
    if constexpr (N == 3) DMA_RUN(DMA_STEP(" sc1") DMA_STEP(" sc1") DMA_STEP(" sc1"));
    if constexpr (N == 4) DMA_RUN(DMA_STEP(" sc1") DMA_STEP(" sc1") DMA_STEP(" sc1") DMA_STEP(" sc1"));
  } else {
    if constexpr (N == 8) DMA_RUN(DMA_STEP("") DMA_STEP("") DMA_STEP("") DMA_STEP("") DMA_STEP("") DMA_STEP("") DMA_STEP("") DMA_STEP(""));
  }
#undef DMA_RUN
}

typedef __amdgpu_buffer_rsrc_t rsrc_t;
DI rsrc_t mkrsrc(const void* p, u32 bytes) { return __builtin_amdgcn_make_buffer_rsrc((void*)p, 0, bytes, 0x00020000); }
DI u32x4 ld16(rsrc_t r, u32 voff, u32 soff) { return __builtin_amdgcn_raw_buffer_load_b128(r, voff, soff, 16); }
DI void st16(rsrc_t r, u32 voff, u32 soff, u32x4 v) { __builtin_amdgcn_raw_buffer_store_b128(v, r, voff, soff, 16); }
DI void st8(rsrc_t r, u32 voff, u32 soff, u32x2 v) { __builtin_amdgcn_raw_buffer_store_b64(v, r, voff, soff, 16); }
DI void st4(rsrc_t r, u32 voff, u32 soff, u32 v) { __builtin_amdgcn_raw_buffer_store_b32(v, r, voff, soff, 16); }
DI bf16x8 asbf(u32x4 v) { return __builtin_bit_cast(bf16x8, v); }
DI u32x4 asu(bf16x8 v) { return __builtin_bit_cast(u32x4, v); }
DI u32 umax(u32 a, u32 b) { return a > b ? a : b; }
DI u32 max4(u32x4 v) { return umax(umax(v[0], v[1]), umax(v[2], v[3])); }

constexpr u64 SPIN_TICKS = 400000ull;
DI bool timed_out(u64 t0) { return __builtin_amdgcn_s_memrealtime() - t0 > SPIN_TICKS; }

#define STAMP(i) do { } while (0)
#define ASTAMP(it, pos) do { } while (0)
#define PSTAMP(sb, i) do { } while (0)
#define MFMA_GAP16(v) asm volatile("s_nop 7" : "+v"(v))
#define MFMA_GAP32(v) asm volatile("s_nop 7\n\ts_nop 3" : "+v"(v))

DI bool flags_ok(u32x4 a, u32x4 b, u32 lo, u32 hi) {
  return __all(a[0] == lo && a[1] == hi && a[2] == lo && a[3] == hi && b[0] == lo && b[1] == hi && b[2] == lo && b[3] == hi);
}
DI void wait_flags(rsrc_t wsr, u32 base, u32 taglo, u32 taghi, int lane, u64 t0) {
  for (;;) {
    const u32x4 a = ld16(wsr, lane * 32, base), b = ld16(wsr, lane * 32 + 16, base);
    if (flags_ok(a, b, taglo, taghi)) break;
    if (timed_out(t0)) break;
    __builtin_amdgcn_s_sleep(1);
    FENCE();
  }
}

DI bf16x8 ones_frag() { return asbf((u32x4){0x3F803F80u, 0x3F803F80u, 0x3F803F80u, 0x3F803F80u}); }
DI float diag_pick(f32x4 g, int lane) { const int r = lane & 3; float a = (r & 1) ? g[1] : g[0], b = (r & 1) ? g[3] : g[2]; return (r & 2) ? b : a; }

struct RowStat { float mu, rstd, var; };
DI RowStat finish_stat(float s, float q) {
  RowStat r; r.mu = s * (1.0f / 512.0f);
  r.var = fmaxf(q * (1.0f / 512.0f) - r.mu * r.mu, 0.0f);
  r.rstd = __builtin_amdgcn_rsqf(r.var + LN_EPS);
  return r;
}
DI bool unwritten(const bf16x8 (&f)[4]) {
  return __any(umax(umax(max4(asu(f[0])), max4(asu(f[1]))), umax(max4(asu(f[2])), max4(asu(f[3])))) == SENT);
}

template <int I, int N, class F> DI void static_for(F&& f) {
  if constexpr (I < N) { f(std::integral_constant<int, I>{}); static_for<I + 1, N>(f); }
}
struct Job { const char* src; u32 prow; bool perm; };
DI Job retile_job(u32 f, u64 wo, u64 w1, u64 w2, u64 w3, u64 w4) {
  const bool big = f >= 1536, last = f >= 3328;
  const u32 g = f - 3328, T = last ? g / 112 : big ? (f - 1536) >> 5 : (f >> 5) & 15, ks = last ? g % 112 : f & 31;
  const u32 prow = big && !last ? 3584 : 1024;
  u64 m = f < 1024 ? w1 : w2;
  m = f < 512 ? wo : m; m = big ? w3 : m; m = last ? w4 : m;
  return Job{(const char*)(m + (u64)(16 * ks * prow + T * 64)), prow, last || f < 512};
}

#define A3_N0 6
#define A4_N0 6

__global__ __launch_bounds__(512, 2) void fused_block_kernel(const u16* __restrict__ x, const u16* __restrict__ wq, const u16* __restrict__ wk,
                                                             const u16* __restrict__ wv, const u16* __restrict__ wo, const u16* __restrict__ w1,
                                                             const u16* __restrict__ w2, const u16* __restrict__ w3, const u16* __restrict__ w4,
                                                             u16* __restrict__ out, char* ws) {
  const int tid = threadIdx.x, lane = tid & 63, w = __builtin_amdgcn_readfirstlane(tid >> 6);
  const int b = blockIdx.x;
  const int cl = (b & 7) * 4 + (b >> 6), j = (b >> 3) & 7;
  const u32 lb = lds_base();
  const u32 l16 = lane * 16;
  {
    u64 pc;
    asm volatile("s_getpc_b64 %0" : "=s"(pc));
    dma1((const void*)(pc & ~63ull), (u32)((((b >> 3) * 8 + w) % CODE_KB) * 1024) + l16, lb + L_DUM1);
  }
  asm volatile("" :: "s"(x), "s"(wq), "s"(wk), "s"(wv), "s"(wo), "s"(w1), "s"(w2), "s"(w3), "s"(w4), "s"(out), "s"(ws));
  const u64 t0 = __builtin_amdgcn_s_memrealtime();
  const int i16 = lane & 15, g4 = lane >> 4, r32 = lane & 31, h2 = lane >> 5;
  STAMP(0);

  const int kvrt = j >> 1, kvmat = j & 1;
  auto grow = [&](int rho) { return (rho < 32 ? 0 : 1024 - 32) + 32 * cl + rho; };
  const int R0 = grow(16 * kvrt);
  u32x4 xk0, xk1;
  u64 tag;
  {
    const u32 xo = (u32)(i16 * 1024 + g4 * 32);
    asm volatile("global_load_dwordx4 %0, %2, %3\n\tglobal_load_dwordx4 %1, %2, %3 offset:16"
                 : "=&v"(xk0), "=&v"(xk1) : "v"(xo), "s"((const char*)x + R0 * 1024 + w * 128) : "memory");
    dma2((const char*)(kvmat ? wv : wk) + w * 2048, l16, lb + L_WKV + w * 2048);
    asm volatile("s_load_dwordx2 %0, %1, 0x0" : "=s"(tag) : "s"(ws + WS_EPOCH) : "memory");
    const char* xr = (const char*)x + grow(8 * w) * 1024 - 48;
    dma4_rows(xr, l16 + 48, l16 + 32, l16 + 16, l16, lb + L_XIMG + 8 * w * XP);
    dma4_rows(xr + 4096, l16 + 48, l16 + 32, l16 + 16, l16, lb + L_XIMG + (8 * w + 4) * XP);
    dma_strided<8, false, 0x2000>((const char*)wq + w * 65536 + j * 128, (u32)((lane >> 3) * 1024 + (((lane & 7) ^ (4 * ((lane >> 4) & 1))) * 16)), lb + L_WQ + w * 8192, 1024);
    asm volatile("s_waitcnt vmcnt(16) lgkmcnt(0)" : "+v"(xk0), "+v"(xk1), "+s"(tag) :: "memory");
  }
  tag += 1ull;
  const u32 taglo = (u32)tag, taghi = (u32)(tag >> 32);
  const rsrc_t wsr = mkrsrc(ws, WS_END);
  const bf16x8 one = ones_frag();
  STAMP(1);

  {
    f32x4 am = {0, 0, 0, 0}, ac = {0, 0, 0, 0}, as = {0, 0, 0, 0}, aq = {0, 0, 0, 0};
    const u32 wa = L_WKV + w * 2048 + (16 * g4 + (i16 >> 2)) * 32 + (i16 & 3) * 8;
    const bf16x8 wf0 = cat(ldstr(wa), ldstr(wa + 128)), wf1 = cat(ldstr(wa + 256), ldstr(wa + 384));
    const bf16x8 xf0 = asbf(xk0), xf1 = asbf(xk1);
    as = mfma16(one, xf0, as); aq = mfma16(xf0, xf0, aq); as = mfma16(one, xf1, as); aq = mfma16(xf1, xf1, aq);
    if (kvmat) {
      am = mfma16(xf0, wf0, am); ac = mfma16(one, wf0, ac); am = mfma16(xf1, wf1, am); ac = mfma16(one, wf1, ac);
      MFMA_GAP16(am); MFMA_GAP16(ac);
      if (lane < 16) ldsw<float>(S_PC + (w * 16 + lane) * 4, ac[0]);
    } else {
      am = mfma16(wf0, xf0, am); ac = mfma16(wf0, one, ac); am = mfma16(wf1, xf1, am); ac = mfma16(wf1, one, ac);
      MFMA_GAP16(am); MFMA_GAP16(ac);
      if (i16 == 0) ldsw<f32x4>(S_PC + (w * 16 + 4 * g4) * 4, ac);
    }
    ldsw<f32x4>(S_PM + w * 1024 + l16, am);
    if (lane < 16) ldsw<float>(S_PS + (lane * 8 + w) * 4, as[0]);
    if ((i16 >> 2) == g4) ldsw<float>(S_PQ + (i16 * 8 + w) * 4, diag_pick(aq, lane));
    BAR();
  }
  if (w == 0) {
    {
      const f32x4 s0 = ldsr<f32x4>(S_PS + i16 * 32), s1 = ldsr<f32x4>(S_PS + i16 * 32 + 16);
      const f32x4 q0 = ldsr<f32x4>(S_PQ + i16 * 32), q1 = ldsr<f32x4>(S_PQ + i16 * 32 + 16);
      const f32x4 s = s0 + s1, q = q0 + q1;
      const RowStat st = finish_stat((s[0] + s[1]) + (s[2] + s[3]), (q[0] + q[1]) + (q[2] + q[3]));
      if (lane < 16) { ldsw<float>(S_FIN + lane * 4, st.mu); ldsw<float>(S_FIN + 64 + lane * 4, st.rstd); }
    }
    f32x4 m = {0, 0, 0, 0};
#pragma unroll
    for (int ww = 0; ww < 8; ++ww) m += ldsr<f32x4>(S_PM + ww * 1024 + l16);
    f32x4 v;
    u32 off;
    if (kvmat) {
      float c1 = 0.f;
#pragma unroll
      for (int ww = 0; ww < 8; ++ww) c1 += ldsr<float>(S_PC + (ww * 16 + i16) * 4);
      asm volatile("s_waitcnt lgkmcnt(0)" ::: "memory");
      const f32x4 mu = ldsr<f32x4>(S_FIN + g4 * 16), rs = ldsr<f32x4>(S_FIN + 64 + g4 * 16);
      v = rs * (m - mu * c1);
      off = WS_V + (R0 >> 5) * 1024 + ((R0 >> 4) & 1) * 512 + (g4 & 1) * 256 + i16 * 16 + (g4 >> 1) * 8;
    } else {
      f32x4 c4 = {0, 0, 0, 0};
#pragma unroll
      for (int ww = 0; ww < 8; ++ww) c4 += ldsr<f32x4>(S_PC + (ww * 16 + 4 * g4) * 4);
      asm volatile("s_waitcnt lgkmcnt(0)" ::: "memory");
      const float mu = ldsr<float>(S_FIN + i16 * 4), rs = ldsr<float>(S_FIN + 64 + i16 * 4);
      v = rs * (m - mu * c4);
      off = WS_K + (R0 >> 5) * 1024 + (g4 & 1) * 512 + ((R0 & 31) + i16) * 16 + (g4 >> 1) * 8;
    }
    st8(wsr, off, 0, (u32x2){cvtpk(v[0], v[1]), cvtpk(v[2], v[3])});
    WAITV(1);
  } else {
    WAITV(0);
  }
#define JOB(i) retile_job((u32)(b * 20 + w * 5 + (i)), (u64)wo, (u64)w1, (u64)w2, (u64)w3, (u64)w4)
  u32x4 rts[5];
  if (w < 4) {
#pragma unroll
    for (int i = 0; i < 5; ++i) {
      const Job jb = JOB(i);
      rts[i] = *(const GAS u32x4*)(jb.src + (u32)((lane >> 2) * jb.prow + (lane & 3) * 16));
    }
  }
  FENCE();
  STAMP(2);
  const int q_hp = w & 1, q_rt = (w >> 1) & 1, q_kh = w >> 2;
  const int head = 2 * q_hp + q_kh;
  bf16x8 qf;
  {
    BAR();
    STAMP(3);
    {
      f32x4 as = {0, 0, 0, 0}, aq = {0, 0, 0, 0};
      const int rt = w & 3, kh = w >> 2;
      const u32 sa = L_XIMG + (16 * rt + i16) * XP + kh * 512 + g4 * 16;
#pragma unroll
      for (int t = 0; t < 8; ++t) {
        const bf16x8 xf = ldsr<bf16x8>(sa + t * 64);
        as = mfma16(one, xf, as); aq = mfma16(xf, xf, aq);
      }
      MFMA_GAP16(as); MFMA_GAP16(aq);
      if (lane < 16) ldsw<float>(S_QS + ((16 * rt + lane) * 2 + kh) * 4, as[0]);
      if ((i16 >> 2) == g4) ldsw<float>(S_QS + 512 + ((16 * rt + i16) * 2 + kh) * 4, diag_pick(aq, lane));
    }
    f32x16 acc = {}, accc = {};
    const int q = i16 >> 2, p = i16 & 3, cb = g4 & 1;
    const u32 wa = L_WQ + (256 * q_kh + 8 * h2 + q) * 128 + (((4 * q_hp + 2 * cb + (p >> 1)) ^ (4 * ((q >> 1) & 1))) * 16) + (p & 1) * 8;
    const u32 xa = L_XIMG + (32 * q_rt + r32) * XP + q_kh * 512 + h2 * 16;
#pragma unroll
    for (int t = 0; t < 16; ++t) {
      const bf16x8 af = cat(ldstr(wa + t * 2048), ldstr(wa + t * 2048 + 512));
      acc = mfma32(af, ldsr<bf16x8>(xa + t * 32), acc);
    }
    const u32 wb = wa + q_rt * 16384;
#pragma unroll
    for (int t = 0; t < 8; ++t) accc = mfma32(cat(ldstr(wb + t * 2048), ldstr(wb + t * 2048 + 512)), one, accc);
    if (w == 0) {
      WAITV(5);
      if (lane < 8) __hip_atomic_store((GAS u64*)(ws + WS_FKV + lane * 2048 + b * 8), tag, __ATOMIC_RELAXED, __HIP_MEMORY_SCOPE_AGENT);
    }
    MFMA_GAP32(acc); MFMA_GAP32(accc);
    f32x4 k0, k1, s0, s1;
    if (q_kh) {
      k0 = (f32x4){acc[8], acc[9], acc[10], acc[11]}; k1 = (f32x4){acc[12], acc[13], acc[14], acc[15]};
      s0 = (f32x4){acc[0], acc[1], acc[2], acc[3]}; s1 = (f32x4){acc[4], acc[5], acc[6], acc[7]};
    } else {
      k0 = (f32x4){acc[0], acc[1], acc[2], acc[3]}; k1 = (f32x4){acc[4], acc[5], acc[6], acc[7]};
      s0 = (f32x4){acc[8], acc[9], acc[10], acc[11]}; s1 = (f32x4){acc[12], acc[13], acc[14], acc[15]};
    }
    ldsw<f32x4>(L_WKV + w * 2048 + l16, s0); ldsw<f32x4>(L_WKV + w * 2048 + 1024 + l16, s1);
    if (r32 == 0) {
#pragma unroll
      for (int a = 0; a < 4; ++a) ldsw<f32x4>(S_QC + (((q_hp * 2 + q_rt) * 2 + q_kh) * 32 + 8 * a + 4 * h2) * 4, (f32x4){accc[4 * a], accc[4 * a + 1], accc[4 * a + 2], accc[4 * a + 3]});
    }
    BAR();
    k0 += ldsr<f32x4>(L_WKV + (w ^ 4) * 2048 + l16); k1 += ldsr<f32x4>(L_WKV + (w ^ 4) * 2048 + 1024 + l16);
    f32x4 c0 = {0, 0, 0, 0}, c1 = {0, 0, 0, 0};
#pragma unroll
    for (int s = 0; s < 4; ++s) {
      c0 += ldsr<f32x4>(S_QC + ((q_hp * 4 + s) * 32 + 16 * q_kh + 4 * h2) * 4);
      c1 += ldsr<f32x4>(S_QC + ((q_hp * 4 + s) * 32 + 16 * q_kh + 8 + 4 * h2) * 4);
    }
    const f32x2 sp = ldsr<f32x2>(S_QS + (32 * q_rt + r32) * 8), qp = ldsr<f32x2>(S_QS + 512 + (32 * q_rt + r32) * 8);
    const RowStat st = finish_stat(sp[0] + sp[1], qp[0] + qp[1]);
    const float sc = st.rstd * QSCALE;
    k0 = (k0 - st.mu * c0) * sc; k1 = (k1 - st.mu * c1) * sc;
    qf = opaque(asbf((u32x4){cvtpk(k0[0], k0[1]), cvtpk(k0[2], k0[3]), cvtpk(k1[0], k1[1]), cvtpk(k1[2], k1[3])}));
  }
  STAMP(4);
  auto blk_of = [&](int half) { return half * 32 + cl; };
  const int dlt = (j >> 1) == 0 ? 1 : (j >> 1) == 1 ? -1 : (j >> 1) == 2 ? 2 : -2;
  const u32 pc_in = (u32)((4 * j + (w >> 1)) * 1024 + (w & 1) * 512);
  auto hx_off = [&](u32 base, int half) { return base + blk_of(half) * 32768 + pc_in + lane * 8; };
  auto ts_off = [&](int half) {
    const u32 l = opaque<u32>(lane);
    const u32 R = (u32)(blk_of(half) * 32 + (int)(l >> 1) + dlt);
    return R < 2048u ? WS_TS + (R >> 5) * 32768 + pc_in + (R & 31) * 16 + (l & 1) * 8 : OOB;
  };
  auto ox_off = [&]() { return WS_OX + blk_of(q_rt) * 32768 + (4 * j + head) * 1024 + l16; };
  {
    if (w == 1) {
      wait_flags(wsr, WS_FKV + (b & 7) * 2048, taglo, taghi, lane, t0);
      if (b == 0 && lane == 0) __hip_atomic_store((GAS u64*)(ws + WS_EPOCH), tag, __ATOMIC_RELAXED, __HIP_MEMORY_SCOPE_AGENT);
    }
    BAR();
    STAMP(5);
    dma_strided<3, true, 0x2000>(ws + WS_K, w * 1024 + l16, lb + L_K + w * 1024, 8192);
    dma_strided<2, true, 0x2000>(ws + WS_V, w * 1024 + l16, lb + L_V + w * 1024, 8192);
    if (tid < 272) ldsw<u32x4>(L_ONES + tid * 16, (u32x4){0x3F803F80u, 0x3F803F80u, 0x3F803F80u, 0x3F803F80u});
    if (tid == 511) ldsw<u32x2>(L_CNT, (u32x2){0u, 0u});
    WAITV(0);
    BAR();
    STAMP(6);
  }
  u32 opk[4];
  bf16x8 a_o[8], a_1[8], a_2[8], a3[32];
  u32x2 xres[2];
  {
    u32 ka = L_K + l16;
    const bool isv = r32 < 16;
    u32 va = isv ? L_V + h2 * 256 + r32 * 16 : L_ONES + ((r32 >= 20 && r32 < 28) ? 64 : 0);
    const u32 vinc = isv ? 4096u : 0u;
    f32x16 o = {};
    f32x16 negm;
    {
      const bf16x8 kf = ldsr<bf16x8>(ka);
      f32x16 z = {};
      const f32x16 s = mfma32(kf, qf, z);
      float m = fmaxf(fmaxf(s[0], s[1]), fmaxf(s[2], s[3]));
#pragma unroll
      for (int i = 4; i < 16; i += 2) m = fmaxf(m, fmaxf(s[i], s[i + 1]));
      const auto rr = __builtin_amdgcn_permlane32_swap(__float_as_uint(m), __float_as_uint(m), false, false);
      m = fmaxf(__uint_as_float(rr[0]), __uint_as_float(rr[1]));
#pragma unroll
      for (int i = 0; i < 16; ++i) negm[i] = -m;
    }
    f32x16 s = mfma32(ldsr<bf16x8>(ka), qf, negm);
    auto tiles4 = [&]() {
#pragma unroll
      for (int u = 0; u < 4; ++u) {
        const bf16x8 v0 = ldsr<bf16x8>(va + u * 1024), v1 = ldsr<bf16x8>(va + u * 1024 + 512);
        const bf16x8 kn = ldsr<bf16x8>(ka + (u + 1) * 1024);
        const f32x16 sn = mfma32(kn, qf, negm);
        u32 pk[8];
#pragma unroll
        for (int i = 0; i < 8; ++i) pk[i] = cvtpk(__builtin_amdgcn_exp2f(s[2 * i]), __builtin_amdgcn_exp2f(s[2 * i + 1]));
        o = mfma32(v0, asbf((u32x4){pk[0], pk[1], pk[2], pk[3]}), o);
        o = mfma32(v1, asbf((u32x4){pk[4], pk[5], pk[6], pk[7]}), o);
        s = sn;
      }
      va += vinc; ka += 4096;
    };
    if (w < 4) __builtin_amdgcn_s_setprio(2); else __builtin_amdgcn_s_setprio(1);
    auto kv_rest = [&](u32 img, u32 limg, int c0, auto n) {
      dma_strided<n.value, true, 4096>(ws + img + c0 * 8192, opaque<u32>((w & 3) * 1024 + l16), lb + limg + c0 * 8192 + (w & 3) * 1024, 4096);
    };
    using N2 = std::integral_constant<int, 2>; using N4 = std::integral_constant<int, 4>;
    auto lds_count = [&](u32 off) {
      u32 n;
      asm volatile("ds_add_rtn_u32 %0, %1, %2\n\ts_waitcnt lgkmcnt(0)" : "=v"(n) : "v"(lb + off), "v"(lane == 0 ? 1u : 0u) : "memory");
      return (u32)__builtin_amdgcn_readfirstlane(n);
    };
    const u32x4 s4 = {SENT, SENT, SENT, SENT};
    u32x4 fa, fb;
    auto fetch = [&](int p, u32 l) {
      const u32 fo = (2 * j * 32 + 4 * w) * 1024;
      if (p < 24) {
        const int t = p & 7;
        const bf16x8 v = asbf(ld16(wsr, l + (t & 3) * 1024, (p < 8 ? WS_WO : p < 16 ? WS_W1 : WS_W2) + fo + (t >> 2) * 32768));
        if (p < 8) a_o[t] = v; else if (p < 16) a_1[t] = v; else a_2[t] = v;
      } else if (p < 26) {
        xres[p - 24] = *(const GAS u32x2*)((const char*)x + (u32)((blk_of(p - 24) * 32 + (l >> 5)) * 1024 + (64 * j + 8 * w) * 2 + ((l >> 4) & 1) * 8));
      } else if (p < 26 + A3_N0) {
        a3[p - 26] = asbf(ld16(wsr, w < 7 ? ((7 * j + w) * 32) * 1024 + l : OOB, WS_W3 + (p - 26) * 1024));
      } else if (p < 33 + A3_N0) {
        dma1(ws + WS_W3 + (p - 26 - A3_N0) * 1024, (((u32)(b >> 6) & 3u) * 8 + w) * 7168 + j * 229376 + l, lb + L_DUM1);
      }
    };
    static_for<0, 16>([&](auto IT) {
      constexpr int it = IT.value;
      ASTAMP(it, 0);
      if (w < 4) {
        if (it == 0) {
          const u32 l = opaque<u32>(lane);
          const u32 sb = L_STG + w * 5120;
#pragma unroll
          for (int i = 0; i < 5; ++i) ldsw<u32x4>(sb + i * 1024 + l * 16, rts[i]);
          const u32 tb = sb + ((l & 15) >> 2) * 64 + ((l >> 4) & 1) * 32 + (l & 3) * 8;
#pragma unroll
          for (int i = 0; i < 5; ++i) {
            const bool perm = JOB(i).perm;
            const u32 t0a = tb + i * 1024 + (l >> 5) * (perm ? 256 : 512);
            st16(wsr, l * 16, WS_WO + (b * 20 + w * 5 + i) * 1024, asu(cat(ldstr(t0a), ldstr(t0a + (perm ? 512 : 256)))));
          }
        } else if (it == 1) {
          const u32 l = opaque<u32>(lane);
#pragma unroll
          for (int half = 0; half < 2; ++half) {
            const u32 po = blk_of(half) * 32768 + (4 * j + w) * 1024;
            st16(wsr, l * 16, WS_OX + po, s4); st16(wsr, l * 16, WS_HX + po, s4); st16(wsr, l * 16, WS_X1 + po, s4);
            const int R = blk_of(half) * 32 + (int)(l & 31) + dlt;
            const u32 mk = (u32)R < 2048u ? SENT : 0u;
            st16(wsr, ((R & 2047) >> 5) * 32768 + (l >> 5) * 512 + (R & 31) * 16, WS_TS + (4 * j + w) * 1024, (u32x4){mk, mk, mk, mk});
          }
        } else if (it == 2) {
          const u32 uo = WS_UX + (blk_of(w >> 1) * 112 + 14 * j + 7 * (w & 1)) * 1024;
          const u32 l = opaque<u32>(l16);
#pragma unroll
          for (int i = 0; i < 7; ++i) st16(wsr, l + (i & 3) * 1024, uo + (i >> 2) * 4096, s4);
        } else if (it == 5) {
          WAITV(0);
          if (lds_count(L_CNT + 4) == 3 && lane < 8) __hip_atomic_store((GAS u64*)(ws + WS_FW + lane * 2048 + b * 8), tag, __ATOMIC_RELAXED, __HIP_MEMORY_SCOPE_AGENT);
        }
      } else {
        if (it == 0) {
          kv_rest(WS_V, L_V, 2, N4{}); kv_rest(WS_K, L_K, 3, N4{}); kv_rest(WS_V, L_V, 4, N4{}); kv_rest(WS_K, L_K, 5, N4{});
          kv_rest(WS_V, L_V, 6, N4{}); kv_rest(WS_K, L_K, 7, N2{});
        } else if (it == 1) {
          WAITV(0);
          lds_count(L_CNT);
        }
      }
      if (it == 4) {
        while (__builtin_amdgcn_readfirstlane(*(volatile LAS u32*)((LAS char*)smem + L_CNT)) != 4) {
          if (timed_out(t0)) break;
          __builtin_amdgcn_s_sleep(1);
        }
        FENCE();
      }
      if (it == 7) {
        const u32 l = opaque<u32>(l16);
        fa = ld16(wsr, l * 2, WS_FW + (b & 7) * 2048); fb = ld16(wsr, l * 2 + 16, WS_FW + (b & 7) * 2048);
      }
      if (it == 8) {
        if (!flags_ok(fa, fb, taglo, taghi)) wait_flags(wsr, WS_FW + (b & 7) * 2048, taglo, taghi, lane, t0);
      }
      if (it >= 8) {
        const u32 l = opaque<u32>(l16);
#pragma unroll
        for (int p = 5 * (it - 8); p < 5 * (it - 7); ++p) fetch(p, l);
      }
      if (it == 12 && w < 4) __builtin_amdgcn_s_setprio(0);
      FENCE();
      ASTAMP(it, 1);
      tiles4();
      FENCE();
      if (it == 9) STAMP(7);
    });
    STAMP(8);
    __builtin_amdgcn_s_setprio(0);
    ka = L_K + l16;
    float l = o[8];
    if (!__all(l <= 1.8446744e19f)) {
      float m = -INFINITY;
      o = (f32x16){};
      u32 vb = isv ? L_V + h2 * 256 + r32 * 16 : L_ONES;
      for (int t = 0; t < 64; ++t) {
        f32x16 z = {};
        const f32x16 sx = mfma32(ldsr<bf16x8>(ka + t * 1024), qf, z);
        float mt = sx[0];
#pragma unroll
        for (int i = 1; i < 16; ++i) mt = fmaxf(mt, sx[i]);
        const auto rr = __builtin_amdgcn_permlane32_swap(__float_as_uint(mt), __float_as_uint(mt), false, false);
        mt = fmaxf(fmaxf(__uint_as_float(rr[0]), __uint_as_float(rr[1])), m);
        const float f = __builtin_amdgcn_exp2f(m - mt);
        m = mt;
        u32 pk[8];
#pragma unroll
        for (int i = 0; i < 8; ++i) pk[i] = cvtpk(__builtin_amdgcn_exp2f(sx[2 * i] - m), __builtin_amdgcn_exp2f(sx[2 * i + 1] - m));
#pragma unroll
        for (int i = 0; i < 16; ++i) o[i] *= f;
        o = mfma32(ldsr<bf16x8>(vb), asbf((u32x4){pk[0], pk[1], pk[2], pk[3]}), o);
        o = mfma32(ldsr<bf16x8>(vb + 512), asbf((u32x4){pk[4], pk[5], pk[6], pk[7]}), o);
        vb += isv ? 1024u : 0u;
      }
      MFMA_GAP32(o);
      l = o[8];
    }
    const float rl = __builtin_amdgcn_rcpf(l);
#pragma unroll
    for (int i = 0; i < 4; ++i) opk[i] = canon(cvtpk(o[2 * i] * rl, o[2 * i + 1] * rl));
  }
  st16(wsr, ox_off(), 0, (u32x4){opk[0], opk[1], opk[2], opk[3]});
  STAMP(9);
  bf16x8 sel[4];
  auto make_sel = [&]() {
    const u32 l = opaque<u32>(lane), e = (l & 1) ? 0x3F800000u : 0x00003F80u;
#pragma unroll
    for (int q = 0; q < 4; ++q) {
      const bool on = ((l >> 2) & 3) == q;
      const u32 d0 = (on && !(l & 2)) ? e : 0u, d1 = (on && (l & 2)) ? e : 0u;
      sel[q] = asbf((u32x4){d0, d1, d0, d1});
    }
  };
  make_sel();
  const u32 gvo = w * 4096 + l16;
  auto pw_of = [&](u32 l) { return L_P + ((l >> 3) & 3) * 1024 + (w >> 1) * 256 + (l & 7) * 32 + (l >> 5) * 16 + (((w ^ (l >> 3)) & 1) * 8); };
  const u32 pw = pw_of(lane);
  const u32 pr = L_P + gvo;
  auto g_load = [&](bf16x8 (&f)[4], u32 src) {
#pragma unroll
    for (int t = 0; t < 4; ++t) f[t] = asbf(ld16(wsr, gvo + t * 1024, src));
  };
  auto g_wait = [&](bf16x8 (&f)[4], u32 src) {
    while (unwritten(f)) {
      if (timed_out(t0)) break;
      __builtin_amdgcn_s_sleep(1);
      FENCE();
      g_load(f, src);
    }
  };
  auto row_stats = [&](int half, const bf16x8 (&f)[4]) {
    float s0 = 0.f, s1 = 0.f, q0 = 0.f, q1 = 0.f;
#pragma unroll
    for (int t = 0; t < 4; ++t) {
      const u32x4 d = asu(f[t]);
      s0 = dot2(d[0], 0x3F803F80u, s0); q0 = dot2(d[0], d[0], q0); s1 = dot2(d[1], 0x3F803F80u, s1); q1 = dot2(d[1], d[1], q1);
      s0 = dot2(d[2], 0x3F803F80u, s0); q0 = dot2(d[2], d[2], q0); s1 = dot2(d[3], 0x3F803F80u, s1); q1 = dot2(d[3], d[3], q1);
    }
    const f32x2 sq = {swap32_add(s0 + s1), swap32_add(q0 + q1)};
    if (lane < 32) ldsw<f32x2>(L_STAT + half * STH + lane * STP + w * 8, sq);
  };
  auto partials = [&](int half, f32x16& acc0, f32x16& acc1, u32 pw) {
#pragma unroll
    for (int a = 0; a < 4; ++a) {
      ldsw<u32x2>(pw + half * 32768 + a * 4096, (u32x2){cvtpk(acc0[4 * a], acc0[4 * a + 1]), cvtpk(acc0[4 * a + 2], acc0[4 * a + 3])});
      ldsw<u32x2>(pw + half * 32768 + (4 + a) * 4096, (u32x2){cvtpk(acc1[4 * a], acc1[4 * a + 1]), cvtpk(acc1[4 * a + 2], acc1[4 * a + 3])});
    }
  };
  auto slice_gemm = [&](int half, const bf16x8 (&a)[8], const bf16x8 (&f)[4]) {
    f32x16 acc0 = {}, acc1 = {};
#pragma unroll
    for (int t = 0; t < 4; ++t) { acc0 = mfma32(a[t], f[t], acc0); acc1 = mfma32(a[4 + t], f[t], acc1); }
    partials(half, acc0, acc1, pw);
  };
  auto reduce_at = [&](int half, u32 pr_) -> f32x4 {
    f32x4 acc = {0, 0, 0, 0};
#pragma unroll
    for (int q = 0; q < 4; ++q) acc = mfma16(sel[q], ldsr<bf16x8>(pr_ + half * 32768 + q * 1024), acc);
    return acc;
  };
  auto reduce = [&](int half) { return reduce_at(half, pr); };
  auto src_of = [&](u32 base, int half) { return base + blk_of(half) * 32768; };
  auto pack4 = [&](f32x4 v) { return (u32x2){canon(cvtpk(v[0], v[1])), canon(cvtpk(v[2], v[3]))}; };
  const u32 a3off = w < 7 ? ((7 * j + w) * 32) * 1024 + l16 : OOB;
  auto a3_load = [&](int t0_, int t1_) {
#pragma unroll
    for (int t = 0; t < 32; ++t) if (t >= t0_ && t < t1_) a3[t] = asbf(ld16(wsr, a3off, WS_W3 + t * 1024));
  };

  f32x4 hres[2], xp[2], cs1;
  bf16x8 fA[4], fB[4];
  auto phase = [&](const bf16x8 (&a)[8], u32 src, bool stats, auto&& first, auto&& epi, auto&& put, auto&& pre, auto&& slotA, auto&& slotB, int sbase) {
    PSTAMP(sbase, 0);
    g_wait(fA, src_of(src, 0));
    STAMP(sbase);
    PSTAMP(sbase, 1);
    if (stats) row_stats(0, fA);
    first();
    PSTAMP(sbase, 2);
    slice_gemm(0, a, fA);
    PSTAMP(sbase, 3);
    g_load(fB, src_of(src, 1));
    slotA();
    PSTAMP(sbase, 4);
    BAR();
    PSTAMP(sbase, 5);
    const f32x4 rA = reduce(0);
    const u32x2 vA = epi(0, rA);
    PSTAMP(sbase, 6);
    put(0, vA);
    PSTAMP(sbase, 7);
    g_wait(fB, src_of(src, 1));
    STAMP(sbase + 1);
    PSTAMP(sbase, 8);
    if (stats) row_stats(1, fB);
    PSTAMP(sbase, 9);
    slice_gemm(1, a, fB);
    PSTAMP(sbase, 10);
    slotB();
    PSTAMP(sbase, 11);
    BAR();
    PSTAMP(sbase, 12);
    const u32x2 vB = epi(1, reduce(1));
    PSTAMP(sbase, 13);
    pre();
    PSTAMP(sbase, 14);
    put(1, vB);
    PSTAMP(sbase, 15);
  };
  auto lane_stat = [&](int half) {
    const u32 sa = L_STAT + half * STH + (lane >> 1) * STP + (lane & 1) * 32;
    const f32x4 p0 = ldsr<f32x4>(sa), p1 = ldsr<f32x4>(sa + 16);
    return finish_stat(xor1_add((p0[0] + p0[2]) + (p1[0] + p1[2])), xor1_add((p0[1] + p0[3]) + (p1[1] + p1[3])));
  };
  const u32 a4off = (2 * j * 112 + 14 * w) * 1024 + l16;
  g_load(fA, src_of(WS_OX, 0));
  {
    float c[2] = {0.f, 0.f};
#pragma unroll
    for (int t = 0; t < 8; ++t) {
      const u32x4 d = asu(a_1[t]);
#pragma unroll
      for (int e = 0; e < 4; ++e) c[t >> 2] = dot2(d[e], 0x3F803F80u, c[t >> 2]);
    }
    const float c0 = swap32_add(c[0]), c1 = swap32_add(c[1]);
    ldsw<float>(L_CS1P + w * 256 + lane * 4, lane < 32 ? c0 : c1);
  }
  phase(a_o, WS_OX, false,
        [&]() { BAR(); },
        [&](int half, f32x4 acc) {
          if (half == 0) {
            cs1 = (f32x4){0.f, 0.f, 0.f, 0.f};
#pragma unroll
            for (int v = 0; v < 8; ++v) cs1 += ldsr<f32x4>(L_CS1P + v * 256 + w * 32 + (lane & 1) * 16);
          }
          hres[half] = acc + (f32x4){bflo(xres[half][0]), bfhi(xres[half][0]), bflo(xres[half][1]), bfhi(xres[half][1])};
          return pack4(hres[half]);
        },
        [&](int half, u32x2 v) { st8(wsr, hx_off(WS_HX, half), 0, v); },
        [&]() { g_load(fA, src_of(WS_HX, 0)); },
        [&]() {
#pragma unroll
          for (int i = 0; i < 7; ++i) dma1(ws + WS_W4 + i * 1024, (((u32)(b >> 6) & 3u) * 8 + w) * 7168 + j * 229376 + l16, lb + L_DUM1);
        },
        [&]() { a3_load(A3_N0, A3_N0 + 5); }, 10);
  STAMP(12);
  phase(a_1, WS_HX, true, [&]() { },
        [&](int half, f32x4 acc) {
          const RowStat st = lane_stat(half);
          const float r2 = __builtin_amdgcn_rsqf(st.var * st.rstd * st.rstd + LN_EPS), rr = st.rstd * r2;
          xp[half] = (hres[half] - st.mu) * st.rstd;
          const f32x4 pre = (acc - st.mu * cs1) * rr;
          return pack4((f32x4){gelu(pre[0]), gelu(pre[1]), gelu(pre[2]), gelu(pre[3])});
        },
        [&](int half, u32x2 v) { st8(wsr, ts_off(half), 0, v); },
        [&]() { g_load(fA, src_of(WS_TS, 0)); },
        [&]() { a3_load(A3_N0 + 5, A3_N0 + 10); },
        [&]() { a3_load(A3_N0 + 10, A3_N0 + 13); dma4_sc1(ws + WS_W4 + 10 * 1024, a4off, lb + L_A4 + w * 8192); }, 13);
  STAMP(15);
  phase(a_2, WS_TS, false, [&]() { },
        [&](int half, f32x4 acc) {
          const f32x4 x1 = xp[half] + acc;
          ldsw<f32x4>(L_RES + half * 8192 + w * 1024 + l16, hres[half] + x1);
          return pack4(x1);
        },
        [&](int half, u32x2 v) { st8(wsr, hx_off(WS_X1, half), 0, v); },
        [&]() { dma4_sc1(ws + src_of(WS_X1, 0), gvo, lb + L_P + w * 4096); },
        [&]() { a3_load(A3_N0 + 13, A3_N0 + 16); dma4_sc1(ws + WS_W4 + 114688 + 10 * 1024, a4off, lb + L_A4 + w * 8192 + 4096); },
        [&]() { a3_load(A3_N0 + 16, 32); }, 16);
  STAMP(18);
  bf16x8 a4[10][2];
  auto a4_load = [&](int n) { a4[n >> 1][n & 1] = asbf(ld16(wsr, a4off, WS_W4 + (n & 1) * 114688 + (n >> 1) * 1024)); };
  u32x4 uf[14];
  auto u_load1 = [&](int half, int t) { uf[t] = ld16(wsr, l16, WS_UX + (blk_of(half) * 112 + w * 14 + t) * 1024); };
  {
    auto x1_finish = [&](int half) {
      for (;;) {
#pragma unroll
        for (int t = 0; t < 4; ++t) fA[t] = ldsr<bf16x8>(pr + half * 32768 + t * 1024);
        if (!unwritten(fA)) break;
        if (timed_out(t0)) break;
        __builtin_amdgcn_s_sleep(1);
        dma4_sc1(ws + src_of(WS_X1, half), gvo, lb + L_P + half * 32768 + w * 4096);
        WAITV(0);
      }
      row_stats(half, fA);
    };
    struct UOut { float rstd, nm; f32x4 cs[2]; float gv[4]; u32 up[8]; u32 l; };
    auto u_stat = [&](UOut& u, int half) {
      u.l = opaque<u32>(lane);
      const u32 sr = L_STAT + half * STH + (u.l & 31) * STP;
      const f32x4 p0 = ldsr<f32x4>(sr), p1 = ldsr<f32x4>(sr + 16), p2 = ldsr<f32x4>(sr + 32), p3 = ldsr<f32x4>(sr + 48);
      const f32x4 p = (p0 + p1) + (p2 + p3);
      const RowStat st = finish_stat(p[0] + p[2], p[1] + p[3]);
      u.rstd = st.rstd; u.nm = -st.mu * st.rstd;
    };
    auto u_cs = [&](UOut& u, int a) { u.cs[a & 1] = ldsr<f32x4>(L_CS + (32 * w + 8 * a) * 4 + (u.l >> 5) * 16); };
    auto u_val = [&](UOut& u, int half, const f32x16& acc, int v) {
      const int a = v >> 2, e = v & 3;
      u.gv[e] = gelu(acc[v] * u.rstd + u.nm * u.cs[a & 1][e]);
      if (e == 3) { u.up[2 * a] = canon(cvtpk(u.gv[0], u.gv[1])); u.up[2 * a + 1] = canon(cvtpk(u.gv[2], u.gv[3])); }
      if (v == 7 || v == 15) {
        const u32 uo = w < 7 ? WS_UX + (blk_of(half) * 112 + j * 14 + 2 * w) * 1024 + u.l * 16 : OOB;
        if (v == 7) st16(wsr, uo, 0, (u32x4){u.up[0], u.up[1], u.up[2], u.up[3]});
        else st16(wsr, uo, 1024, (u32x4){u.up[4], u.up[5], u.up[6], u.up[7]});
      }
    };
    WAITV(1);
    x1_finish(0);
    BAR();
    STAMP(19);
    dma4_sc1(ws + src_of(WS_X1, 1), gvo, lb + L_P + 32768 + w * 4096);
    const u32 xb = L_P + (w < 7 ? l16 : 0u);
    f32x16 accA = {}, accB = {};
    bf16x8 bq[4];
    {
      float c0 = 0.f, c1 = 0.f;
#pragma unroll
      for (int t = 0; t < 4; ++t) bq[t] = ldsr<bf16x8>(xb + t * 1024);
#pragma unroll
      for (int t = 0; t < 32; ++t) {
        accA = mfma32(a3[t], bq[t & 3], accA);
        if (t + 4 < 32) bq[t & 3] = ldsr<bf16x8>(xb + (t + 4) * 1024);
        const u32x4 d = asu(a3[t]);
        c0 = dot2(d[0], 0x3F803F80u, c0); c1 = dot2(d[1], 0x3F803F80u, c1); c0 = dot2(d[2], 0x3F803F80u, c0); c1 = dot2(d[3], 0x3F803F80u, c1);
        if (t >= 4 && (t & 3) == 0 && t / 4 - 1 < A4_N0) a4_load(t / 4 - 1);
        __builtin_amdgcn_sched_barrier(0);
      }
      const float c = swap32_add(c0 + c1);
      if (lane < 32) ldsw<float>(L_CS + w * 128 + lane * 4, c);
    }
    STAMP(20);
    asm volatile("s_waitcnt vmcnt(%0)" :: "n"(A4_N0) : "memory");
    x1_finish(1);
    BAR();
    STAMP(21);
    {
      UOut u;
#pragma unroll
      for (int t = 0; t < 4; ++t) bq[t] = ldsr<bf16x8>(xb + 32768 + t * 1024);
      u_stat(u, 0); u_cs(u, 0);
#pragma unroll
      for (int t = 0; t < 32; ++t) {
        if (t < 16 && (t & 3) == 1 && t + 3 < 16) u_cs(u, (t + 3) >> 2);
        accB = mfma32(a3[t], bq[t & 3], accB);
        if (t + 4 < 32) bq[t & 3] = ldsr<bf16x8>(xb + 32768 + (t + 4) * 1024);
        if (t < 16) u_val(u, 0, accA, t);
        const int n = A4_N0 + t - 1;
        if (t >= 1 && n < 20) a4_load(n);
        __builtin_amdgcn_sched_barrier(0);
      }
    }
    STAMP(22);
    {
      UOut u;
      u_stat(u, 1); u_cs(u, 0);
#pragma unroll
      for (int v = 0; v < 16; ++v) {
        if ((v & 3) == 0 && v + 4 < 16) u_cs(u, (v + 4) >> 2);
        if (v < 14) u_load1(0, v);
        u_val(u, 1, accB, v);
        __builtin_amdgcn_sched_barrier(0);
      }
    }
    STAMP(23);
  }
  {
    auto u_wait = [&](int half) {
      for (;;) {
        u32 m = 0;
#pragma unroll
        for (int t = 0; t < 14; ++t) m = umax(m, max4(uf[t]));
        if (!__any(m == SENT)) break;
        if (timed_out(t0)) break;
        __builtin_amdgcn_s_sleep(2);
        FENCE();
#pragma unroll
        for (int t = 0; t < 14; ++t) u_load1(half, t);
      }
    };
#pragma unroll
    for (int half = 0; half < 2; ++half) {
      u_wait(half);
      STAMP(24 + half);
      const u32 sa = L_A4 + w * 8192 + opaque<u32>(l16);
      f32x16 acc0 = {}, acc1 = {};
      bf16x8 sq[4][2];
#pragma unroll
      for (int t = 0; t < 14; ++t) {
        if (t >= 8 && t < 12) { sq[t - 8][0] = ldsr<bf16x8>(sa + (t - 8) * 1024); sq[t - 8][1] = ldsr<bf16x8>(sa + 4096 + (t - 8) * 1024); }
        acc0 = mfma32(t < 10 ? a4[t < 10 ? t : 0][0] : sq[t < 10 ? 0 : t - 10][0], asbf(uf[t]), acc0);
        acc1 = mfma32(t < 10 ? a4[t < 10 ? t : 0][1] : sq[t < 10 ? 0 : t - 10][1], asbf(uf[t]), acc1);
        if (half == 0 && t >= 7) { u_load1(1, 2 * (t - 7)); u_load1(1, 2 * (t - 7) + 1); }
        __builtin_amdgcn_sched_barrier(0);
      }
      if (half == 0) BAR();
      partials(half, acc0, acc1, pw_of(opaque<u32>(lane)));
      FENCE();
    }
    STAMP(26);
    make_sel();
    BAR();
#pragma unroll
    for (int half = 0; half < 2; ++half) {
      const u32 l = opaque<u32>(lane);
      const f32x4 v = reduce_at(half, L_P + w * 4096 + l * 16) + ldsr<f32x4>(L_RES + half * 8192 + w * 1024 + l * 16);
      *(GAS u32x2*)((char*)out + (u32)((blk_of(half) * 32 + (l >> 1)) * 1024 + (64 * j + 8 * w + 4 * (l & 1)) * 2)) = (u32x2){cvtpk(v[0], v[1]), cvtpk(v[2], v[3])};
    }
  }
  STAMP(27);
}

extern "C" void kernel_launch(void* const* d_in, const int* in_sizes, int n_in, void* d_out, int out_size, void* d_ws, size_t ws_size, hipStream_t stream) {
  (void)in_sizes; (void)n_in; (void)out_size; (void)ws_size;
  fused_block_kernel<<<256, 512, 0, stream>>>((const u16*)d_in[0], (const u16*)d_in[1], (const u16*)d_in[2], (const u16*)d_in[3], (const u16*)d_in[4],
                                              (const u16*)d_in[6], (const u16*)d_in[7], (const u16*)d_in[8], (const u16*)d_in[9], (u16*)d_out, (char*)d_ws);
}
```

```cpp
#define SPIN_AT 18
#define SPIN_T 6000ull
#include <hip/hip_runtime.h>
#include <stdint.h>
#include <type_traits>

typedef unsigned short u16;
typedef unsigned int u32;
typedef unsigned long long u64;
typedef short bf16x8 __attribute__((ext_vector_type(8)));
typedef short s16x4 __attribute__((ext_vector_type(4)));
typedef float f32x2 __attribute__((ext_vector_type(2)));
typedef float f32x4 __attribute__((ext_vector_type(4)));
typedef float f32x16 __attribute__((ext_vector_type(16)));
typedef u32 u32x2 __attribute__((ext_vector_type(2)));
typedef u32 u32x4 __attribute__((ext_vector_type(4)));
typedef __bf16 bf16x2_t __attribute__((ext_vector_type(2)));
#define LAS __attribute__((address_space(3)))
#define GAS __attribute__((address_space(1)))
#define DI __device__ __forceinline__

constexpr u32 WS_EPOCH = 0x0;
constexpr u32 WS_FKV = 0x1000;
constexpr u32 WS_FW = 0x5000;
constexpr u32 WS_K = 0x10000;
constexpr u32 WS_V = 0x20000;
constexpr u32 WS_DBG = 0x1E00000;
constexpr u32 WS_WO = 0x100000, WS_W1 = 0x180000, WS_W2 = 0x200000, WS_W3 = 0x280000, WS_W4 = 0x440000;
constexpr u32 WS_OX = 0x600000, WS_HX = 0x800000, WS_TS = 0xA00000, WS_X1 = 0xC00000, WS_UX = 0xE00000;
constexpr u32 WS_STAMP = 0x2000000;
constexpr u32 WS_END = 0x2100000;
constexpr u32 OOB = 0x80000000u;

constexpr u32 XP = 1040;
constexpr u32 L_XIMG = 0;
constexpr u32 L_WQ = 66560;
constexpr u32 L_WKV = 132096;
constexpr u32 S_PM = 148480, S_PC = 156672, S_PS = 157184, S_PQ = 157696, S_FIN = 158208, S_QS = 158336, S_QC = 159360;
constexpr u32 L_DUM1 = 160384;
constexpr u32 L_CNT = 161408;
constexpr u32 L_K = 0, L_V = 65536, L_ONES = 131072;
constexpr u32 L_STG = 135424;
constexpr u32 L_CS1P = 155904;
constexpr u32 L_P = 0;
constexpr u32 L_STAT = 65536;
constexpr u32 STP = 80, STH = 2560;
constexpr u32 L_CS = 71680;
constexpr u32 L_RES = 73728;
constexpr u32 L_A4 = 90112;
constexpr u32 L_STAMP = 161792;
constexpr u32 LDS_BYTES = 163840;

__shared__ __attribute__((aligned(16))) char smem[LDS_BYTES];

constexpr u32 SENT = 0xFFFFFFFFu;
constexpr float LN_EPS = 1e-5f;
constexpr float QSCALE = 0.25f * 1.4426950408889634f;
#define CODE_KB 43

DI f32x16 mfma32(bf16x8 a, bf16x8 b, f32x16 c) { return __builtin_amdgcn_mfma_f32_32x32x16_bf16(a, b, c, 0, 0, 0); }
DI f32x4 mfma16(bf16x8 a, bf16x8 b, f32x4 c) { return __builtin_amdgcn_mfma_f32_16x16x32_bf16(a, b, c, 0, 0, 0); }
DI u32 cvtpk(float lo, float hi) { f32x2 v = {lo, hi}; bf16x2_t b = __builtin_convertvector(v, bf16x2_t); return __builtin_bit_cast(u32, b); }
DI u32 canon(u32 d) { return d < 0xFFFFFFFEu ? d : 0xFFFFFFFEu; }
DI float bflo(u32 d) { return __uint_as_float(d << 16); }
DI float bfhi(u32 d) { return __uint_as_float(d & 0xFFFF0000u); }
DI float dot2(u32 a, u32 b, float c) { return __builtin_amdgcn_fdot2_f32_bf16(__builtin_bit_cast(bf16x2_t, a), __builtin_bit_cast(bf16x2_t, b), c, false); }
DI float gelu(float x) {
  const float c1 = -2.0f * 0.7978845608028654f * 1.4426950408889634f, c2 = c1 * 0.044715f;
  const float e = __builtin_amdgcn_exp2f(x * (c1 + c2 * x * x));
  return x * __builtin_amdgcn_rcpf(1.0f + e);
}
DI float swap32_add(float v) {
  const auto r = __builtin_amdgcn_permlane32_swap(__float_as_uint(v), __float_as_uint(v), false, false);
  return __uint_as_float(r[0]) + __uint_as_float(r[1]);
}
DI float xor1_add(float v) { return v + __int_as_float(__builtin_amdgcn_mov_dpp(__float_as_int(v), 0xB1, 0xF, 0xF, true)); }

#define BAR() asm volatile("s_waitcnt lgkmcnt(0)\n\ts_barrier" ::: "memory")
#define WAITV(n) asm volatile("s_waitcnt vmcnt(" #n ")" ::: "memory")
#define FENCE() asm volatile("" ::: "memory")
template <class T> DI T opaque(T v) { asm volatile("" : "+v"(v)); return v; }

template <class T> DI T ldsr(u32 off) { return *(const LAS T*)((LAS char*)smem + off); }
template <class T> DI void ldsw(u32 off, T v) { *(LAS T*)((LAS char*)smem + off) = v; }
DI s16x4 ldstr(u32 off) {
  typedef short v4i16_t __attribute__((ext_vector_type(4)));
  return __builtin_bit_cast(s16x4, __builtin_amdgcn_ds_read_tr16_b64_v4i16((LAS v4i16_t*)((LAS char*)smem + off)));
}
DI bf16x8 cat(s16x4 a, s16x4 b) { return __builtin_shufflevector(a, b, 0, 1, 2, 3, 4, 5, 6, 7); }
DI u32 lds_base() { return (u32)(uintptr_t)(LAS char*)smem; }

#define DMA_HEAD "s_mov_b32 %[k], m0\n\ts_mov_b32 m0, %[lds]\n\ts_nop 0\n\t"
#define DMA_TAIL "s_mov_b32 m0, %[k]"
DI void dma1(const void* sbase, u32 voff, u32 ldsdst) {
  u32 keep;
  asm volatile(DMA_HEAD "global_load_lds_dwordx4 %[v], %[s]\n\t" DMA_TAIL : [k] "=&s"(keep) : [v] "v"(voff), [s] "s"(sbase), [lds] "s"(ldsdst) : "memory");
}
DI void dma2(const void* sbase, u32 voff, u32 ldsdst) {
  u32 keep;
  asm volatile(DMA_HEAD "global_load_lds_dwordx4 %[v], %[s]\n\tglobal_load_lds_dwordx4 %[v], %[s] offset:1024\n\t" DMA_TAIL
               : [k] "=&s"(keep) : [v] "v"(voff), [s] "s"(sbase), [lds] "s"(ldsdst) : "memory");
}
DI void dma4_rows(const void* sbase, u32 v0, u32 v1, u32 v2, u32 v3, u32 ldsdst) {
  u32 keep;
  asm volatile(DMA_HEAD "global_load_lds_dwordx4 %[a], %[s]\n\tglobal_load_lds_dwordx4 %[b], %[s] offset:1040\n\t"
               "global_load_lds_dwordx4 %[c], %[s] offset:2080\n\tglobal_load_lds_dwordx4 %[d], %[s] offset:3120\n\t" DMA_TAIL
               : [k] "=&s"(keep) : [a] "v"(v0), [b] "v"(v1), [c] "v"(v2), [d] "v"(v3), [s] "s"(sbase), [lds] "s"(ldsdst) : "memory");
}
DI void dma4_sc1(const void* sbase, u32 voff, u32 ldsdst) {
  u32 keep;
  asm volatile(DMA_HEAD "global_load_lds_dwordx4 %[v], %[s] sc1\n\tglobal_load_lds_dwordx4 %[v], %[s] offset:1024 sc1\n\t"
               "global_load_lds_dwordx4 %[v], %[s] offset:2048 sc1\n\tglobal_load_lds_dwordx4 %[v], %[s] offset:3072 sc1\n\t" DMA_TAIL
               : [k] "=&s"(keep) : [v] "v"(voff), [s] "s"(sbase), [lds] "s"(ldsdst) : "memory");
}
#define DMA_STEP(SC) "global_load_lds_dwordx4 %[v], %[s]" SC "\n\ts_add_u32 m0, m0, %[ls]\n\tv_add_u32 %[v], %[gs], %[v]\n\t"
template <int N, bool SC1, u32 GS>
DI void dma_strided(const void* sbase, u32 voff, u32 ldsdst, u32 lstep) {
  u32 keep;
  static_assert(N == 2 || N == 3 || N == 4 || N == 8, "");
#define DMA_RUN(BODY) asm volatile(DMA_HEAD BODY DMA_TAIL : [k] "=&s"(keep), [v] "+v"(voff) : [s] "s"(sbase), [lds] "s"(ldsdst), [ls] "s"(lstep), [gs] "n"(GS) : "memory", "scc")
  if constexpr (SC1) {
    if constexpr (N == 2) DMA_RUN(DMA_STEP(" sc1") DMA_STEP(" sc1"));
    if constexpr (N == 3) DMA_RUN(DMA_STEP(" sc1") DMA_STEP(" sc1") DMA_STEP(" sc1"));
    if constexpr (N == 4) DMA_RUN(DMA_STEP(" sc1") DMA_STEP(" sc1") DMA_STEP(" sc1") DMA_STEP(" sc1"));
  } else {
    if constexpr (N == 8) DMA_RUN(DMA_STEP("") DMA_STEP("") DMA_STEP("") DMA_STEP("") DMA_STEP("") DMA_STEP("") DMA_STEP("") DMA_STEP(""));
  }
#undef DMA_RUN
}

typedef __amdgpu_buffer_rsrc_t rsrc_t;
DI rsrc_t mkrsrc(const void* p, u32 bytes) { return __builtin_amdgcn_make_buffer_rsrc((void*)p, 0, bytes, 0x00020000); }
DI u32x4 ld16(rsrc_t r, u32 voff, u32 soff) { return __builtin_amdgcn_raw_buffer_load_b128(r, voff, soff, 16); }
DI void st16(rsrc_t r, u32 voff, u32 soff, u32x4 v) { __builtin_amdgcn_raw_buffer_store_b128(v, r, voff, soff, 16); }
DI void st8(rsrc_t r, u32 voff, u32 soff, u32x2 v) { __builtin_amdgcn_raw_buffer_store_b64(v, r, voff, soff, 16); }
DI void st4(rsrc_t r, u32 voff, u32 soff, u32 v) { __builtin_amdgcn_raw_buffer_store_b32(v, r, voff, soff, 16); }
DI bf16x8 asbf(u32x4 v) { return __builtin_bit_cast(bf16x8, v); }
DI u32x4 asu(bf16x8 v) { return __builtin_bit_cast(u32x4, v); }
DI u32 umax(u32 a, u32 b) { return a > b ? a : b; }
DI u32 max4(u32x4 v) { return umax(umax(v[0], v[1]), umax(v[2], v[3])); }

constexpr u64 SPIN_TICKS = 400000ull;
DI bool timed_out(u64 t0) { return __builtin_amdgcn_s_memrealtime() - t0 > SPIN_TICKS; }

#define STAMP(i) do { } while (0)
#define ASTAMP(it, pos) do { } while (0)
#define PSTAMP(sb, i) do { } while (0)
#undef STAMP
#define STAMP(i) do { if ((i) == SPIN_AT) { while (__builtin_amdgcn_s_memrealtime() - t0 < SPIN_T) __builtin_amdgcn_s_sleep(1); } } while (0)
#define MFMA_GAP16(v) asm volatile("s_nop 7" : "+v"(v))
#define MFMA_GAP32(v) asm volatile("s_nop 7\n\ts_nop 3" : "+v"(v))

DI bool flags_ok(u32x4 a, u32x4 b, u32 lo, u32 hi) {
  return __all(a[0] == lo && a[1] == hi && a[2] == lo && a[3] == hi && b[0] == lo && b[1] == hi && b[2] == lo && b[3] == hi);
}
DI void wait_flags(rsrc_t wsr, u32 base, u32 taglo, u32 taghi, int lane, u64 t0) {
  for (;;) {
    const u32x4 a = ld16(wsr, lane * 32, base), b = ld16(wsr, lane * 32 + 16, base);
    if (flags_ok(a, b, taglo, taghi)) break;
    if (timed_out(t0)) break;
    __builtin_amdgcn_s_sleep(1);
    FENCE();
  }
}

DI bf16x8 ones_frag() { return asbf((u32x4){0x3F803F80u, 0x3F803F80u, 0x3F803F80u, 0x3F803F80u}); }
DI float diag_pick(f32x4 g, int lane) { const int r = lane & 3; float a = (r & 1) ? g[1] : g[0], b = (r & 1) ? g[3] : g[2]; return (r & 2) ? b : a; }

struct RowStat { float mu, rstd, var; };
DI RowStat finish_stat(float s, float q) {
  RowStat r; r.mu = s * (1.0f / 512.0f);
  r.var = fmaxf(q * (1.0f / 512.0f) - r.mu * r.mu, 0.0f);
  r.rstd = __builtin_amdgcn_rsqf(r.var + LN_EPS);
  return r;
}
DI bool unwritten(const bf16x8 (&f)[4]) {
  return __any(umax(umax(max4(asu(f[0])), max4(asu(f[1]))), umax(max4(asu(f[2])), max4(asu(f[3])))) == SENT);
}

template <int I, int N, class F> DI void static_for(F&& f) {
  if constexpr (I < N) { f(std::integral_constant<int, I>{}); static_for<I + 1, N>(f); }
}
struct Job { const char* src; u32 prow; bool perm; };
DI Job retile_job(u32 f, u64 wo, u64 w1, u64 w2, u64 w3, u64 w4) {
  const bool big = f >= 1536, last = f >= 3328;
  const u32 g = f - 3328, T = last ? g / 112 : big ? (f - 1536) >> 5 : (f >> 5) & 15, ks = last ? g % 112 : f & 31;
  const u32 prow = big && !last ? 3584 : 1024;
  u64 m = f < 1024 ? w1 : w2;
  m = f < 512 ? wo : m; m = big ? w3 : m; m = last ? w4 : m;
  return Job{(const char*)(m + (u64)(16 * ks * prow + T * 64)), prow, last || f < 512};
}

#define A3_N0 6
#define A4_N0 6

__global__ __launch_bounds__(512, 2) void fused_block_kernel(const u16* __restrict__ x, const u16* __restrict__ wq, const u16* __restrict__ wk,
                                                             const u16* __restrict__ wv, const u16* __restrict__ wo, const u16* __restrict__ w1,
                                                             const u16* __restrict__ w2, const u16* __restrict__ w3, const u16* __restrict__ w4,
                                                             u16* __restrict__ out, char* ws) {
  const int tid = threadIdx.x, lane = tid & 63, w = __builtin_amdgcn_readfirstlane(tid >> 6);
  const int b = blockIdx.x;
  const int cl = (b & 7) * 4 + (b >> 6), j = (b >> 3) & 7;
  const u32 lb = lds_base();
  const u32 l16 = lane * 16;
  {
    u64 pc;
    asm volatile("s_getpc_b64 %0" : "=s"(pc));
    dma1((const void*)(pc & ~63ull), (u32)((((b >> 3) * 8 + w) % CODE_KB) * 1024) + l16, lb + L_DUM1);
  }
  asm volatile("" :: "s"(x), "s"(wq), "s"(wk), "s"(wv), "s"(wo), "s"(w1), "s"(w2), "s"(w3), "s"(w4), "s"(out), "s"(ws));
  const u64 t0 = __builtin_amdgcn_s_memrealtime();
  const int i16 = lane & 15, g4 = lane >> 4, r32 = lane & 31, h2 = lane >> 5;
  STAMP(0);

  const int kvrt = j >> 1, kvmat = j & 1;
  auto grow = [&](int rho) { return (rho < 32 ? 0 : 1024 - 32) + 32 * cl + rho; };
  const int R0 = grow(16 * kvrt);
  u32x4 xk0, xk1;
  u64 tag;
  {
    const u32 xo = (u32)(i16 * 1024 + g4 * 32);
    asm volatile("global_load_dwordx4 %0, %2, %3\n\tglobal_load_dwordx4 %1, %2, %3 offset:16"
                 : "=&v"(xk0), "=&v"(xk1) : "v"(xo), "s"((const char*)x + R0 * 1024 + w * 128) : "memory");
    dma2((const char*)(kvmat ? wv : wk) + w * 2048, l16, lb + L_WKV + w * 2048);
    asm volatile("s_load_dwordx2 %0, %1, 0x0" : "=s"(tag) : "s"(ws + WS_EPOCH) : "memory");
    const char* xr = (const char*)x + grow(8 * w) * 1024 - 48;
    dma4_rows(xr, l16 + 48, l16 + 32, l16 + 16, l16, lb + L_XIMG + 8 * w * XP);
    dma4_rows(xr + 4096, l16 + 48, l16 + 32, l16 + 16, l16, lb + L_XIMG + (8 * w + 4) * XP);
    dma_strided<8, false, 0x2000>((const char*)wq + w * 65536 + j * 128, (u32)((lane >> 3) * 1024 + (((lane & 7) ^ (4 * ((lane >> 4) & 1))) * 16)), lb + L_WQ + w * 8192, 1024);
    asm volatile("s_waitcnt vmcnt(16) lgkmcnt(0)" : "+v"(xk0), "+v"(xk1), "+s"(tag) :: "memory");
  }
  tag += 1ull;
  const u32 taglo = (u32)tag, taghi = (u32)(tag >> 32);
  const rsrc_t wsr = mkrsrc(ws, WS_END);
  const bf16x8 one = ones_frag();
  STAMP(1);

  {
    f32x4 am = {0, 0, 0, 0}, ac = {0, 0, 0, 0}, as = {0, 0, 0, 0}, aq = {0, 0, 0, 0};
    const u32 wa = L_WKV + w * 2048 + (16 * g4 + (i16 >> 2)) * 32 + (i16 & 3) * 8;
    const bf16x8 wf0 = cat(ldstr(wa), ldstr(wa + 128)), wf1 = cat(ldstr(wa + 256), ldstr(wa + 384));
    const bf16x8 xf0 = asbf(xk0), xf1 = asbf(xk1);
    as = mfma16(one, xf0, as); aq = mfma16(xf0, xf0, aq); as = mfma16(one, xf1, as); aq = mfma16(xf1, xf1, aq);
    if (kvmat) {
      am = mfma16(xf0, wf0, am); ac = mfma16(one, wf0, ac); am = mfma16(xf1, wf1, am); ac = mfma16(one, wf1, ac);
      MFMA_GAP16(am); MFMA_GAP16(ac);
      if (lane < 16) ldsw<float>(S_PC + (w * 16 + lane) * 4, ac[0]);
    } else {
      am = mfma16(wf0, xf0, am); ac = mfma16(wf0, one, ac); am = mfma16(wf1, xf1, am); ac = mfma16(wf1, one, ac);
      MFMA_GAP16(am); MFMA_GAP16(ac);
      if (i16 == 0) ldsw<f32x4>(S_PC + (w * 16 + 4 * g4) * 4, ac);
    }
    ldsw<f32x4>(S_PM + w * 1024 + l16, am);
    if (lane < 16) ldsw<float>(S_PS + (lane * 8 + w) * 4, as[0]);
    if ((i16 >> 2) == g4) ldsw<float>(S_PQ + (i16 * 8 + w) * 4, diag_pick(aq, lane));
    BAR();
  }
  if (w == 0) {
    {
      const f32x4 s0 = ldsr<f32x4>(S_PS + i16 * 32), s1 = ldsr<f32x4>(S_PS + i16 * 32 + 16);
      const f32x4 q0 = ldsr<f32x4>(S_PQ + i16 * 32), q1 = ldsr<f32x4>(S_PQ + i16 * 32 + 16);
      const f32x4 s = s0 + s1, q = q0 + q1;
      const RowStat st = finish_stat((s[0] + s[1]) + (s[2] + s[3]), (q[0] + q[1]) + (q[2] + q[3]));
      if (lane < 16) { ldsw<float>(S_FIN + lane * 4, st.mu); ldsw<float>(S_FIN + 64 + lane * 4, st.rstd); }
    }
    f32x4 m = {0, 0, 0, 0};
#pragma unroll
    for (int ww = 0; ww < 8; ++ww) m += ldsr<f32x4>(S_PM + ww * 1024 + l16);
    f32x4 v;
    u32 off;
    if (kvmat) {
      float c1 = 0.f;
#pragma unroll
      for (int ww = 0; ww < 8; ++ww) c1 += ldsr<float>(S_PC + (ww * 16 + i16) * 4);
      asm volatile("s_waitcnt lgkmcnt(0)" ::: "memory");
      const f32x4 mu = ldsr<f32x4>(S_FIN + g4 * 16), rs = ldsr<f32x4>(S_FIN + 64 + g4 * 16);
      v = rs * (m - mu * c1);
      off = WS_V + (R0 >> 5) * 1024 + ((R0 >> 4) & 1) * 512 + (g4 & 1) * 256 + i16 * 16 + (g4 >> 1) * 8;
    } else {
      f32x4 c4 = {0, 0, 0, 0};
#pragma unroll
      for (int ww = 0; ww < 8; ++ww) c4 += ldsr<f32x4>(S_PC + (ww * 16 + 4 * g4) * 4);
      asm volatile("s_waitcnt lgkmcnt(0)" ::: "memory");
      const float mu = ldsr<float>(S_FIN + i16 * 4), rs = ldsr<float>(S_FIN + 64 + i16 * 4);
      v = rs * (m - mu * c4);
      off = WS_K + (R0 >> 5) * 1024 + (g4 & 1) * 512 + ((R0 & 31) + i16) * 16 + (g4 >> 1) * 8;
    }
    st8(wsr, off, 0, (u32x2){cvtpk(v[0], v[1]), cvtpk(v[2], v[3])});
    WAITV(1);
  } else {
    WAITV(0);
  }
#define JOB(i) retile_job((u32)(b * 20 + w * 5 + (i)), (u64)wo, (u64)w1, (u64)w2, (u64)w3, (u64)w4)
  u32x4 rts[5];
  if (w < 4) {
#pragma unroll
    for (int i = 0; i < 5; ++i) {
      const Job jb = JOB(i);
      rts[i] = *(const GAS u32x4*)(jb.src + (u32)((lane >> 2) * jb.prow + (lane & 3) * 16));
    }
  }
  FENCE();
  STAMP(2);
  const int q_hp = w & 1, q_rt = (w >> 1) & 1, q_kh = w >> 2;
  const int head = 2 * q_hp + q_kh;
  bf16x8 qf;
  {
    BAR();
    STAMP(3);
    {
      f32x4 as = {0, 0, 0, 0}, aq = {0, 0, 0, 0};
      const int rt = w & 3, kh = w >> 2;
      const u32 sa = L_XIMG + (16 * rt + i16) * XP + kh * 512 + g4 * 16;
#pragma unroll
      for (int t = 0; t < 8; ++t) {
        const bf16x8 xf = ldsr<bf16x8>(sa + t * 64);
        as = mfma16(one, xf, as); aq = mfma16(xf, xf, aq);
      }
      MFMA_GAP16(as); MFMA_GAP16(aq);
      if (lane < 16) ldsw<float>(S_QS + ((16 * rt + lane) * 2 + kh) * 4, as[0]);
      if ((i16 >> 2) == g4) ldsw<float>(S_QS + 512 + ((16 * rt + i16) * 2 + kh) * 4, diag_pick(aq, lane));
    }
    f32x16 acc = {}, accc = {};
    const int q = i16 >> 2, p = i16 & 3, cb = g4 & 1;
    const u32 wa = L_WQ + (256 * q_kh + 8 * h2 + q) * 128 + (((4 * q_hp + 2 * cb + (p >> 1)) ^ (4 * ((q >> 1) & 1))) * 16) + (p & 1) * 8;
    const u32 xa = L_XIMG + (32 * q_rt + r32) * XP + q_kh * 512 + h2 * 16;
#pragma unroll
    for (int t = 0; t < 16; ++t) {
      const bf16x8 af = cat(ldstr(wa + t * 2048), ldstr(wa + t * 2048 + 512));
      acc = mfma32(af, ldsr<bf16x8>(xa + t * 32), acc);
    }
    const u32 wb = wa + q_rt * 16384;
#pragma unroll
    for (int t = 0; t < 8; ++t) accc = mfma32(cat(ldstr(wb + t * 2048), ldstr(wb + t * 2048 + 512)), one, accc);
    if (w == 0) {
      WAITV(5);
      if (lane < 8) __hip_atomic_store((GAS u64*)(ws + WS_FKV + lane * 2048 + b * 8), tag, __ATOMIC_RELAXED, __HIP_MEMORY_SCOPE_AGENT);
    }
    MFMA_GAP32(acc); MFMA_GAP32(accc);
    f32x4 k0, k1, s0, s1;
    if (q_kh) {
      k0 = (f32x4){acc[8], acc[9], acc[10], acc[11]}; k1 = (f32x4){acc[12], acc[13], acc[14], acc[15]};
      s0 = (f32x4){acc[0], acc[1], acc[2], acc[3]}; s1 = (f32x4){acc[4], acc[5], acc[6], acc[7]};
    } else {
      k0 = (f32x4){acc[0], acc[1], acc[2], acc[3]}; k1 = (f32x4){acc[4], acc[5], acc[6], acc[7]};
      s0 = (f32x4){acc[8], acc[9], acc[10], acc[11]}; s1 = (f32x4){acc[12], acc[13], acc[14], acc[15]};
    }
    ldsw<f32x4>(L_WKV + w * 2048 + l16, s0); ldsw<f32x4>(L_WKV + w * 2048 + 1024 + l16, s1);
    if (r32 == 0) {
#pragma unroll
      for (int a = 0; a < 4; ++a) ldsw<f32x4>(S_QC + (((q_hp * 2 + q_rt) * 2 + q_kh) * 32 + 8 * a + 4 * h2) * 4, (f32x4){accc[4 * a], accc[4 * a + 1], accc[4 * a + 2], accc[4 * a + 3]});
    }
    BAR();
    k0 += ldsr<f32x4>(L_WKV + (w ^ 4) * 2048 + l16); k1 += ldsr<f32x4>(L_WKV + (w ^ 4) * 2048 + 1024 + l16);
    f32x4 c0 = {0, 0, 0, 0}, c1 = {0, 0, 0, 0};
#pragma unroll
    for (int s = 0; s < 4; ++s) {
      c0 += ldsr<f32x4>(S_QC + ((q_hp * 4 + s) * 32 + 16 * q_kh + 4 * h2) * 4);
      c1 += ldsr<f32x4>(S_QC + ((q_hp * 4 + s) * 32 + 16 * q_kh + 8 + 4 * h2) * 4);
    }
    const f32x2 sp = ldsr<f32x2>(S_QS + (32 * q_rt + r32) * 8), qp = ldsr<f32x2>(S_QS + 512 + (32 * q_rt + r32) * 8);
    const RowStat st = finish_stat(sp[0] + sp[1], qp[0] + qp[1]);
    const float sc = st.rstd * QSCALE;
    k0 = (k0 - st.mu * c0) * sc; k1 = (k1 - st.mu * c1) * sc;
    qf = opaque(asbf((u32x4){cvtpk(k0[0], k0[1]), cvtpk(k0[2], k0[3]), cvtpk(k1[0], k1[1]), cvtpk(k1[2], k1[3])}));
  }
  STAMP(4);
  auto blk_of = [&](int half) { return half * 32 + cl; };
  const int dlt = (j >> 1) == 0 ? 1 : (j >> 1) == 1 ? -1 : (j >> 1) == 2 ? 2 : -2;
  const u32 pc_in = (u32)((4 * j + (w >> 1)) * 1024 + (w & 1) * 512);
  auto hx_off = [&](u32 base, int half) { return base + blk_of(half) * 32768 + pc_in + lane * 8; };
  auto ts_off = [&](int half) {
    const u32 l = opaque<u32>(lane);
    const u32 R = (u32)(blk_of(half) * 32 + (int)(l >> 1) + dlt);
    return R < 2048u ? WS_TS + (R >> 5) * 32768 + pc_in + (R & 31) * 16 + (l & 1) * 8 : OOB;
  };
  auto ox_off = [&]() { return WS_OX + blk_of(q_rt) * 32768 + (4 * j + head) * 1024 + l16; };
  {
    if (w == 1) {
      wait_flags(wsr, WS_FKV + (b & 7) * 2048, taglo, taghi, lane, t0);
      if (b == 0 && lane == 0) __hip_atomic_store((GAS u64*)(ws + WS_EPOCH), tag, __ATOMIC_RELAXED, __HIP_MEMORY_SCOPE_AGENT);
    }
    BAR();
    STAMP(5);
    dma_strided<3, true, 0x2000>(ws + WS_K, w * 1024 + l16, lb + L_K + w * 1024, 8192);
    dma_strided<2, true, 0x2000>(ws + WS_V, w * 1024 + l16, lb + L_V + w * 1024, 8192);
    if (tid < 272) ldsw<u32x4>(L_ONES + tid * 16, (u32x4){0x3F803F80u, 0x3F803F80u, 0x3F803F80u, 0x3F803F80u});
    if (tid == 511) ldsw<u32x2>(L_CNT, (u32x2){0u, 0u});
    WAITV(0);
    BAR();
    STAMP(6);
  }
  u32 opk[4];
  bf16x8 a_o[8], a_1[8], a_2[8], a3[32];
  u32x2 xres[2];
  {
    u32 ka = L_K + l16;
    const bool isv = r32 < 16;
    u32 va = isv ? L_V + h2 * 256 + r32 * 16 : L_ONES + ((r32 >= 20 && r32 < 28) ? 64 : 0);
    const u32 vinc = isv ? 4096u : 0u;
    f32x16 o = {};
    f32x16 negm;
    {
      const bf16x8 kf = ldsr<bf16x8>(ka);
      f32x16 z = {};
      const f32x16 s = mfma32(kf, qf, z);
      float m = fmaxf(fmaxf(s[0], s[1]), fmaxf(s[2], s[3]));
#pragma unroll
      for (int i = 4; i < 16; i += 2) m = fmaxf(m, fmaxf(s[i], s[i + 1]));
      const auto rr = __builtin_amdgcn_permlane32_swap(__float_as_uint(m), __float_as_uint(m), false, false);
      m = fmaxf(__uint_as_float(rr[0]), __uint_as_float(rr[1]));
#pragma unroll
      for (int i = 0; i < 16; ++i) negm[i] = -m;
    }
    f32x16 s = mfma32(ldsr<bf16x8>(ka), qf, negm);
    auto tiles4 = [&]() {
#pragma unroll
      for (int u = 0; u < 4; ++u) {
        const bf16x8 v0 = ldsr<bf16x8>(va + u * 1024), v1 = ldsr<bf16x8>(va + u * 1024 + 512);
        const bf16x8 kn = ldsr<bf16x8>(ka + (u + 1) * 1024);
        const f32x16 sn = mfma32(kn, qf, negm);
        u32 pk[8];
#pragma unroll
        for (int i = 0; i < 8; ++i) pk[i] = cvtpk(__builtin_amdgcn_exp2f(s[2 * i]), __builtin_amdgcn_exp2f(s[2 * i + 1]));
        o = mfma32(v0, asbf((u32x4){pk[0], pk[1], pk[2], pk[3]}), o);
        o = mfma32(v1, asbf((u32x4){pk[4], pk[5], pk[6], pk[7]}), o);
        s = sn;
      }
      va += vinc; ka += 4096;
    };
    if (w < 4) __builtin_amdgcn_s_setprio(2); else __builtin_amdgcn_s_setprio(1);
    auto kv_rest = [&](u32 img, u32 limg, int c0, auto n) {
      dma_strided<n.value, true, 4096>(ws + img + c0 * 8192, opaque<u32>((w & 3) * 1024 + l16), lb + limg + c0 * 8192 + (w & 3) * 1024, 4096);
    };
    using N2 = std::integral_constant<int, 2>; using N4 = std::integral_constant<int, 4>;
    auto lds_count = [&](u32 off) {
      u32 n;
      asm volatile("ds_add_rtn_u32 %0, %1, %2\n\ts_waitcnt lgkmcnt(0)" : "=v"(n) : "v"(lb + off), "v"(lane == 0 ? 1u : 0u) : "memory");
      return (u32)__builtin_amdgcn_readfirstlane(n);
    };
    const u32x4 s4 = {SENT, SENT, SENT, SENT};
    u32x4 fa, fb;
    auto fetch = [&](int p, u32 l) {
      const u32 fo = (2 * j * 32 + 4 * w) * 1024;
      if (p < 24) {
        const int t = p & 7;
        const bf16x8 v = asbf(ld16(wsr, l + (t & 3) * 1024, (p < 8 ? WS_WO : p < 16 ? WS_W1 : WS_W2) + fo + (t >> 2) * 32768));
        if (p < 8) a_o[t] = v; else if (p < 16) a_1[t] = v; else a_2[t] = v;
      } else if (p < 26) {
        xres[p - 24] = *(const GAS u32x2*)((const char*)x + (u32)((blk_of(p - 24) * 32 + (l >> 5)) * 1024 + (64 * j + 8 * w) * 2 + ((l >> 4) & 1) * 8));
      } else if (p < 26 + A3_N0) {
        a3[p - 26] = asbf(ld16(wsr, w < 7 ? ((7 * j + w) * 32) * 1024 + l : OOB, WS_W3 + (p - 26) * 1024));
      } else if (p < 33 + A3_N0) {
        dma1(ws + WS_W3 + (p - 26 - A3_N0) * 1024, (((u32)(b >> 6) & 3u) * 8 + w) * 7168 + j * 229376 + l, lb + L_DUM1);
      }
    };
    static_for<0, 16>([&](auto IT) {
      constexpr int it = IT.value;
      ASTAMP(it, 0);
      if (w < 4) {
        if (it == 0) {
          const u32 l = opaque<u32>(lane);
          const u32 sb = L_STG + w * 5120;
#pragma unroll
          for (int i = 0; i < 5; ++i) ldsw<u32x4>(sb + i * 1024 + l * 16, rts[i]);
          const u32 tb = sb + ((l & 15) >> 2) * 64 + ((l >> 4) & 1) * 32 + (l & 3) * 8;
#pragma unroll
          for (int i = 0; i < 5; ++i) {
            const bool perm = JOB(i).perm;
            const u32 t0a = tb + i * 1024 + (l >> 5) * (perm ? 256 : 512);
            st16(wsr, l * 16, WS_WO + (b * 20 + w * 5 + i) * 1024, asu(cat(ldstr(t0a), ldstr(t0a + (perm ? 512 : 256)))));
          }
        } else if (it == 1) {
          const u32 l = opaque<u32>(lane);
#pragma unroll
          for (int half = 0; half < 2; ++half) {
            const u32 po = blk_of(half) * 32768 + (4 * j + w) * 1024;
            st16(wsr, l * 16, WS_OX + po, s4); st16(wsr, l * 16, WS_HX + po, s4); st16(wsr, l * 16, WS_X1 + po, s4);
            const int R = blk_of(half) * 32 + (int)(l & 31) + dlt;
            const u32 mk = (u32)R < 2048u ? SENT : 0u;
            st16(wsr, ((R & 2047) >> 5) * 32768 + (l >> 5) * 512 + (R & 31) * 16, WS_TS + (4 * j + w) * 1024, (u32x4){mk, mk, mk, mk});
          }
        } else if (it == 2) {
          const u32 uo = WS_UX + (blk_of(w >> 1) * 112 + 14 * j + 7 * (w & 1)) * 1024;
          const u32 l = opaque<u32>(l16);
#pragma unroll
          for (int i = 0; i < 7; ++i) st16(wsr, l + (i & 3) * 1024, uo + (i >> 2) * 4096, s4);
        } else if (it == 5) {
          WAITV(0);
          if (lds_count(L_CNT + 4) == 3 && lane < 8) __hip_atomic_store((GAS u64*)(ws + WS_FW + lane * 2048 + b * 8), tag, __ATOMIC_RELAXED, __HIP_MEMORY_SCOPE_AGENT);
        }
      } else {
        if (it == 0) {
          kv_rest(WS_V, L_V, 2, N4{}); kv_rest(WS_K, L_K, 3, N4{}); kv_rest(WS_V, L_V, 4, N4{}); kv_rest(WS_K, L_K, 5, N4{});
          kv_rest(WS_V, L_V, 6, N4{}); kv_rest(WS_K, L_K, 7, N2{});
        } else if (it == 1) {
          WAITV(0);
          lds_count(L_CNT);
        }
      }
      if (it == 4) {
        while (__builtin_amdgcn_readfirstlane(*(volatile LAS u32*)((LAS char*)smem + L_CNT)) != 4) {
          if (timed_out(t0)) break;
          __builtin_amdgcn_s_sleep(1);
        }
        FENCE();
      }
      if (it == 7) {
        const u32 l = opaque<u32>(l16);
        fa = ld16(wsr, l * 2, WS_FW + (b & 7) * 2048); fb = ld16(wsr, l * 2 + 16, WS_FW + (b & 7) * 2048);
      }
      if (it == 8) {
        if (!flags_ok(fa, fb, taglo, taghi)) wait_flags(wsr, WS_FW + (b & 7) * 2048, taglo, taghi, lane, t0);
      }
      if (it >= 8) {
        const u32 l = opaque<u32>(l16);
#pragma unroll
        for (int p = 5 * (it - 8); p < 5 * (it - 7); ++p) fetch(p, l);
      }
      if (it == 12 && w < 4) __builtin_amdgcn_s_setprio(0);
      FENCE();
      ASTAMP(it, 1);
      tiles4();
      FENCE();
      if (it == 9) STAMP(7);
    });
    STAMP(8);
    __builtin_amdgcn_s_setprio(0);
    ka = L_K + l16;
    float l = o[8];
    if (!__all(l <= 1.8446744e19f)) {
      float m = -INFINITY;
      o = (f32x16){};
      u32 vb = isv ? L_V + h2 * 256 + r32 * 16 : L_ONES;
      for (int t = 0; t < 64; ++t) {
        f32x16 z = {};
        const f32x16 sx = mfma32(ldsr<bf16x8>(ka + t * 1024), qf, z);
        float mt = sx[0];
#pragma unroll
        for (int i = 1; i < 16; ++i) mt = fmaxf(mt, sx[i]);
        const auto rr = __builtin_amdgcn_permlane32_swap(__float_as_uint(mt), __float_as_uint(mt), false, false);
        mt = fmaxf(fmaxf(__uint_as_float(rr[0]), __uint_as_float(rr[1])), m);
        const float f = __builtin_amdgcn_exp2f(m - mt);
        m = mt;
        u32 pk[8];
#pragma unroll
        for (int i = 0; i < 8; ++i) pk[i] = cvtpk(__builtin_amdgcn_exp2f(sx[2 * i] - m), __builtin_amdgcn_exp2f(sx[2 * i + 1] - m));
#pragma unroll
        for (int i = 0; i < 16; ++i) o[i] *= f;
        o = mfma32(ldsr<bf16x8>(vb), asbf((u32x4){pk[0], pk[1], pk[2], pk[3]}), o);
        o = mfma32(ldsr<bf16x8>(vb + 512), asbf((u32x4){pk[4], pk[5], pk[6], pk[7]}), o);
        vb += isv ? 1024u : 0u;
      }
      MFMA_GAP32(o);
      l = o[8];
    }
    const float rl = __builtin_amdgcn_rcpf(l);
#pragma unroll
    for (int i = 0; i < 4; ++i) opk[i] = canon(cvtpk(o[2 * i] * rl, o[2 * i + 1] * rl));
  }
  st16(wsr, ox_off(), 0, (u32x4){opk[0], opk[1], opk[2], opk[3]});
  STAMP(9);
  bf16x8 sel[4];
  auto make_sel = [&]() {
    const u32 l = opaque<u32>(lane), e = (l & 1) ? 0x3F800000u : 0x00003F80u;
#pragma unroll
    for (int q = 0; q < 4; ++q) {
      const bool on = ((l >> 2) & 3) == q;
      const u32 d0 = (on && !(l & 2)) ? e : 0u, d1 = (on && (l & 2)) ? e : 0u;
      sel[q] = asbf((u32x4){d0, d1, d0, d1});
    }
  };
  make_sel();
  const u32 gvo = w * 4096 + l16;
  auto pw_of = [&](u32 l) { return L_P + ((l >> 3) & 3) * 1024 + (w >> 1) * 256 + (l & 7) * 32 + (l >> 5) * 16 + (((w ^ (l >> 3)) & 1) * 8); };
  const u32 pw = pw_of(lane);
  const u32 pr = L_P + gvo;
  auto g_load = [&](bf16x8 (&f)[4], u32 src) {
#pragma unroll
    for (int t = 0; t < 4; ++t) f[t] = asbf(ld16(wsr, gvo + t * 1024, src));
  };
  auto g_wait = [&](bf16x8 (&f)[4], u32 src) {
    while (unwritten(f)) {
      if (timed_out(t0)) break;
      __builtin_amdgcn_s_sleep(1);
      FENCE();
      g_load(f, src);
    }
  };
  auto row_stats = [&](int half, const bf16x8 (&f)[4]) {
    float s0 = 0.f, s1 = 0.f, q0 = 0.f, q1 = 0.f;
#pragma unroll
    for (int t = 0; t < 4; ++t) {
      const u32x4 d = asu(f[t]);
      s0 = dot2(d[0], 0x3F803F80u, s0); q0 = dot2(d[0], d[0], q0); s1 = dot2(d[1], 0x3F803F80u, s1); q1 = dot2(d[1], d[1], q1);
      s0 = dot2(d[2], 0x3F803F80u, s0); q0 = dot2(d[2], d[2], q0); s1 = dot2(d[3], 0x3F803F80u, s1); q1 = dot2(d[3], d[3], q1);
    }
    const f32x2 sq = {swap32_add(s0 + s1), swap32_add(q0 + q1)};
    if (lane < 32) ldsw<f32x2>(L_STAT + half * STH + lane * STP + w * 8, sq);
  };
  auto partials = [&](int half, f32x16& acc0, f32x16& acc1, u32 pw) {
#pragma unroll
    for (int a = 0; a < 4; ++a) {
      ldsw<u32x2>(pw + half * 32768 + a * 4096, (u32x2){cvtpk(acc0[4 * a], acc0[4 * a + 1]), cvtpk(acc0[4 * a + 2], acc0[4 * a + 3])});
      ldsw<u32x2>(pw + half * 32768 + (4 + a) * 4096, (u32x2){cvtpk(acc1[4 * a], acc1[4 * a + 1]), cvtpk(acc1[4 * a + 2], acc1[4 * a + 3])});
    }
  };
  auto slice_gemm = [&](int half, const bf16x8 (&a)[8], const bf16x8 (&f)[4]) {
    f32x16 acc0 = {}, acc1 = {};
#pragma unroll
    for (int t = 0; t < 4; ++t) { acc0 = mfma32(a[t], f[t], acc0); acc1 = mfma32(a[4 + t], f[t], acc1); }
    partials(half, acc0, acc1, pw);
  };
  auto reduce_at = [&](int half, u32 pr_) -> f32x4 {
    f32x4 acc = {0, 0, 0, 0};
#pragma unroll
    for (int q = 0; q < 4; ++q) acc = mfma16(sel[q], ldsr<bf16x8>(pr_ + half * 32768 + q * 1024), acc);
    return acc;
  };
  auto reduce = [&](int half) { return reduce_at(half, pr); };
  auto src_of = [&](u32 base, int half) { return base + blk_of(half) * 32768; };
  auto pack4 = [&](f32x4 v) { return (u32x2){canon(cvtpk(v[0], v[1])), canon(cvtpk(v[2], v[3]))}; };
  const u32 a3off = w < 7 ? ((7 * j + w) * 32) * 1024 + l16 : OOB;
  auto a3_load = [&](int t0_, int t1_) {
#pragma unroll
    for (int t = 0; t < 32; ++t) if (t >= t0_ && t < t1_) a3[t] = asbf(ld16(wsr, a3off, WS_W3 + t * 1024));
  };

  f32x4 hres[2], xp[2], cs1;
  bf16x8 fA[4], fB[4];
  auto phase = [&](const bf16x8 (&a)[8], u32 src, bool stats, auto&& first, auto&& epi, auto&& put, auto&& pre, auto&& slotA, auto&& slotB, int sbase) {
    PSTAMP(sbase, 0);
    g_wait(fA, src_of(src, 0));
    STAMP(sbase);
    PSTAMP(sbase, 1);
    if (stats) row_stats(0, fA);
    first();
    PSTAMP(sbase, 2);
    slice_gemm(0, a, fA);
    PSTAMP(sbase, 3);
    g_load(fB, src_of(src, 1));
    slotA();
    PSTAMP(sbase, 4);
    BAR();
    PSTAMP(sbase, 5);
    const f32x4 rA = reduce(0);
    const u32x2 vA = epi(0, rA);
    PSTAMP(sbase, 6);
    put(0, vA);
    PSTAMP(sbase, 7);
    g_wait(fB, src_of(src, 1));
    STAMP(sbase + 1);
    PSTAMP(sbase, 8);
    if (stats) row_stats(1, fB);
    PSTAMP(sbase, 9);
    slice_gemm(1, a, fB);
    PSTAMP(sbase, 10);
    slotB();
    PSTAMP(sbase, 11);
    BAR();
    PSTAMP(sbase, 12);
    const u32x2 vB = epi(1, reduce(1));
    PSTAMP(sbase, 13);
    pre();
    PSTAMP(sbase, 14);
    put(1, vB);
    PSTAMP(sbase, 15);
  };
  auto lane_stat = [&](int half) {
    const u32 sa = L_STAT + half * STH + (lane >> 1) * STP + (lane & 1) * 32;
    const f32x4 p0 = ldsr<f32x4>(sa), p1 = ldsr<f32x4>(sa + 16);
    return finish_stat(xor1_add((p0[0] + p0[2]) + (p1[0] + p1[2])), xor1_add((p0[1] + p0[3]) + (p1[1] + p1[3])));
  };
  const u32 a4off = (2 * j * 112 + 14 * w) * 1024 + l16;
  g_load(fA, src_of(WS_OX, 0));
  {
    float c[2] = {0.f, 0.f};
#pragma unroll
    for (int t = 0; t < 8; ++t) {
      const u32x4 d = asu(a_1[t]);
#pragma unroll
      for (int e = 0; e < 4; ++e) c[t >> 2] = dot2(d[e], 0x3F803F80u, c[t >> 2]);
    }
    const float c0 = swap32_add(c[0]), c1 = swap32_add(c[1]);
    ldsw<float>(L_CS1P + w * 256 + lane * 4, lane < 32 ? c0 : c1);
  }
  phase(a_o, WS_OX, false,
        [&]() { BAR(); },
        [&](int half, f32x4 acc) {
          if (half == 0) {
            cs1 = (f32x4){0.f, 0.f, 0.f, 0.f};
#pragma unroll
            for (int v = 0; v < 8; ++v) cs1 += ldsr<f32x4>(L_CS1P + v * 256 + w * 32 + (lane & 1) * 16);
          }
          hres[half] = acc + (f32x4){bflo(xres[half][0]), bfhi(xres[half][0]), bflo(xres[half][1]), bfhi(xres[half][1])};
          return pack4(hres[half]);
        },
        [&](int half, u32x2 v) { st8(wsr, hx_off(WS_HX, half), 0, v); },
        [&]() { g_load(fA, src_of(WS_HX, 0)); },
        [&]() {
#pragma unroll
          for (int i = 0; i < 7; ++i) dma1(ws + WS_W4 + i * 1024, (((u32)(b >> 6) & 3u) * 8 + w) * 7168 + j * 229376 + l16, lb + L_DUM1);
        },
        [&]() { a3_load(A3_N0, A3_N0 + 5); }, 10);
  STAMP(12);
  phase(a_1, WS_HX, true, [&]() { },
        [&](int half, f32x4 acc) {
          const RowStat st = lane_stat(half);
          const float r2 = __builtin_amdgcn_rsqf(st.var * st.rstd * st.rstd + LN_EPS), rr = st.rstd * r2;
          xp[half] = (hres[half] - st.mu) * st.rstd;
          const f32x4 pre = (acc - st.mu * cs1) * rr;
          return pack4((f32x4){gelu(pre[0]), gelu(pre[1]), gelu(pre[2]), gelu(pre[3])});
        },
        [&](int half, u32x2 v) { st8(wsr, ts_off(half), 0, v); },
        [&]() { g_load(fA, src_of(WS_TS, 0)); },
        [&]() { a3_load(A3_N0 + 5, A3_N0 + 10); },
        [&]() { a3_load(A3_N0 + 10, A3_N0 + 13); dma4_sc1(ws + WS_W4 + 10 * 1024, a4off, lb + L_A4 + w * 8192); }, 13);
  STAMP(15);
  phase(a_2, WS_TS, false, [&]() { },
        [&](int half, f32x4 acc) {
          const f32x4 x1 = xp[half] + acc;
          ldsw<f32x4>(L_RES + half * 8192 + w * 1024 + l16, hres[half] + x1);
          return pack4(x1);
        },
        [&](int half, u32x2 v) { st8(wsr, hx_off(WS_X1, half), 0, v); },
        [&]() { dma4_sc1(ws + src_of(WS_X1, 0), gvo, lb + L_P + w * 4096); },
        [&]() { a3_load(A3_N0 + 13, A3_N0 + 16); dma4_sc1(ws + WS_W4 + 114688 + 10 * 1024, a4off, lb + L_A4 + w * 8192 + 4096); },
        [&]() { a3_load(A3_N0 + 16, 32); }, 16);
  STAMP(18);
  bf16x8 a4[10][2];
  auto a4_load = [&](int n) { a4[n >> 1][n & 1] = asbf(ld16(wsr, a4off, WS_W4 + (n & 1) * 114688 + (n >> 1) * 1024)); };
  u32x4 uf[14];
  auto u_load1 = [&](int half, int t) { uf[t] = ld16(wsr, l16, WS_UX + (blk_of(half) * 112 + w * 14 + t) * 1024); };
  {
    auto x1_finish = [&](int half) {
      for (;;) {
#pragma unroll
        for (int t = 0; t < 4; ++t) fA[t] = ldsr<bf16x8>(pr + half * 32768 + t * 1024);
        if (!unwritten(fA)) break;
        if (timed_out(t0)) break;
        __builtin_amdgcn_s_sleep(1);
        dma4_sc1(ws + src_of(WS_X1, half), gvo, lb + L_P + half * 32768 + w * 4096);
        WAITV(0);
      }
      row_stats(half, fA);
    };
    struct UOut { float rstd, nm; f32x4 cs[2]; float gv[4]; u32 up[8]; u32 l; };
    auto u_stat = [&](UOut& u, int half) {
      u.l = opaque<u32>(lane);
      const u32 sr = L_STAT + half * STH + (u.l & 31) * STP;
      const f32x4 p0 = ldsr<f32x4>(sr), p1 = ldsr<f32x4>(sr + 16), p2 = ldsr<f32x4>(sr + 32), p3 = ldsr<f32x4>(sr + 48);
      const f32x4 p = (p0 + p1) + (p2 + p3);
      const RowStat st = finish_stat(p[0] + p[2], p[1] + p[3]);
      u.rstd = st.rstd; u.nm = -st.mu * st.rstd;
    };
    auto u_cs = [&](UOut& u, int a) { u.cs[a & 1] = ldsr<f32x4>(L_CS + (32 * w + 8 * a) * 4 + (u.l >> 5) * 16); };
    auto u_val = [&](UOut& u, int half, const f32x16& acc, int v) {
      const int a = v >> 2, e = v & 3;
      u.gv[e] = gelu(acc[v] * u.rstd + u.nm * u.cs[a & 1][e]);
      if (e == 3) { u.up[2 * a] = canon(cvtpk(u.gv[0], u.gv[1])); u.up[2 * a + 1] = canon(cvtpk(u.gv[2], u.gv[3])); }
      if (v == 7 || v == 15) {
        const u32 uo = w < 7 ? WS_UX + (blk_of(half) * 112 + j * 14 + 2 * w) * 1024 + u.l * 16 : OOB;
        if (v == 7) st16(wsr, uo, 0, (u32x4){u.up[0], u.up[1], u.up[2], u.up[3]});
        else st16(wsr, uo, 1024, (u32x4){u.up[4], u.up[5], u.up[6], u.up[7]});
      }
    };
    WAITV(1);
    x1_finish(0);
    BAR();
    STAMP(19);
    dma4_sc1(ws + src_of(WS_X1, 1), gvo, lb + L_P + 32768 + w * 4096);
    const u32 xb = L_P + (w < 7 ? l16 : 0u);
    f32x16 accA = {}, accB = {};
    bf16x8 bq[4];
    {
      float c0 = 0.f, c1 = 0.f;
#pragma unroll
      for (int t = 0; t < 4; ++t) bq[t] = ldsr<bf16x8>(xb + t * 1024);
#pragma unroll
      for (int t = 0; t < 32; ++t) {
        accA = mfma32(a3[t], bq[t & 3], accA);
        if (t + 4 < 32) bq[t & 3] = ldsr<bf16x8>(xb + (t + 4) * 1024);
        const u32x4 d = asu(a3[t]);
        c0 = dot2(d[0], 0x3F803F80u, c0); c1 = dot2(d[1], 0x3F803F80u, c1); c0 = dot2(d[2], 0x3F803F80u, c0); c1 = dot2(d[3], 0x3F803F80u, c1);
        if (t >= 4 && (t & 3) == 0 && t / 4 - 1 < A4_N0) a4_load(t / 4 - 1);
        __builtin_amdgcn_sched_barrier(0);
      }
      const float c = swap32_add(c0 + c1);
      if (lane < 32) ldsw<float>(L_CS + w * 128 + lane * 4, c);
    }
    STAMP(20);
    asm volatile("s_waitcnt vmcnt(%0)" :: "n"(A4_N0) : "memory");
    x1_finish(1);
    BAR();
    STAMP(21);
    {
      UOut u;
#pragma unroll
      for (int t = 0; t < 4; ++t) bq[t] = ldsr<bf16x8>(xb + 32768 + t * 1024);
      u_stat(u, 0); u_cs(u, 0);
#pragma unroll
      for (int t = 0; t < 32; ++t) {
        if (t < 16 && (t & 3) == 1 && t + 3 < 16) u_cs(u, (t + 3) >> 2);
        accB = mfma32(a3[t], bq[t & 3], accB);
        if (t + 4 < 32) bq[t & 3] = ldsr<bf16x8>(xb + 32768 + (t + 4) * 1024);
        if (t < 16) u_val(u, 0, accA, t);
        const int n = A4_N0 + t - 1;
        if (t >= 1 && n < 20) a4_load(n);
        __builtin_amdgcn_sched_barrier(0);
      }
    }
    STAMP(22);
    {
      UOut u;
      u_stat(u, 1); u_cs(u, 0);
#pragma unroll
      for (int v = 0; v < 16; ++v) {
        if ((v & 3) == 0 && v + 4 < 16) u_cs(u, (v + 4) >> 2);
        if (v < 14) u_load1(0, v);
        u_val(u, 1, accB, v);
        __builtin_amdgcn_sched_barrier(0);
      }
    }
    STAMP(23);
  }
  {
    auto u_wait = [&](int half) {
      for (;;) {
        u32 m = 0;
#pragma unroll
        for (int t = 0; t < 14; ++t) m = umax(m, max4(uf[t]));
        if (!__any(m == SENT)) break;
        if (timed_out(t0)) break;
        __builtin_amdgcn_s_sleep(2);
        FENCE();
#pragma unroll
        for (int t = 0; t < 14; ++t) u_load1(half, t);
      }
    };
#pragma unroll
    for (int half = 0; half < 2; ++half) {
      u_wait(half);
      STAMP(24 + half);
      const u32 sa = L_A4 + w * 8192 + opaque<u32>(l16);
      f32x16 acc0 = {}, acc1 = {};
      bf16x8 sq[4][2];
#pragma unroll
      for (int t = 0; t < 14; ++t) {
        if (t >= 8 && t < 12) { sq[t - 8][0] = ldsr<bf16x8>(sa + (t - 8) * 1024); sq[t - 8][1] = ldsr<bf16x8>(sa + 4096 + (t - 8) * 1024); }
        acc0 = mfma32(t < 10 ? a4[t < 10 ? t : 0][0] : sq[t < 10 ? 0 : t - 10][0], asbf(uf[t]), acc0);
        acc1 = mfma32(t < 10 ? a4[t < 10 ? t : 0][1] : sq[t < 10 ? 0 : t - 10][1], asbf(uf[t]), acc1);
        if (half == 0 && t >= 7) { u_load1(1, 2 * (t - 7)); u_load1(1, 2 * (t - 7) + 1); }
        __builtin_amdgcn_sched_barrier(0);
      }
      if (half == 0) BAR();
      partials(half, acc0, acc1, pw_of(opaque<u32>(lane)));
      FENCE();
    }
    STAMP(26);
    make_sel();
    BAR();
#pragma unroll
    for (int half = 0; half < 2; ++half) {
      const u32 l = opaque<u32>(lane);
      const f32x4 v = reduce_at(half, L_P + w * 4096 + l * 16) + ldsr<f32x4>(L_RES + half * 8192 + w * 1024 + l * 16);
      *(GAS u32x2*)((char*)out + (u32)((blk_of(half) * 32 + (l >> 1)) * 1024 + (64 * j + 8 * w + 4 * (l & 1)) * 2)) = (u32x2){cvtpk(v[0], v[1]), cvtpk(v[2], v[3])};
    }
  }
  STAMP(27);
}

extern "C" void kernel_launch(void* const* d_in, const int* in_sizes, int n_in, void* d_out, int out_size, void* d_ws, size_t ws_size, hipStream_t stream) {
  (void)in_sizes; (void)n_in; (void)out_size; (void)ws_size;
  fused_block_kernel<<<256, 512, 0, stream>>>((const u16*)d_in[0], (const u16*)d_in[1], (const u16*)d_in[2], (const u16*)d_in[3], (const u16*)d_in[4],
                                              (const u16*)d_in[6], (const u16*)d_in[7], (const u16*)d_in[8], (const u16*)d_in[9], (u16*)d_out, (char*)d_ws);
}
```

```cpp
#include <hip/hip_runtime.h>
#include <stdint.h>
#include <type_traits>

typedef unsigned short u16;
typedef unsigned int u32;
typedef unsigned long long u64;
typedef short bf16x8 __attribute__((ext_vector_type(8)));
typedef short s16x4 __attribute__((ext_vector_type(4)));
typedef float f32x2 __attribute__((ext_vector_type(2)));
typedef float f32x4 __attribute__((ext_vector_type(4)));
typedef float f32x16 __attribute__((ext_vector_type(16)));
typedef u32 u32x2 __attribute__((ext_vector_type(2)));
typedef u32 u32x4 __attribute__((ext_vector_type(4)));
typedef __bf16 bf16x2_t __attribute__((ext_vector_type(2)));
#define LAS __attribute__((address_space(3)))
#define GAS __attribute__((address_space(1)))
#define DI __device__ __forceinline__

constexpr u32 WS_EPOCH = 0x0;
constexpr u32 WS_FKV = 0x1000;
constexpr u32 WS_FW = 0x5000;
constexpr u32 WS_K = 0x10000;
constexpr u32 WS_V = 0x20000;
constexpr u32 WS_DBG = 0x1E00000;
constexpr u32 WS_WO = 0x100000, WS_W1 = 0x180000, WS_W2 = 0x200000, WS_W3 = 0x280000, WS_W4 = 0x440000;
constexpr u32 WS_OX = 0x600000, WS_HX = 0x800000, WS_TS = 0xA00000, WS_X1 = 0xC00000, WS_UX = 0xE00000;
constexpr u32 WS_STAMP = 0x2000000;
constexpr u32 WS_END = 0x2100000;
constexpr u32 OOB = 0x80000000u;

constexpr u32 XP = 1040;
constexpr u32 L_XIMG = 0;
constexpr u32 L_WQ = 66560;
constexpr u32 L_WKV = 132096;
constexpr u32 S_PM = 148480, S_PC = 156672, S_PS = 157184, S_PQ = 157696, S_FIN = 158208, S_QS = 158336, S_QC = 159360;
constexpr u32 L_DUM1 = 160384;
constexpr u32 L_CNT = 161408;
constexpr u32 L_K = 0, L_V = 65536, L_ONES = 131072;
constexpr u32 L_STG = 135424;
constexpr u32 L_CS1P = 155904;
constexpr u32 L_P = 0;
constexpr u32 L_STAT = 65536;
constexpr u32 STP = 80, STH = 2560;
constexpr u32 L_CS = 71680;
constexpr u32 L_RES = 73728;
constexpr u32 L_A4 = 90112;
constexpr u32 L_STAMP = 161792;
constexpr u32 LDS_BYTES = 163840;

__shared__ __attribute__((aligned(16))) char smem[LDS_BYTES];

constexpr u32 SENT = 0xFFFFFFFFu;
constexpr float LN_EPS = 1e-5f;
constexpr float QSCALE = 0.25f * 1.4426950408889634f;
#define CODE_KB 43

DI f32x16 mfma32(bf16x8 a, bf16x8 b, f32x16 c) { return __builtin_amdgcn_mfma_f32_32x32x16_bf16(a, b, c, 0, 0, 0); }
DI f32x4 mfma16(bf16x8 a, bf16x8 b, f32x4 c) { return __builtin_amdgcn_mfma_f32_16x16x32_bf16(a, b, c, 0, 0, 0); }
DI u32 cvtpk(float lo, float hi) { f32x2 v = {lo, hi}; bf16x2_t b = __builtin_convertvector(v, bf16x2_t); return __builtin_bit_cast(u32, b); }
DI u32 canon(u32 d) { return d < 0xFFFFFFFEu ? d : 0xFFFFFFFEu; }
DI float bflo(u32 d) { return __uint_as_float(d << 16); }
DI float bfhi(u32 d) { return __uint_as_float(d & 0xFFFF0000u); }
DI float dot2(u32 a, u32 b, float c) { return __builtin_amdgcn_fdot2_f32_bf16(__builtin_bit_cast(bf16x2_t, a), __builtin_bit_cast(bf16x2_t, b), c, false); }
DI float gelu(float x) {
  const float c1 = -2.0f * 0.7978845608028654f * 1.4426950408889634f, c2 = c1 * 0.044715f;
  const float e = __builtin_amdgcn_exp2f(x * (c1 + c2 * x * x));
  return x * __builtin_amdgcn_rcpf(1.0f + e);
}
DI float swap32_add(float v) {
  const auto r = __builtin_amdgcn_permlane32_swap(__float_as_uint(v), __float_as_uint(v), false, false);
  return __uint_as_float(r[0]) + __uint_as_float(r[1]);
}
DI float xor1_add(float v) { return v + __int_as_float(__builtin_amdgcn_mov_dpp(__float_as_int(v), 0xB1, 0xF, 0xF, true)); }

#define BAR() asm volatile("s_waitcnt lgkmcnt(0)\n\ts_barrier" ::: "memory")
#define WAITV(n) asm volatile("s_waitcnt vmcnt(" #n ")" ::: "memory")
#define FENCE() asm volatile("" ::: "memory")
template <class T> DI T opaque(T v) { asm volatile("" : "+v"(v)); return v; }

template <class T> DI T ldsr(u32 off) { return *(const LAS T*)((LAS char*)smem + off); }
template <class T> DI void ldsw(u32 off, T v) { *(LAS T*)((LAS char*)smem + off) = v; }
DI s16x4 ldstr(u32 off) {
  typedef short v4i16_t __attribute__((ext_vector_type(4)));
  return __builtin_bit_cast(s16x4, __builtin_amdgcn_ds_read_tr16_b64_v4i16((LAS v4i16_t*)((LAS char*)smem + off)));
}
DI bf16x8 cat(s16x4 a, s16x4 b) { return __builtin_shufflevector(a, b, 0, 1, 2, 3, 4, 5, 6, 7); }
DI u32 lds_base() { return (u32)(uintptr_t)(LAS char*)smem; }

#define DMA_HEAD "s_mov_b32 %[k], m0\n\ts_mov_b32 m0, %[lds]\n\ts_nop 0\n\t"
#define DMA_TAIL "s_mov_b32 m0, %[k]"
DI void dma1(const void* sbase, u32 voff, u32 ldsdst) {
  u32 keep;
  asm volatile(DMA_HEAD "global_load_lds_dwordx4 %[v], %[s]\n\t" DMA_TAIL : [k] "=&s"(keep) : [v] "v"(voff), [s] "s"(sbase), [lds] "s"(ldsdst) : "memory");
}
DI void dma2(const void* sbase, u32 voff, u32 ldsdst) {
  u32 keep;
  asm volatile(DMA_HEAD "global_load_lds_dwordx4 %[v], %[s]\n\tglobal_load_lds_dwordx4 %[v], %[s] offset:1024\n\t" DMA_TAIL
               : [k] "=&s"(keep) : [v] "v"(voff), [s] "s"(sbase), [lds] "s"(ldsdst) : "memory");
}
DI void dma4_rows(const void* sbase, u32 v0, u32 v1, u32 v2, u32 v3, u32 ldsdst) {
  u32 keep;
  asm volatile(DMA_HEAD "global_load_lds_dwordx4 %[a], %[s]\n\tglobal_load_lds_dwordx4 %[b], %[s] offset:1040\n\t"
               "global_load_lds_dwordx4 %[c], %[s] offset:2080\n\tglobal_load_lds_dwordx4 %[d], %[s] offset:3120\n\t" DMA_TAIL
               : [k] "=&s"(keep) : [a] "v"(v0), [b] "v"(v1), [c] "v"(v2), [d] "v"(v3), [s] "s"(sbase), [lds] "s"(ldsdst) : "memory");
}
DI void dma4_sc1(const void* sbase, u32 voff, u32 ldsdst) {
  u32 keep;
  asm volatile(DMA_HEAD "global_load_lds_dwordx4 %[v], %[s] sc1\n\tglobal_load_lds_dwordx4 %[v], %[s] offset:1024 sc1\n\t"
               "global_load_lds_dwordx4 %[v], %[s] offset:2048 sc1\n\tglobal_load_lds_dwordx4 %[v], %[s] offset:3072 sc1\n\t" DMA_TAIL
               : [k] "=&s"(keep) : [v] "v"(voff), [s] "s"(sbase), [lds] "s"(ldsdst) : "memory");
}
#define DMA_STEP(SC) "global_load_lds_dwordx4 %[v], %[s]" SC "\n\ts_add_u32 m0, m0, %[ls]\n\tv_add_u32 %[v], %[gs], %[v]\n\t"
template <int N, bool SC1, u32 GS>
DI void dma_strided(const void* sbase, u32 voff, u32 ldsdst, u32 lstep) {
  u32 keep;
  static_assert(N == 2 || N == 3 || N == 4 || N == 8, "");
#define DMA_RUN(BODY) asm volatile(DMA_HEAD BODY DMA_TAIL : [k] "=&s"(keep), [v] "+v"(voff) : [s] "s"(sbase), [lds] "s"(ldsdst), [ls] "s"(lstep), [gs] "n"(GS) : "memory", "scc")
  if constexpr (SC1) {
    if constexpr (N == 2) DMA_RUN(DMA_STEP(" sc1") DMA_STEP(" sc1"));
    if constexpr (N == 3) DMA_RUN(DMA_STEP(" sc1") DMA_STEP(" sc1") DMA_STEP(" sc1"));
    if constexpr (N == 4) DMA_RUN(DMA_STEP(" sc1") DMA_STEP(" sc1") DMA_STEP(" sc1") DMA_STEP(" sc1"));
  } else {
    if constexpr (N == 8) DMA_RUN(DMA_STEP("") DMA_STEP("") DMA_STEP("") DMA_STEP("") DMA_STEP("") DMA_STEP("") DMA_STEP("") DMA_STEP(""));
  }
#undef DMA_RUN
}

typedef __amdgpu_buffer_rsrc_t rsrc_t;
DI rsrc_t mkrsrc(const void* p, u32 bytes) { return __builtin_amdgcn_make_buffer_rsrc((void*)p, 0, bytes, 0x00020000); }
DI u32x4 ld16(rsrc_t r, u32 voff, u32 soff) { return __builtin_amdgcn_raw_buffer_load_b128(r, voff, soff, 16); }
DI void st16(rsrc_t r, u32 voff, u32 soff, u32x4 v) { __builtin_amdgcn_raw_buffer_store_b128(v, r, voff, soff, 16); }
DI void st8(rsrc_t r, u32 voff, u32 soff, u32x2 v) { __builtin_amdgcn_raw_buffer_store_b64(v, r, voff, soff, 16); }
DI void st4(rsrc_t r, u32 voff, u32 soff, u32 v) { __builtin_amdgcn_raw_buffer_store_b32(v, r, voff, soff, 16); }
DI bf16x8 asbf(u32x4 v) { return __builtin_bit_cast(bf16x8, v); }
DI u32x4 asu(bf16x8 v) { return __builtin_bit_cast(u32x4, v); }
DI u32 umax(u32 a, u32 b) { return a > b ? a : b; }
DI u32 max4(u32x4 v) { return umax(umax(v[0], v[1]), umax(v[2], v[3])); }

constexpr u64 SPIN_TICKS = 400000ull;
DI bool timed_out(u64 t0) { return __builtin_amdgcn_s_memrealtime() - t0 > SPIN_TICKS; }

#define STAMP(i) do { } while (0)
#define ASTAMP(it, pos) do { } while (0)
#define PSTAMP(sb, i) do { } while (0)
#define MFMA_GAP16(v) asm volatile("s_nop 7" : "+v"(v))
#define MFMA_GAP32(v) asm volatile("s_nop 7\n\ts_nop 3" : "+v"(v))

DI bool flags_ok(u32x4 a, u32x4 b, u32 lo, u32 hi) {
  return __all(a[0] == lo && a[1] == hi && a[2] == lo && a[3] == hi && b[0] == lo && b[1] == hi && b[2] == lo && b[3] == hi);
}
DI void wait_flags(rsrc_t wsr, u32 base, u32 taglo, u32 taghi, int lane, u64 t0) {
  for (;;) {
    const u32x4 a = ld16(wsr, lane * 32, base), b = ld16(wsr, lane * 32 + 16, base);
    if (flags_ok(a, b, taglo, taghi)) break;
    if (timed_out(t0)) break;
    __builtin_amdgcn_s_sleep(1);
    FENCE();
  }
}

DI bf16x8 ones_frag() { return asbf((u32x4){0x3F803F80u, 0x3F803F80u, 0x3F803F80u, 0x3F803F80u}); }
DI float diag_pick(f32x4 g, int lane) { const int r = lane & 3; float a = (r & 1) ? g[1] : g[0], b = (r & 1) ? g[3] : g[2]; return (r & 2) ? b : a; }

struct RowStat { float mu, rstd, var; };
DI RowStat finish_stat(float s, float q) {
  RowStat r; r.mu = s * (1.0f / 512.0f);
  r.var = fmaxf(q * (1.0f / 512.0f) - r.mu * r.mu, 0.0f);
  r.rstd = __builtin_amdgcn_rsqf(r.var + LN_EPS);
  return r;
}
DI bool unwritten(const bf16x8 (&f)[4]) {
  return __any(umax(umax(max4(asu(f[0])), max4(asu(f[1]))), umax(max4(asu(f[2])), max4(asu(f[3])))) == SENT);
}

template <int I, int N, class F> DI void static_for(F&& f) {
  if constexpr (I < N) { f(std::integral_constant<int, I>{}); static_for<I + 1, N>(f); }
}
struct Job { const char* src; u32 prow; bool perm; };
DI Job retile_job(u32 f, u64 wo, u64 w1, u64 w2, u64 w3, u64 w4) {
  const bool big = f >= 1536, last = f >= 3328;
  const u32 g = f - 3328, T = last ? g / 112 : big ? (f - 1536) >> 5 : (f >> 5) & 15, ks = last ? g % 112 : f & 31;
  const u32 prow = big && !last ? 3584 : 1024;
  u64 m = f < 1024 ? w1 : w2;
  m = f < 512 ? wo : m; m = big ? w3 : m; m = last ? w4 : m;
  return Job{(const char*)(m + (u64)(16 * ks * prow + T * 64)), prow, last || f < 512};
}

#define A3_N0 6
#define A4_N0 6

__global__ __launch_bounds__(512, 2) void fused_block_kernel(const u16* __restrict__ x, const u16* __restrict__ wq, const u16* __restrict__ wk,
                                                             const u16* __restrict__ wv, const u16* __restrict__ wo, const u16* __restrict__ w1,
                                                             const u16* __restrict__ w2, const u16* __restrict__ w3, const u16* __restrict__ w4,
                                                             u16* __restrict__ out, char* ws) {
  const int tid = threadIdx.x, lane = tid & 63, w = __builtin_amdgcn_readfirstlane(tid >> 6);
  const int b = blockIdx.x;
  const int cl = (b & 7) * 4 + (b >> 6), j = (b >> 3) & 7;
  const u32 lb = lds_base();
  const u32 l16 = lane * 16;
  {
    u64 pc;
    asm volatile("s_getpc_b64 %0" : "=s"(pc));
    dma1((const void*)(pc & ~63ull), (u32)((((b >> 3) * 8 + w) % CODE_KB) * 1024) + l16, lb + L_DUM1);
  }
  asm volatile("" :: "s"(x), "s"(wq), "s"(wk), "s"(wv), "s"(wo), "s"(w1), "s"(w2), "s"(w3), "s"(w4), "s"(out), "s"(ws));
  const u64 t0 = __builtin_amdgcn_s_memrealtime();
  const int i16 = lane & 15, g4 = lane >> 4, r32 = lane & 31, h2 = lane >> 5;
  STAMP(0);

  const int kvrt = j >> 1, kvmat = j & 1;
  auto grow = [&](int rho) { return (rho < 32 ? 0 : 1024 - 32) + 32 * cl + rho; };
  const int R0 = grow(16 * kvrt);
  u32x4 xk0, xk1;
  u64 tag;
  {
    const u32 xo = (u32)(i16 * 1024 + g4 * 32);
    asm volatile("global_load_dwordx4 %0, %2, %3\n\tglobal_load_dwordx4 %1, %2, %3 offset:16"
                 : "=&v"(xk0), "=&v"(xk1) : "v"(xo), "s"((const char*)x + R0 * 1024 + w * 128) : "memory");
    dma2((const char*)(kvmat ? wv : wk) + w * 2048, l16, lb + L_WKV + w * 2048);
    asm volatile("s_load_dwordx2 %0, %1, 0x0" : "=s"(tag) : "s"(ws + WS_EPOCH) : "memory");
    const char* xr = (const char*)x + grow(8 * w) * 1024 - 48;
    dma4_rows(xr, l16 + 48, l16 + 32, l16 + 16, l16, lb + L_XIMG + 8 * w * XP);
    dma4_rows(xr + 4096, l16 + 48, l16 + 32, l16 + 16, l16, lb + L_XIMG + (8 * w + 4) * XP);
    dma_strided<8, false, 0x2000>((const char*)wq + w * 65536 + j * 128, (u32)((lane >> 3) * 1024 + (((lane & 7) ^ (4 * ((lane >> 4) & 1))) * 16)), lb + L_WQ + w * 8192, 1024);
    asm volatile("s_waitcnt vmcnt(16) lgkmcnt(0)" : "+v"(xk0), "+v"(xk1), "+s"(tag) :: "memory");
  }
  tag += 1ull;
  const u32 taglo = (u32)tag, taghi = (u32)(tag >> 32);
  const rsrc_t wsr = mkrsrc(ws, WS_END);
  const bf16x8 one = ones_frag();
  STAMP(1);

  {
    f32x4 am = {0, 0, 0, 0}, ac = {0, 0, 0, 0}, as = {0, 0, 0, 0}, aq = {0, 0, 0, 0};
    const u32 wa = L_WKV + w * 2048 + (16 * g4 + (i16 >> 2)) * 32 + (i16 & 3) * 8;
    const bf16x8 wf0 = cat(ldstr(wa), ldstr(wa + 128)), wf1 = cat(ldstr(wa + 256), ldstr(wa + 384));
    const bf16x8 xf0 = asbf(xk0), xf1 = asbf(xk1);
    as = mfma16(one, xf0, as); aq = mfma16(xf0, xf0, aq); as = mfma16(one, xf1, as); aq = mfma16(xf1, xf1, aq);
    if (kvmat) {
      am = mfma16(xf0, wf0, am); ac = mfma16(one, wf0, ac); am = mfma16(xf1, wf1, am); ac = mfma16(one, wf1, ac);
      MFMA_GAP16(am); MFMA_GAP16(ac);
      if (lane < 16) ldsw<float>(S_PC + (w * 16 + lane) * 4, ac[0]);
    } else {
      am = mfma16(wf0, xf0, am); ac = mfma16(wf0, one, ac); am = mfma16(wf1, xf1, am); ac = mfma16(wf1, one, ac);
      MFMA_GAP16(am); MFMA_GAP16(ac);
      if (i16 == 0) ldsw<f32x4>(S_PC + (w * 16 + 4 * g4) * 4, ac);
    }
    ldsw<f32x4>(S_PM + w * 1024 + l16, am);
    if (lane < 16) ldsw<float>(S_PS + (lane * 8 + w) * 4, as[0]);
    if ((i16 >> 2) == g4) ldsw<float>(S_PQ + (i16 * 8 + w) * 4, diag_pick(aq, lane));
    BAR();
  }
  if (w == 0) {
    {
      const f32x4 s0 = ldsr<f32x4>(S_PS + i16 * 32), s1 = ldsr<f32x4>(S_PS + i16 * 32 + 16);
      const f32x4 q0 = ldsr<f32x4>(S_PQ + i16 * 32), q1 = ldsr<f32x4>(S_PQ + i16 * 32 + 16);
      const f32x4 s = s0 + s1, q = q0 + q1;
      const RowStat st = finish_stat((s[0] + s[1]) + (s[2] + s[3]), (q[0] + q[1]) + (q[2] + q[3]));
      if (lane < 16) { ldsw<float>(S_FIN + lane * 4, st.mu); ldsw<float>(S_FIN + 64 + lane * 4, st.rstd); }
    }
    f32x4 m = {0, 0, 0, 0};
#pragma unroll
    for (int ww = 0; ww < 8; ++ww) m += ldsr<f32x4>(S_PM + ww * 1024 + l16);
    f32x4 v;
    u32 off;
    if (kvmat) {
      float c1 = 0.f;
#pragma unroll
      for (int ww = 0; ww < 8; ++ww) c1 += ldsr<float>(S_PC + (ww * 16 + i16) * 4);
      asm volatile("s_waitcnt lgkmcnt(0)" ::: "memory");
      const f32x4 mu = ldsr<f32x4>(S_FIN + g4 * 16), rs = ldsr<f32x4>(S_FIN + 64 + g4 * 16);
      v = rs * (m - mu * c1);
      off = WS_V + (R0 >> 5) * 1024 + ((R0 >> 4) & 1) * 512 + (g4 & 1) * 256 + i16 * 16 + (g4 >> 1) * 8;
    } else {
      f32x4 c4 = {0, 0, 0, 0};
#pragma unroll
      for (int ww = 0; ww < 8; ++ww) c4 += ldsr<f32x4>(S_PC + (ww * 16 + 4 * g4) * 4);
      asm volatile("s_waitcnt lgkmcnt(0)" ::: "memory");
      const float mu = ldsr<float>(S_FIN + i16 * 4), rs = ldsr<float>(S_FIN + 64 + i16 * 4);
      v = rs * (m - mu * c4);
      off = WS_K + (R0 >> 5) * 1024 + (g4 & 1) * 512 + ((R0 & 31) + i16) * 16 + (g4 >> 1) * 8;
    }
    st8(wsr, off, 0, (u32x2){cvtpk(v[0], v[1]), cvtpk(v[2], v[3])});
    WAITV(1);
  } else {
    WAITV(0);
  }
#define JOB(i) retile_job((u32)(b * 20 + w * 5 + (i)), (u64)wo, (u64)w1, (u64)w2, (u64)w3, (u64)w4)
  u32x4 rts[5];
  if (w < 4) {
#pragma unroll
    for (int i = 0; i < 5; ++i) {
      const Job jb = JOB(i);
      rts[i] = *(const GAS u32x4*)(jb.src + (u32)((lane >> 2) * jb.prow + (lane & 3) * 16));
    }
  }
  FENCE();
  STAMP(2);
  const int q_hp = w & 1, q_rt = (w >> 1) & 1, q_kh = w >> 2;
  const int head = 2 * q_hp + q_kh;
  bf16x8 qf;
  {
    BAR();
    STAMP(3);
    {
      f32x4 as = {0, 0, 0, 0}, aq = {0, 0, 0, 0};
      const int rt = w & 3, kh = w >> 2;
      const u32 sa = L_XIMG + (16 * rt + i16) * XP + kh * 512 + g4 * 16;
#pragma unroll
      for (int t = 0; t < 8; ++t) {
        const bf16x8 xf = ldsr<bf16x8>(sa + t * 64);
        as = mfma16(one, xf, as); aq = mfma16(xf, xf, aq);
      }
      MFMA_GAP16(as); MFMA_GAP16(aq);
      if (lane < 16) ldsw<float>(S_QS + ((16 * rt + lane) * 2 + kh) * 4, as[0]);
      if ((i16 >> 2) == g4) ldsw<float>(S_QS + 512 + ((16 * rt + i16) * 2 + kh) * 4, diag_pick(aq, lane));
    }
    f32x16 acc = {}, accc = {};
    const int q = i16 >> 2, p = i16 & 3, cb = g4 & 1;
    const u32 wa = L_WQ + (256 * q_kh + 8 * h2 + q) * 128 + (((4 * q_hp + 2 * cb + (p >> 1)) ^ (4 * ((q >> 1) & 1))) * 16) + (p & 1) * 8;
    const u32 xa = L_XIMG + (32 * q_rt + r32) * XP + q_kh * 512 + h2 * 16;
#pragma unroll
    for (int t = 0; t < 16; ++t) {
      const bf16x8 af = cat(ldstr(wa + t * 2048), ldstr(wa + t * 2048 + 512));
      acc = mfma32(af, ldsr<bf16x8>(xa + t * 32), acc);
    }
    const u32 wb = wa + q_rt * 16384;
#pragma unroll
    for (int t = 0; t < 8; ++t) accc = mfma32(cat(ldstr(wb + t * 2048), ldstr(wb + t * 2048 + 512)), one, accc);
    if (w == 0) {
      WAITV(5);
      if (lane < 8) __hip_atomic_store((GAS u64*)(ws + WS_FKV + lane * 2048 + b * 8), tag, __ATOMIC_RELAXED, __HIP_MEMORY_SCOPE_AGENT);
    }
    MFMA_GAP32(acc); MFMA_GAP32(accc);
    f32x4 k0, k1, s0, s1;
    if (q_kh) {
      k0 = (f32x4){acc[8], acc[9], acc[10], acc[11]}; k1 = (f32x4){acc[12], acc[13], acc[14], acc[15]};
      s0 = (f32x4){acc[0], acc[1], acc[2], acc[3]}; s1 = (f32x4){acc[4], acc[5], acc[6], acc[7]};
    } else {
      k0 = (f32x4){acc[0], acc[1], acc[2], acc[3]}; k1 = (f32x4){acc[4], acc[5], acc[6], acc[7]};
      s0 = (f32x4){acc[8], acc[9], acc[10], acc[11]}; s1 = (f32x4){acc[12], acc[13], acc[14], acc[15]};
    }
    ldsw<f32x4>(L_WKV + w * 2048 + l16, s0); ldsw<f32x4>(L_WKV + w * 2048 + 1024 + l16, s1);
    if (r32 == 0) {
#pragma unroll
      for (int a = 0; a < 4; ++a) ldsw<f32x4>(S_QC + (((q_hp * 2 + q_rt) * 2 + q_kh) * 32 + 8 * a + 4 * h2) * 4, (f32x4){accc[4 * a], accc[4 * a + 1], accc[4 * a + 2], accc[4 * a + 3]});
    }
    BAR();
    k0 += ldsr<f32x4>(L_WKV + (w ^ 4) * 2048 + l16); k1 += ldsr<f32x4>(L_WKV + (w ^ 4) * 2048 + 1024 + l16);
    f32x4 c0 = {0, 0, 0, 0}, c1 = {0, 0, 0, 0};
#pragma unroll
    for (int s = 0; s < 4; ++s) {
      c0 += ldsr<f32x4>(S_QC + ((q_hp * 4 + s) * 32 + 16 * q_kh + 4 * h2) * 4);
      c1 += ldsr<f32x4>(S_QC + ((q_hp * 4 + s) * 32 + 16 * q_kh + 8 + 4 * h2) * 4);
    }
    const f32x2 sp = ldsr<f32x2>(S_QS + (32 * q_rt + r32) * 8), qp = ldsr<f32x2>(S_QS + 512 + (32 * q_rt + r32) * 8);
    const RowStat st = finish_stat(sp[0] + sp[1], qp[0] + qp[1]);
    const float sc = st.rstd * QSCALE;
    k0 = (k0 - st.mu * c0) * sc; k1 = (k1 - st.mu * c1) * sc;
    qf = opaque(asbf((u32x4){cvtpk(k0[0], k0[1]), cvtpk(k0[2], k0[3]), cvtpk(k1[0], k1[1]), cvtpk(k1[2], k1[3])}));
  }
  STAMP(4);
  auto blk_of = [&](int half) { return half * 32 + cl; };
  const int dlt = (j >> 1) == 0 ? 1 : (j >> 1) == 1 ? -1 : (j >> 1) == 2 ? 2 : -2;
  const u32 pc_in = (u32)((4 * j + (w >> 1)) * 1024 + (w & 1) * 512);
  auto hx_off = [&](u32 base, int half) { return base + blk_of(half) * 32768 + pc_in + lane * 8; };
  auto ts_off = [&](int half) {
    const u32 l = opaque<u32>(lane);
    const u32 R = (u32)(blk_of(half) * 32 + (int)(l >> 1) + dlt);
    return R < 2048u ? WS_TS + (R >> 5) * 32768 + pc_in + (R & 31) * 16 + (l & 1) * 8 : OOB;
  };
  auto ox_off = [&]() { return WS_OX + blk_of(q_rt) * 32768 + (4 * j + head) * 1024 + l16; };
  {
    if (w == 1) {
      wait_flags(wsr, WS_FKV + (b & 7) * 2048, taglo, taghi, lane, t0);
      if (b == 0 && lane == 0) __hip_atomic_store((GAS u64*)(ws + WS_EPOCH), tag, __ATOMIC_RELAXED, __HIP_MEMORY_SCOPE_AGENT);
    }
    BAR();
    STAMP(5);
    dma_strided<3, true, 0x2000>(ws + WS_K, w * 1024 + l16, lb + L_K + w * 1024, 8192);
    dma_strided<2, true, 0x2000>(ws + WS_V, w * 1024 + l16, lb + L_V + w * 1024, 8192);
    if (tid < 272) ldsw<u32x4>(L_ONES + tid * 16, (u32x4){0x3F803F80u, 0x3F803F80u, 0x3F803F80u, 0x3F803F80u});
    if (tid == 511) ldsw<u32x2>(L_CNT, (u32x2){0u, 0u});
    WAITV(0);
    BAR();
    STAMP(6);
  }
  u32 opk[4];
  bf16x8 a_o[8], a_1[8], a_2[8], a3[32];
  u32x2 xres[2];
  {
    u32 ka = L_K + l16;
    const bool isv = r32 < 16;
    u32 va = isv ? L_V + h2 * 256 + r32 * 16 : L_ONES + ((r32 >= 20 && r32 < 28) ? 64 : 0);
    const u32 vinc = isv ? 4096u : 0u;
    f32x16 o = {};
    f32x16 negm;
    {
      const bf16x8 kf = ldsr<bf16x8>(ka);
      f32x16 z = {};
      const f32x16 s = mfma32(kf, qf, z);
      float m = fmaxf(fmaxf(s[0], s[1]), fmaxf(s[2], s[3]));
#pragma unroll
      for (int i = 4; i < 16; i += 2) m = fmaxf(m, fmaxf(s[i], s[i + 1]));
      const auto rr = __builtin_amdgcn_permlane32_swap(__float_as_uint(m), __float_as_uint(m), false, false);
      m = fmaxf(__uint_as_float(rr[0]), __uint_as_float(rr[1]));
#pragma unroll
      for (int i = 0; i < 16; ++i) negm[i] = -m;
    }
    f32x16 s = mfma32(ldsr<bf16x8>(ka), qf, negm);
    auto tiles4 = [&]() {
#pragma unroll
      for (int u = 0; u < 4; ++u) {
        const bf16x8 v0 = ldsr<bf16x8>(va + u * 1024), v1 = ldsr<bf16x8>(va + u * 1024 + 512);
        const bf16x8 kn = ldsr<bf16x8>(ka + (u + 1) * 1024);
        const f32x16 sn = mfma32(kn, qf, negm);
        u32 pk[8];
#pragma unroll
        for (int i = 0; i < 8; ++i) pk[i] = cvtpk(__builtin_amdgcn_exp2f(s[2 * i]), __builtin_amdgcn_exp2f(s[2 * i + 1]));
        o = mfma32(v0, asbf((u32x4){pk[0], pk[1], pk[2], pk[3]}), o);
        o = mfma32(v1, asbf((u32x4){pk[4], pk[5], pk[6], pk[7]}), o);
        s = sn;
      }
      va += vinc; ka += 4096;
    };
    if (w < 4) __builtin_amdgcn_s_setprio(2); else __builtin_amdgcn_s_setprio(1);
    auto kv_rest = [&](u32 img, u32 limg, int c0, auto n) {
      dma_strided<n.value, true, 4096>(ws + img + c0 * 8192, opaque<u32>((w & 3) * 1024 + l16), lb + limg + c0 * 8192 + (w & 3) * 1024, 4096);
    };
    using N2 = std::integral_constant<int, 2>; using N4 = std::integral_constant<int, 4>;
    auto lds_count = [&](u32 off) {
      u32 n;
      asm volatile("ds_add_rtn_u32 %0, %1, %2\n\ts_waitcnt lgkmcnt(0)" : "=v"(n) : "v"(lb + off), "v"(lane == 0 ? 1u : 0u) : "memory");
      return (u32)__builtin_amdgcn_readfirstlane(n);
    };
    const u32x4 s4 = {SENT, SENT, SENT, SENT};
    u32x4 fa, fb;
    auto fetch = [&](int p, u32 l) {
      const u32 fo = (2 * j * 32 + 4 * w) * 1024;
      if (p < 24) {
        const int t = p & 7;
        const bf16x8 v = asbf(ld16(wsr, l + (t & 3) * 1024, (p < 8 ? WS_WO : p < 16 ? WS_W1 : WS_W2) + fo + (t >> 2) * 32768));
        if (p < 8) a_o[t] = v; else if (p < 16) a_1[t] = v; else a_2[t] = v;
      } else if (p < 26) {
        xres[p - 24] = *(const GAS u32x2*)((const char*)x + (u32)((blk_of(p - 24) * 32 + (l >> 5)) * 1024 + (64 * j + 8 * w) * 2 + ((l >> 4) & 1) * 8));
      } else if (p < 26 + A3_N0) {
        a3[p - 26] = asbf(ld16(wsr, w < 7 ? ((7 * j + w) * 32) * 1024 + l : OOB, WS_W3 + (p - 26) * 1024));
      } else if (p < 33 + A3_N0) {
        dma1(ws + WS_W3 + (p - 26 - A3_N0) * 1024, (((u32)(b >> 6) & 3u) * 8 + w) * 7168 + j * 229376 + l, lb + L_DUM1);
      }
    };
    static_for<0, 16>([&](auto IT) {
      constexpr int it = IT.value;
      ASTAMP(it, 0);
      if (w < 4) {
        if (it == 0) {
          const u32 l = opaque<u32>(lane);
          const u32 sb = L_STG + w * 5120;
#pragma unroll
          for (int i = 0; i < 5; ++i) ldsw<u32x4>(sb + i * 1024 + l * 16, rts[i]);
          const u32 tb = sb + ((l & 15) >> 2) * 64 + ((l >> 4) & 1) * 32 + (l & 3) * 8;
#pragma unroll
          for (int i = 0; i < 5; ++i) {
            const bool perm = JOB(i).perm;
            const u32 t0a = tb + i * 1024 + (l >> 5) * (perm ? 256 : 512);
            st16(wsr, l * 16, WS_WO + (b * 20 + w * 5 + i) * 1024, asu(cat(ldstr(t0a), ldstr(t0a + (perm ? 512 : 256)))));
          }
        } else if (it == 1) {
          const u32 l = opaque<u32>(lane);
#pragma unroll
          for (int half = 0; half < 2; ++half) {
            const u32 po = blk_of(half) * 32768 + (4 * j + w) * 1024;
            st16(wsr, l * 16, WS_OX + po, s4); st16(wsr, l * 16, WS_HX + po, s4); st16(wsr, l * 16, WS_X1 + po, s4);
            const int R = blk_of(half) * 32 + (int)(l & 31) + dlt;
            const u32 mk = (u32)R < 2048u ? SENT : 0u;
            st16(wsr, ((R & 2047) >> 5) * 32768 + (l >> 5) * 512 + (R & 31) * 16, WS_TS + (4 * j + w) * 1024, (u32x4){mk, mk, mk, mk});
          }
        } else if (it == 2) {
          const u32 uo = WS_UX + (blk_of(w >> 1) * 112 + 14 * j + 7 * (w & 1)) * 1024;
          const u32 l = opaque<u32>(l16);
#pragma unroll
          for (int i = 0; i < 7; ++i) st16(wsr, l + (i & 3) * 1024, uo + (i >> 2) * 4096, s4);
        } else if (it == 5) {
          WAITV(0);
          if (lds_count(L_CNT + 4) == 3 && lane < 8) __hip_atomic_store((GAS u64*)(ws + WS_FW + lane * 2048 + b * 8), tag, __ATOMIC_RELAXED, __HIP_MEMORY_SCOPE_AGENT);
        }
      } else {
        if (it == 0) {
          kv_rest(WS_V, L_V, 2, N4{}); kv_rest(WS_K, L_K, 3, N4{}); kv_rest(WS_V, L_V, 4, N4{}); kv_rest(WS_K, L_K, 5, N4{});
          kv_rest(WS_V, L_V, 6, N4{}); kv_rest(WS_K, L_K, 7, N2{});
        } else if (it == 1) {
          WAITV(0);
          lds_count(L_CNT);
        }
      }
      if (it == 4) {
        while (__builtin_amdgcn_readfirstlane(*(volatile LAS u32*)((LAS char*)smem + L_CNT)) != 4) {
          if (timed_out(t0)) break;
          __builtin_amdgcn_s_sleep(1);
        }
        FENCE();
      }
      if (it == 7) {
        const u32 l = opaque<u32>(l16);
        fa = ld16(wsr, l * 2, WS_FW + (b & 7) * 2048); fb = ld16(wsr, l * 2 + 16, WS_FW + (b & 7) * 2048);
      }
      if (it == 8) {
        if (!flags_ok(fa, fb, taglo, taghi)) wait_flags(wsr, WS_FW + (b & 7) * 2048, taglo, taghi, lane, t0);
      }
      if (it >= 8) {
        const u32 l = opaque<u32>(l16);
#pragma unroll
        for (int p = 5 * (it - 8); p < 5 * (it - 7); ++p) fetch(p, l);
      }
      if (it == 12 && w < 4) __builtin_amdgcn_s_setprio(0);
      FENCE();
      ASTAMP(it, 1);
      tiles4();
      FENCE();
      if (it == 9) STAMP(7);
    });
    STAMP(8);
    __builtin_amdgcn_s_setprio(0);
    ka = L_K + l16;
    float l = o[8];
    if (!__all(l <= 1.8446744e19f)) {
      float m = -INFINITY;
      o = (f32x16){};
      u32 vb = isv ? L_V + h2 * 256 + r32 * 16 : L_ONES;
      for (int t = 0; t < 64; ++t) {
        f32x16 z = {};
        const f32x16 sx = mfma32(ldsr<bf16x8>(ka + t * 1024), qf, z);
        float mt = sx[0];
#pragma unroll
        for (int i = 1; i < 16; ++i) mt = fmaxf(mt, sx[i]);
        const auto rr = __builtin_amdgcn_permlane32_swap(__float_as_uint(mt), __float_as_uint(mt), false, false);
        mt = fmaxf(fmaxf(__uint_as_float(rr[0]), __uint_as_float(rr[1])), m);
        const float f = __builtin_amdgcn_exp2f(m - mt);
        m = mt;
        u32 pk[8];
#pragma unroll
        for (int i = 0; i < 8; ++i) pk[i] = cvtpk(__builtin_amdgcn_exp2f(sx[2 * i] - m), __builtin_amdgcn_exp2f(sx[2 * i + 1] - m));
#pragma unroll
        for (int i = 0; i < 16; ++i) o[i] *= f;
        o = mfma32(ldsr<bf16x8>(vb), asbf((u32x4){pk[0], pk[1], pk[2], pk[3]}), o);
        o = mfma32(ldsr<bf16x8>(vb + 512), asbf((u32x4){pk[4], pk[5], pk[6], pk[7]}), o);
        vb += isv ? 1024u : 0u;
      }
      MFMA_GAP32(o);
      l = o[8];
    }
    const float rl = __builtin_amdgcn_rcpf(l);
#pragma unroll
    for (int i = 0; i < 4; ++i) opk[i] = canon(cvtpk(o[2 * i] * rl, o[2 * i + 1] * rl));
  }
  st16(wsr, ox_off(), 0, (u32x4){opk[0], opk[1], opk[2], opk[3]});
  STAMP(9);
  bf16x8 sel[4];
  auto make_sel = [&]() {
    const u32 l = opaque<u32>(lane), e = (l & 1) ? 0x3F800000u : 0x00003F80u;
#pragma unroll
    for (int q = 0; q < 4; ++q) {
      const bool on = ((l >> 2) & 3) == q;
      const u32 d0 = (on && !(l & 2)) ? e : 0u, d1 = (on && (l & 2)) ? e : 0u;
      sel[q] = asbf((u32x4){d0, d1, d0, d1});
    }
  };
  make_sel();
  const u32 gvo = w * 4096 + l16;
  auto pw_of = [&](u32 l) { return L_P + ((l >> 3) & 3) * 1024 + (w >> 1) * 256 + (l & 7) * 32 + (l >> 5) * 16 + (((w ^ (l >> 3)) & 1) * 8); };
  const u32 pw = pw_of(lane);
  const u32 pr = L_P + gvo;
  auto g_load = [&](bf16x8 (&f)[4], u32 src) {
#pragma unroll
    for (int t = 0; t < 4; ++t) f[t] = asbf(ld16(wsr, gvo + t * 1024, src));
  };
  auto g_wait = [&](bf16x8 (&f)[4], u32 src) {
    while (unwritten(f)) {
      if (timed_out(t0)) break;
      __builtin_amdgcn_s_sleep(1);
      FENCE();
      g_load(f, src);
    }
  };
  auto row_stats = [&](int half, const bf16x8 (&f)[4]) {
    float s0 = 0.f, s1 = 0.f, q0 = 0.f, q1 = 0.f;
#pragma unroll
    for (int t = 0; t < 4; ++t) {
      const u32x4 d = asu(f[t]);
      s0 = dot2(d[0], 0x3F803F80u, s0); q0 = dot2(d[0], d[0], q0); s1 = dot2(d[1], 0x3F803F80u, s1); q1 = dot2(d[1], d[1], q1);
      s0 = dot2(d[2], 0x3F803F80u, s0); q0 = dot2(d[2], d[2], q0); s1 = dot2(d[3], 0x3F803F80u, s1); q1 = dot2(d[3], d[3], q1);
    }
    const f32x2 sq = {swap32_add(s0 + s1), swap32_add(q0 + q1)};
    if (lane < 32) ldsw<f32x2>(L_STAT + half * STH + lane * STP + w * 8, sq);
  };
  auto partials = [&](int half, f32x16& acc0, f32x16& acc1, u32 pw) {
#pragma unroll
    for (int a = 0; a < 4; ++a) {
      ldsw<u32x2>(pw + half * 32768 + a * 4096, (u32x2){cvtpk(acc0[4 * a], acc0[4 * a + 1]), cvtpk(acc0[4 * a + 2], acc0[4 * a + 3])});
      ldsw<u32x2>(pw + half * 32768 + (4 + a) * 4096, (u32x2){cvtpk(acc1[4 * a], acc1[4 * a + 1]), cvtpk(acc1[4 * a + 2], acc1[4 * a + 3])});
    }
  };
  auto slice_gemm = [&](int half, const bf16x8 (&a)[8], const bf16x8 (&f)[4]) {
    f32x16 acc0 = {}, acc1 = {};
#pragma unroll
    for (int t = 0; t < 4; ++t) { acc0 = mfma32(a[t], f[t], acc0); acc1 = mfma32(a[4 + t], f[t], acc1); }
    partials(half, acc0, acc1, pw);
  };
  auto reduce_at = [&](int half, u32 pr_) -> f32x4 {
    f32x4 acc = {0, 0, 0, 0};
#pragma unroll
    for (int q = 0; q < 4; ++q) acc = mfma16(sel[q], ldsr<bf16x8>(pr_ + half * 32768 + q * 1024), acc);
    return acc;
  };
  auto reduce = [&](int half) { return reduce_at(half, pr); };
  auto src_of = [&](u32 base, int half) { return base + blk_of(half) * 32768; };
  auto pack4 = [&](f32x4 v) { return (u32x2){canon(cvtpk(v[0], v[1])), canon(cvtpk(v[2], v[3]))}; };
  const u32 a3off = w < 7 ? ((7 * j + w) * 32) * 1024 + l16 : OOB;
  auto a3_load = [&](int t0_, int t1_) {
#pragma unroll
    for (int t = 0; t < 32; ++t) if (t >= t0_ && t < t1_) a3[t] = asbf(ld16(wsr, a3off, WS_W3 + t * 1024));
  };

  f32x4 hres[2], xp[2], cs1;
  bf16x8 fA[4], fB[4];
  auto phase = [&](const bf16x8 (&a)[8], u32 src, bool stats, auto&& first, auto&& epi, auto&& put, auto&& pre, auto&& slotA, auto&& slotB, int sbase) {
    PSTAMP(sbase, 0);
    g_wait(fA, src_of(src, 0));
    STAMP(sbase);
    PSTAMP(sbase, 1);
    if (stats) row_stats(0, fA);
    first();
    PSTAMP(sbase, 2);
    slice_gemm(0, a, fA);
    PSTAMP(sbase, 3);
    g_load(fB, src_of(src, 1));
    slotA();
    PSTAMP(sbase, 4);
    BAR();
    PSTAMP(sbase, 5);
    const f32x4 rA = reduce(0);
    const u32x2 vA = epi(0, rA);
    PSTAMP(sbase, 6);
    put(0, vA);
    PSTAMP(sbase, 7);
    g_wait(fB, src_of(src, 1));
    STAMP(sbase + 1);
    PSTAMP(sbase, 8);
    if (stats) row_stats(1, fB);
    PSTAMP(sbase, 9);
    slice_gemm(1, a, fB);
    PSTAMP(sbase, 10);
    slotB();
    PSTAMP(sbase, 11);
    BAR();
    PSTAMP(sbase, 12);
    const u32x2 vB = epi(1, reduce(1));
    PSTAMP(sbase, 13);
    pre();
    PSTAMP(sbase, 14);
    put(1, vB);
    PSTAMP(sbase, 15);
  };
  auto lane_stat = [&](int half) {
    const u32 sa = L_STAT + half * STH + (lane >> 1) * STP + (lane & 1) * 32;
    const f32x4 p0 = ldsr<f32x4>(sa), p1 = ldsr<f32x4>(sa + 16);
    return finish_stat(xor1_add((p0[0] + p0[2]) + (p1[0] + p1[2])), xor1_add((p0[1] + p0[3]) + (p1[1] + p1[3])));
  };
  const u32 a4off = (2 * j * 112 + 14 * w) * 1024 + l16;
  g_load(fA, src_of(WS_OX, 0));
  {
    float c[2] = {0.f, 0.f};
#pragma unroll
    for (int t = 0; t < 8; ++t) {
      const u32x4 d = asu(a_1[t]);
#pragma unroll
      for (int e = 0; e < 4; ++e) c[t >> 2] = dot2(d[e], 0x3F803F80u, c[t >> 2]);
    }
    const float c0 = swap32_add(c[0]), c1 = swap32_add(c[1]);
    ldsw<float>(L_CS1P + w * 256 + lane * 4, lane < 32 ? c0 : c1);
  }
  phase(a_o, WS_OX, false,
        [&]() { BAR(); },
        [&](int half, f32x4 acc) {
          if (half == 0) {
            cs1 = (f32x4){0.f, 0.f, 0.f, 0.f};
#pragma unroll
            for (int v = 0; v < 8; ++v) cs1 += ldsr<f32x4>(L_CS1P + v * 256 + w * 32 + (lane & 1) * 16);
          }
          hres[half] = acc + (f32x4){bflo(xres[half][0]), bfhi(xres[half][0]), bflo(xres[half][1]), bfhi(xres[half][1])};
          return pack4(hres[half]);
        },
        [&](int half, u32x2 v) { st8(wsr, hx_off(WS_HX, half), 0, v); },
        [&]() { g_load(fA, src_of(WS_HX, 0)); },
        [&]() {
#pragma unroll
          for (int i = 0; i < 7; ++i) dma1(ws + WS_W4 + i * 1024, (((u32)(b >> 6) & 3u) * 8 + w) * 7168 + j * 229376 + l16, lb + L_DUM1);
        },
        [&]() { a3_load(A3_N0, A3_N0 + 5); }, 10);
  STAMP(12);
  phase(a_1, WS_HX, true, [&]() { },
        [&](int half, f32x4 acc) {
          const RowStat st = lane_stat(half);
          const float r2 = __builtin_amdgcn_rsqf(st.var * st.rstd * st.rstd + LN_EPS), rr = st.rstd * r2;
          xp[half] = (hres[half] - st.mu) * st.rstd;
          const f32x4 pre = (acc - st.mu * cs1) * rr;
          return pack4((f32x4){gelu(pre[0]), gelu(pre[1]), gelu(pre[2]), gelu(pre[3])});
        },
        [&](int half, u32x2 v) { st8(wsr, ts_off(half), 0, v); },
        [&]() { g_load(fA, src_of(WS_TS, 0)); },
        [&]() { a3_load(A3_N0 + 5, A3_N0 + 10); },
        [&]() { a3_load(A3_N0 + 10, A3_N0 + 13); dma4_sc1(ws + WS_W4 + 10 * 1024, a4off, lb + L_A4 + w * 8192); }, 13);
  STAMP(15);
  phase(a_2, WS_TS, false, [&]() { },
        [&](int half, f32x4 acc) {
          const f32x4 x1 = xp[half] + acc;
          ldsw<f32x4>(L_RES + half * 8192 + w * 1024 + l16, hres[half] + x1);
          return pack4(x1);
        },
        [&](int half, u32x2 v) { st8(wsr, hx_off(WS_X1, half), 0, v); },
        [&]() { dma4_sc1(ws + src_of(WS_X1, 0), gvo, lb + L_P + w * 4096); },
        [&]() { a3_load(A3_N0 + 13, A3_N0 + 16); dma4_sc1(ws + WS_W4 + 114688 + 10 * 1024, a4off, lb + L_A4 + w * 8192 + 4096); },
        [&]() { a3_load(A3_N0 + 16, 32); }, 16);
  STAMP(18);
  bf16x8 a4[10][2];
  auto a4_load = [&](int n) { a4[n >> 1][n & 1] = asbf(ld16(wsr, a4off, WS_W4 + (n & 1) * 114688 + (n >> 1) * 1024)); };
  u32x4 uf[14];
  auto u_load1 = [&](int half, int t) { uf[t] = ld16(wsr, l16, WS_UX + (blk_of(half) * 112 + w * 14 + t) * 1024); };
  {
    auto x1_finish = [&](int half) {
      for (;;) {
#pragma unroll
        for (int t = 0; t < 4; ++t) fA[t] = ldsr<bf16x8>(pr + half * 32768 + t * 1024);
        if (!unwritten(fA)) break;
        if (timed_out(t0)) break;
        __builtin_amdgcn_s_sleep(1);
        dma4_sc1(ws + src_of(WS_X1, half), gvo, lb + L_P + half * 32768 + w * 4096);
        WAITV(0);
      }
      row_stats(half, fA);
    };
    struct UOut { float rstd, nm; f32x4 cs[2]; float gv[4]; u32 up[8]; u32 l; };
    auto u_stat = [&](UOut& u, int half) {
      u.l = opaque<u32>(lane);
      const u32 sr = L_STAT + half * STH + (u.l & 31) * STP;
      const f32x4 p0 = ldsr<f32x4>(sr), p1 = ldsr<f32x4>(sr + 16), p2 = ldsr<f32x4>(sr + 32), p3 = ldsr<f32x4>(sr + 48);
      const f32x4 p = (p0 + p1) + (p2 + p3);
      const RowStat st = finish_stat(p[0] + p[2], p[1] + p[3]);
      u.rstd = st.rstd; u.nm = -st.mu * st.rstd;
    };
    auto u_cs = [&](UOut& u, int a) { u.cs[a & 1] = ldsr<f32x4>(L_CS + (32 * w + 8 * a) * 4 + (u.l >> 5) * 16); };
    auto u_val = [&](UOut& u, int half, const f32x16& acc, int v) {
      const int a = v >> 2, e = v & 3;
      u.gv[e] = gelu(acc[v] * u.rstd + u.nm * u.cs[a & 1][e]);
      if (e == 3) { u.up[2 * a] = canon(cvtpk(u.gv[0], u.gv[1])); u.up[2 * a + 1] = canon(cvtpk(u.gv[2], u.gv[3])); }
      if (v == 7 || v == 15) {
        const u32 uo = w < 7 ? WS_UX + (blk_of(half) * 112 + j * 14 + 2 * w) * 1024 + u.l * 16 : OOB;
        if (v == 7) st16(wsr, uo, 0, (u32x4){u.up[0], u.up[1], u.up[2], u.up[3]});
        else st16(wsr, uo, 1024, (u32x4){u.up[4], u.up[5], u.up[6], u.up[7]});
      }
    };
    WAITV(1);
    x1_finish(0);
    BAR();
    STAMP(19);
    dma4_sc1(ws + src_of(WS_X1, 1), gvo, lb + L_P + 32768 + w * 4096);
    const u32 xb = L_P + (w < 7 ? l16 : 0u);
    f32x16 accA = {}, accB = {};
    bf16x8 bq[4];
    {
      float c0 = 0.f, c1 = 0.f;
#pragma unroll
      for (int t = 0; t < 4; ++t) bq[t] = ldsr<bf16x8>(xb + t * 1024);
#pragma unroll
      for (int t = 0; t < 32; ++t) {
        accA = mfma32(a3[t], bq[t & 3], accA);
        if (t + 4 < 32) bq[t & 3] = ldsr<bf16x8>(xb + (t + 4) * 1024);
        const u32x4 d = asu(a3[t]);
        c0 = dot2(d[0], 0x3F803F80u, c0); c1 = dot2(d[1], 0x3F803F80u, c1); c0 = dot2(d[2], 0x3F803F80u, c0); c1 = dot2(d[3], 0x3F803F80u, c1);
        if (t >= 4 && (t & 3) == 0 && t / 4 - 1 < A4_N0) a4_load(t / 4 - 1);
        __builtin_amdgcn_sched_barrier(0);
      }
      const float c = swap32_add(c0 + c1);
      if (lane < 32) ldsw<float>(L_CS + w * 128 + lane * 4, c);
    }
    STAMP(20);
    asm volatile("s_waitcnt vmcnt(%0)" :: "n"(A4_N0) : "memory");
    x1_finish(1);
    BAR();
    STAMP(21);
    {
      UOut u;
#pragma unroll
      for (int t = 0; t < 4; ++t) bq[t] = ldsr<bf16x8>(xb + 32768 + t * 1024);
      u_stat(u, 0); u_cs(u, 0);
#pragma unroll
      for (int t = 0; t < 32; ++t) {
        if (t < 16 && (t & 3) == 1 && t + 3 < 16) u_cs(u, (t + 3) >> 2);
        accB = mfma32(a3[t], bq[t & 3], accB);
        if (t + 4 < 32) bq[t & 3] = ldsr<bf16x8>(xb + 32768 + (t + 4) * 1024);
        if (t < 16) u_val(u, 0, accA, t);
        const int n = A4_N0 + t - 1;
        if (t >= 1 && n < 20) a4_load(n);
        __builtin_amdgcn_sched_barrier(0);
      }
    }
    STAMP(22);
    {
      UOut u;
      u_stat(u, 1); u_cs(u, 0);
#pragma unroll
      for (int v = 0; v < 16; ++v) {
        if ((v & 3) == 0 && v + 4 < 16) u_cs(u, (v + 4) >> 2);
        if (v < 14) u_load1(0, v);
        u_val(u, 1, accB, v);
        __builtin_amdgcn_sched_barrier(0);
      }
    }
    STAMP(23);
  }
  {
    auto u_wait = [&](int half) {
      for (;;) {
        u32 m = 0;
#pragma unroll
        for (int t = 0; t < 14; ++t) m = umax(m, max4(uf[t]));
        if (!__any(m == SENT)) break;
        if (timed_out(t0)) break;
        __builtin_amdgcn_s_sleep(2);
        FENCE();
#pragma unroll
        for (int t = 0; t < 14; ++t) u_load1(half, t);
      }
    };
#pragma unroll
    for (int half = 0; half < 2; ++half) {
      u_wait(half);
      STAMP(24 + half);
      const u32 sa = L_A4 + w * 8192 + opaque<u32>(l16);
      f32x16 acc0 = {}, acc1 = {};
      bf16x8 sq[4][2];
#pragma unroll
      for (int t = 0; t < 14; ++t) {
        if (t >= 8 && t < 12) { sq[t - 8][0] = ldsr<bf16x8>(sa + (t - 8) * 1024); sq[t - 8][1] = ldsr<bf16x8>(sa + 4096 + (t - 8) * 1024); }
        acc0 = mfma32(t < 10 ? a4[t < 10 ? t : 0][0] : sq[t < 10 ? 0 : t - 10][0], asbf(uf[t]), acc0);
        acc1 = mfma32(t < 10 ? a4[t < 10 ? t : 0][1] : sq[t < 10 ? 0 : t - 10][1], asbf(uf[t]), acc1);
        if (half == 0 && t >= 7) { u_load1(1, 2 * (t - 7)); u_load1(1, 2 * (t - 7) + 1); }
        __builtin_amdgcn_sched_barrier(0);
      }
      if (half == 0) BAR();
      partials(half, acc0, acc1, pw_of(opaque<u32>(lane)));
      FENCE();
    }
    STAMP(26);
    make_sel();
    BAR();
#pragma unroll
    for (int half = 0; half < 2; ++half) {
      const u32 l = opaque<u32>(lane);
      const f32x4 v = reduce_at(half, L_P + w * 4096 + l * 16) + ldsr<f32x4>(L_RES + half * 8192 + w * 1024 + l * 16);
      st8(mkrsrc(out, 2097152), (u32)((blk_of(half) * 32 + (l >> 1)) * 1024 + (64 * j + 8 * w + 4 * (l & 1)) * 2), 0, (u32x2){cvtpk(v[0], v[1]), cvtpk(v[2], v[3])});
    }
  }
  STAMP(27);
}

extern "C" void kernel_launch(void* const* d_in, const int* in_sizes, int n_in, void* d_out, int out_size, void* d_ws, size_t ws_size, hipStream_t stream) {
  (void)in_sizes; (void)n_in; (void)out_size; (void)ws_size;
  fused_block_kernel<<<256, 512, 0, stream>>>((const u16*)d_in[0], (const u16*)d_in[1], (const u16*)d_in[2], (const u16*)d_in[3], (const u16*)d_in[4],
                                              (const u16*)d_in[6], (const u16*)d_in[7], (const u16*)d_in[8], (const u16*)d_in[9], (u16*)d_out, (char*)d_ws);
}
```

```cpp
#define SPIN_AT 27
#define SPIN_T 8000ull
#include <hip/hip_runtime.h>
#include <stdint.h>
#include <type_traits>

typedef unsigned short u16;
typedef unsigned int u32;
typedef unsigned long long u64;
typedef short bf16x8 __attribute__((ext_vector_type(8)));
typedef short s16x4 __attribute__((ext_vector_type(4)));
typedef float f32x2 __attribute__((ext_vector_type(2)));
typedef float f32x4 __attribute__((ext_vector_type(4)));
typedef float f32x16 __attribute__((ext_vector_type(16)));
typedef u32 u32x2 __attribute__((ext_vector_type(2)));
typedef u32 u32x4 __attribute__((ext_vector_type(4)));
typedef __bf16 bf16x2_t __attribute__((ext_vector_type(2)));
#define LAS __attribute__((address_space(3)))
#define GAS __attribute__((address_space(1)))
#define DI __device__ __forceinline__

constexpr u32 WS_EPOCH = 0x0;
constexpr u32 WS_FKV = 0x1000;
constexpr u32 WS_FW = 0x5000;
constexpr u32 WS_K = 0x10000;
constexpr u32 WS_V = 0x20000;
constexpr u32 WS_DBG = 0x1E00000;
constexpr u32 WS_WO = 0x100000, WS_W1 = 0x180000, WS_W2 = 0x200000, WS_W3 = 0x280000, WS_W4 = 0x440000;
constexpr u32 WS_OX = 0x600000, WS_HX = 0x800000, WS_TS = 0xA00000, WS_X1 = 0xC00000, WS_UX = 0xE00000;
constexpr u32 WS_STAMP = 0x2000000;
constexpr u32 WS_END = 0x2100000;
constexpr u32 OOB = 0x80000000u;

constexpr u32 XP = 1040;
constexpr u32 L_XIMG = 0;
constexpr u32 L_WQ = 66560;
constexpr u32 L_WKV = 132096;
constexpr u32 S_PM = 148480, S_PC = 156672, S_PS = 157184, S_PQ = 157696, S_FIN = 158208, S_QS = 158336, S_QC = 159360;
constexpr u32 L_DUM1 = 160384;
constexpr u32 L_CNT = 161408;
constexpr u32 L_K = 0, L_V = 65536, L_ONES = 131072;
constexpr u32 L_STG = 135424;
constexpr u32 L_CS1P = 155904;
constexpr u32 L_P = 0;
constexpr u32 L_STAT = 65536;
constexpr u32 STP = 80, STH = 2560;
constexpr u32 L_CS = 71680;
constexpr u32 L_RES = 73728;
constexpr u32 L_A4 = 90112;
constexpr u32 L_STAMP = 161792;
constexpr u32 LDS_BYTES = 163840;

__shared__ __attribute__((aligned(16))) char smem[LDS_BYTES];

constexpr u32 SENT = 0xFFFFFFFFu;
constexpr float LN_EPS = 1e-5f;
constexpr float QSCALE = 0.25f * 1.4426950408889634f;
#define CODE_KB 43

DI f32x16 mfma32(bf16x8 a, bf16x8 b, f32x16 c) { return __builtin_amdgcn_mfma_f32_32x32x16_bf16(a, b, c, 0, 0, 0); }
DI f32x4 mfma16(bf16x8 a, bf16x8 b, f32x4 c) { return __builtin_amdgcn_mfma_f32_16x16x32_bf16(a, b, c, 0, 0, 0); }
DI u32 cvtpk(float lo, float hi) { f32x2 v = {lo, hi}; bf16x2_t b = __builtin_convertvector(v, bf16x2_t); return __builtin_bit_cast(u32, b); }
DI u32 canon(u32 d) { return d < 0xFFFFFFFEu ? d : 0xFFFFFFFEu; }
DI float bflo(u32 d) { return __uint_as_float(d << 16); }
DI float bfhi(u32 d) { return __uint_as_float(d & 0xFFFF0000u); }
DI float dot2(u32 a, u32 b, float c) { return __builtin_amdgcn_fdot2_f32_bf16(__builtin_bit_cast(bf16x2_t, a), __builtin_bit_cast(bf16x2_t, b), c, false); }
DI float gelu(float x) {
  const float c1 = -2.0f * 0.7978845608028654f * 1.4426950408889634f, c2 = c1 * 0.044715f;
  const float e = __builtin_amdgcn_exp2f(x * (c1 + c2 * x * x));
  return x * __builtin_amdgcn_rcpf(1.0f + e);
}
DI float swap32_add(float v) {
  const auto r = __builtin_amdgcn_permlane32_swap(__float_as_uint(v), __float_as_uint(v), false, false);
  return __uint_as_float(r[0]) + __uint_as_float(r[1]);
}
DI float xor1_add(float v) { return v + __int_as_float(__builtin_amdgcn_mov_dpp(__float_as_int(v), 0xB1, 0xF, 0xF, true)); }

#define BAR() asm volatile("s_waitcnt lgkmcnt(0)\n\ts_barrier" ::: "memory")
#define WAITV(n) asm volatile("s_waitcnt vmcnt(" #n ")" ::: "memory")
#define FENCE() asm volatile("" ::: "memory")
template <class T> DI T opaque(T v) { asm volatile("" : "+v"(v)); return v; }

template <class T> DI T ldsr(u32 off) { return *(const LAS T*)((LAS char*)smem + off); }
template <class T> DI void ldsw(u32 off, T v) { *(LAS T*)((LAS char*)smem + off) = v; }
DI s16x4 ldstr(u32 off) {
  typedef short v4i16_t __attribute__((ext_vector_type(4)));
  return __builtin_bit_cast(s16x4, __builtin_amdgcn_ds_read_tr16_b64_v4i16((LAS v4i16_t*)((LAS char*)smem + off)));
}
DI bf16x8 cat(s16x4 a, s16x4 b) { return __builtin_shufflevector(a, b, 0, 1, 2, 3, 4, 5, 6, 7); }
DI u32 lds_base() { return (u32)(uintptr_t)(LAS char*)smem; }

#define DMA_HEAD "s_mov_b32 %[k], m0\n\ts_mov_b32 m0, %[lds]\n\ts_nop 0\n\t"
#define DMA_TAIL "s_mov_b32 m0, %[k]"
DI void dma1(const void* sbase, u32 voff, u32 ldsdst) {
  u32 keep;
  asm volatile(DMA_HEAD "global_load_lds_dwordx4 %[v], %[s]\n\t" DMA_TAIL : [k] "=&s"(keep) : [v] "v"(voff), [s] "s"(sbase), [lds] "s"(ldsdst) : "memory");
}
DI void dma2(const void* sbase, u32 voff, u32 ldsdst) {
  u32 keep;
  asm volatile(DMA_HEAD "global_load_lds_dwordx4 %[v], %[s]\n\tglobal_load_lds_dwordx4 %[v], %[s] offset:1024\n\t" DMA_TAIL
               : [k] "=&s"(keep) : [v] "v"(voff), [s] "s"(sbase), [lds] "s"(ldsdst) : "memory");
}
DI void dma4_rows(const void* sbase, u32 v0, u32 v1, u32 v2, u32 v3, u32 ldsdst) {
  u32 keep;
  asm volatile(DMA_HEAD "global_load_lds_dwordx4 %[a], %[s]\n\tglobal_load_lds_dwordx4 %[b], %[s] offset:1040\n\t"
               "global_load_lds_dwordx4 %[c], %[s] offset:2080\n\tglobal_load_lds_dwordx4 %[d], %[s] offset:3120\n\t" DMA_TAIL
               : [k] "=&s"(keep) : [a] "v"(v0), [b] "v"(v1), [c] "v"(v2), [d] "v"(v3), [s] "s"(sbase), [lds] "s"(ldsdst) : "memory");
}
DI void dma4_sc1(const void* sbase, u32 voff, u32 ldsdst) {
  u32 keep;
  asm volatile(DMA_HEAD "global_load_lds_dwordx4 %[v], %[s] sc1\n\tglobal_load_lds_dwordx4 %[v], %[s] offset:1024 sc1\n\t"
               "global_load_lds_dwordx4 %[v], %[s] offset:2048 sc1\n\tglobal_load_lds_dwordx4 %[v], %[s] offset:3072 sc1\n\t" DMA_TAIL
               : [k] "=&s"(keep) : [v] "v"(voff), [s] "s"(sbase), [lds] "s"(ldsdst) : "memory");
}
#define DMA_STEP(SC) "global_load_lds_dwordx4 %[v], %[s]" SC "\n\ts_add_u32 m0, m0, %[ls]\n\tv_add_u32 %[v], %[gs], %[v]\n\t"
template <int N, bool SC1, u32 GS>
DI void dma_strided(const void* sbase, u32 voff, u32 ldsdst, u32 lstep) {
  u32 keep;
  static_assert(N == 2 || N == 3 || N == 4 || N == 8, "");
#define DMA_RUN(BODY) asm volatile(DMA_HEAD BODY DMA_TAIL : [k] "=&s"(keep), [v] "+v"(voff) : [s] "s"(sbase), [lds] "s"(ldsdst), [ls] "s"(lstep), [gs] "n"(GS) : "memory", "scc")
  if constexpr (SC1) {
    if constexpr (N == 2) DMA_RUN(DMA_STEP(" sc1") DMA_STEP(" sc1"));
    if constexpr (N == 3) DMA_RUN(DMA_STEP(" sc1") DMA_STEP(" sc1") DMA_STEP(" sc1"));
    if constexpr (N == 4) DMA_RUN(DMA_STEP(" sc1") DMA_STEP(" sc1") DMA_STEP(" sc1") DMA_STEP(" sc1"));
  } else {
    if constexpr (N == 8) DMA_RUN(DMA_STEP("") DMA_STEP("") DMA_STEP("") DMA_STEP("") DMA_STEP("") DMA_STEP("") DMA_STEP("") DMA_STEP(""));
  }
#undef DMA_RUN
}

typedef __amdgpu_buffer_rsrc_t rsrc_t;
DI rsrc_t mkrsrc(const void* p, u32 bytes) { return __builtin_amdgcn_make_buffer_rsrc((void*)p, 0, bytes, 0x00020000); }
DI u32x4 ld16(rsrc_t r, u32 voff, u32 soff) { return __builtin_amdgcn_raw_buffer_load_b128(r, voff, soff, 16); }
DI void st16(rsrc_t r, u32 voff, u32 soff, u32x4 v) { __builtin_amdgcn_raw_buffer_store_b128(v, r, voff, soff, 16); }
DI void st8(rsrc_t r, u32 voff, u32 soff, u32x2 v) { __builtin_amdgcn_raw_buffer_store_b64(v, r, voff, soff, 16); }
DI void st4(rsrc_t r, u32 voff, u32 soff, u32 v) { __builtin_amdgcn_raw_buffer_store_b32(v, r, voff, soff, 16); }
DI bf16x8 asbf(u32x4 v) { return __builtin_bit_cast(bf16x8, v); }
DI u32x4 asu(bf16x8 v) { return __builtin_bit_cast(u32x4, v); }
DI u32 umax(u32 a, u32 b) { return a > b ? a : b; }
DI u32 max4(u32x4 v) { return umax(umax(v[0], v[1]), umax(v[2], v[3])); }

constexpr u64 SPIN_TICKS = 400000ull;
DI bool timed_out(u64 t0) { return __builtin_amdgcn_s_memrealtime() - t0 > SPIN_TICKS; }

#define STAMP(i) do { } while (0)
#define ASTAMP(it, pos) do { } while (0)
#define PSTAMP(sb, i) do { } while (0)
#undef STAMP
#define STAMP(i) do { if ((i) == SPIN_AT) { while (__builtin_amdgcn_s_memrealtime() - t0 < SPIN_T) __builtin_amdgcn_s_sleep(1); } } while (0)
#define MFMA_GAP16(v) asm volatile("s_nop 7" : "+v"(v))
#define MFMA_GAP32(v) asm volatile("s_nop 7\n\ts_nop 3" : "+v"(v))

DI bool flags_ok(u32x4 a, u32x4 b, u32 lo, u32 hi) {
  return __all(a[0] == lo && a[1] == hi && a[2] == lo && a[3] == hi && b[0] == lo && b[1] == hi && b[2] == lo && b[3] == hi);
}
DI void wait_flags(rsrc_t wsr, u32 base, u32 taglo, u32 taghi, int lane, u64 t0) {
  for (;;) {
    const u32x4 a = ld16(wsr, lane * 32, base), b = ld16(wsr, lane * 32 + 16, base);
    if (flags_ok(a, b, taglo, taghi)) break;
    if (timed_out(t0)) break;
    __builtin_amdgcn_s_sleep(1);
    FENCE();
  }
}

DI bf16x8 ones_frag() { return asbf((u32x4){0x3F803F80u, 0x3F803F80u, 0x3F803F80u, 0x3F803F80u}); }
DI float diag_pick(f32x4 g, int lane) { const int r = lane & 3; float a = (r & 1) ? g[1] : g[0], b = (r & 1) ? g[3] : g[2]; return (r & 2) ? b : a; }

struct RowStat { float mu, rstd, var; };
DI RowStat finish_stat(float s, float q) {
  RowStat r; r.mu = s * (1.0f / 512.0f);
  r.var = fmaxf(q * (1.0f / 512.0f) - r.mu * r.mu, 0.0f);
  r.rstd = __builtin_amdgcn_rsqf(r.var + LN_EPS);
  return r;
}
DI bool unwritten(const bf16x8 (&f)[4]) {
  return __any(umax(umax(max4(asu(f[0])), max4(asu(f[1]))), umax(max4(asu(f[2])), max4(asu(f[3])))) == SENT);
}

template <int I, int N, class F> DI void static_for(F&& f) {
  if constexpr (I < N) { f(std::integral_constant<int, I>{}); static_for<I + 1, N>(f); }
}
struct Job { const char* src; u32 prow; bool perm; };
DI Job retile_job(u32 f, u64 wo, u64 w1, u64 w2, u64 w3, u64 w4) {
  const bool big = f >= 1536, last = f >= 3328;
  const u32 g = f - 3328, T = last ? g / 112 : big ? (f - 1536) >> 5 : (f >> 5) & 15, ks = last ? g % 112 : f & 31;
  const u32 prow = big && !last ? 3584 : 1024;
  u64 m = f < 1024 ? w1 : w2;
  m = f < 512 ? wo : m; m = big ? w3 : m; m = last ? w4 : m;
  return Job{(const char*)(m + (u64)(16 * ks * prow + T * 64)), prow, last || f < 512};
}

#define A3_N0 6
#define A4_N0 6

__global__ __launch_bounds__(512, 2) void fused_block_kernel(const u16* __restrict__ x, const u16* __restrict__ wq, const u16* __restrict__ wk,
                                                             const u16* __restrict__ wv, const u16* __restrict__ wo, const u16* __restrict__ w1,
                                                             const u16* __restrict__ w2, const u16* __restrict__ w3, const u16* __restrict__ w4,
                                                             u16* __restrict__ out, char* ws) {
  const int tid = threadIdx.x, lane = tid & 63, w = __builtin_amdgcn_readfirstlane(tid >> 6);
  const int b = blockIdx.x;
  const int cl = (b & 7) * 4 + (b >> 6), j = (b >> 3) & 7;
  const u32 lb = lds_base();
  const u32 l16 = lane * 16;
  {
    u64 pc;
    asm volatile("s_getpc_b64 %0" : "=s"(pc));
    dma1((const void*)(pc & ~63ull), (u32)((((b >> 3) * 8 + w) % CODE_KB) * 1024) + l16, lb + L_DUM1);
  }
  asm volatile("" :: "s"(x), "s"(wq), "s"(wk), "s"(wv), "s"(wo), "s"(w1), "s"(w2), "s"(w3), "s"(w4), "s"(out), "s"(ws));
  const u64 t0 = __builtin_amdgcn_s_memrealtime();
  const int i16 = lane & 15, g4 = lane >> 4, r32 = lane & 31, h2 = lane >> 5;
  STAMP(0);

  const int kvrt = j >> 1, kvmat = j & 1;
  auto grow = [&](int rho) { return (rho < 32 ? 0 : 1024 - 32) + 32 * cl + rho; };
  const int R0 = grow(16 * kvrt);
  u32x4 xk0, xk1;
  u64 tag;
  {
    const u32 xo = (u32)(i16 * 1024 + g4 * 32);
    asm volatile("global_load_dwordx4 %0, %2, %3\n\tglobal_load_dwordx4 %1, %2, %3 offset:16"
                 : "=&v"(xk0), "=&v"(xk1) : "v"(xo), "s"((const char*)x + R0 * 1024 + w * 128) : "memory");
    dma2((const char*)(kvmat ? wv : wk) + w * 2048, l16, lb + L_WKV + w * 2048);
    asm volatile("s_load_dwordx2 %0, %1, 0x0" : "=s"(tag) : "s"(ws + WS_EPOCH) : "memory");
    const char* xr = (const char*)x + grow(8 * w) * 1024 - 48;
    dma4_rows(xr, l16 + 48, l16 + 32, l16 + 16, l16, lb + L_XIMG + 8 * w * XP);
    dma4_rows(xr + 4096, l16 + 48, l16 + 32, l16 + 16, l16, lb + L_XIMG + (8 * w + 4) * XP);
    dma_strided<8, false, 0x2000>((const char*)wq + w * 65536 + j * 128, (u32)((lane >> 3) * 1024 + (((lane & 7) ^ (4 * ((lane >> 4) & 1))) * 16)), lb + L_WQ + w * 8192, 1024);
    asm volatile("s_waitcnt vmcnt(16) lgkmcnt(0)" : "+v"(xk0), "+v"(xk1), "+s"(tag) :: "memory");
  }
  tag += 1ull;
  const u32 taglo = (u32)tag, taghi = (u32)(tag >> 32);
  const rsrc_t wsr = mkrsrc(ws, WS_END);
  const bf16x8 one = ones_frag();
  STAMP(1);

  {
    f32x4 am = {0, 0, 0, 0}, ac = {0, 0, 0, 0}, as = {0, 0, 0, 0}, aq = {0, 0, 0, 0};
    const u32 wa = L_WKV + w * 2048 + (16 * g4 + (i16 >> 2)) * 32 + (i16 & 3) * 8;
    const bf16x8 wf0 = cat(ldstr(wa), ldstr(wa + 128)), wf1 = cat(ldstr(wa + 256), ldstr(wa + 384));
    const bf16x8 xf0 = asbf(xk0), xf1 = asbf(xk1);
    as = mfma16(one, xf0, as); aq = mfma16(xf0, xf0, aq); as = mfma16(one, xf1, as); aq = mfma16(xf1, xf1, aq);
    if (kvmat) {
      am = mfma16(xf0, wf0, am); ac = mfma16(one, wf0, ac); am = mfma16(xf1, wf1, am); ac = mfma16(one, wf1, ac);
      MFMA_GAP16(am); MFMA_GAP16(ac);
      if (lane < 16) ldsw<float>(S_PC + (w * 16 + lane) * 4, ac[0]);
    } else {
      am = mfma16(wf0, xf0, am); ac = mfma16(wf0, one, ac); am = mfma16(wf1, xf1, am); ac = mfma16(wf1, one, ac);
      MFMA_GAP16(am); MFMA_GAP16(ac);
      if (i16 == 0) ldsw<f32x4>(S_PC + (w * 16 + 4 * g4) * 4, ac);
    }
    ldsw<f32x4>(S_PM + w * 1024 + l16, am);
    if (lane < 16) ldsw<float>(S_PS + (lane * 8 + w) * 4, as[0]);
    if ((i16 >> 2) == g4) ldsw<float>(S_PQ + (i16 * 8 + w) * 4, diag_pick(aq, lane));
    BAR();
  }
  if (w == 0) {
    {
      const f32x4 s0 = ldsr<f32x4>(S_PS + i16 * 32), s1 = ldsr<f32x4>(S_PS + i16 * 32 + 16);
      const f32x4 q0 = ldsr<f32x4>(S_PQ + i16 * 32), q1 = ldsr<f32x4>(S_PQ + i16 * 32 + 16);
      const f32x4 s = s0 + s1, q = q0 + q1;
      const RowStat st = finish_stat((s[0] + s[1]) + (s[2] + s[3]), (q[0] + q[1]) + (q[2] + q[3]));
      if (lane < 16) { ldsw<float>(S_FIN + lane * 4, st.mu); ldsw<float>(S_FIN + 64 + lane * 4, st.rstd); }
    }
    f32x4 m = {0, 0, 0, 0};
#pragma unroll
    for (int ww = 0; ww < 8; ++ww) m += ldsr<f32x4>(S_PM + ww * 1024 + l16);
    f32x4 v;
    u32 off;
    if (kvmat) {
      float c1 = 0.f;
#pragma unroll
      for (int ww = 0; ww < 8; ++ww) c1 += ldsr<float>(S_PC + (ww * 16 + i16) * 4);
      asm volatile("s_waitcnt lgkmcnt(0)" ::: "memory");
      const f32x4 mu = ldsr<f32x4>(S_FIN + g4 * 16), rs = ldsr<f32x4>(S_FIN + 64 + g4 * 16);
      v = rs * (m - mu * c1);
      off = WS_V + (R0 >> 5) * 1024 + ((R0 >> 4) & 1) * 512 + (g4 & 1) * 256 + i16 * 16 + (g4 >> 1) * 8;
    } else {
      f32x4 c4 = {0, 0, 0, 0};
#pragma unroll
      for (int ww = 0; ww < 8; ++ww) c4 += ldsr<f32x4>(S_PC + (ww * 16 + 4 * g4) * 4);
      asm volatile("s_waitcnt lgkmcnt(0)" ::: "memory");
      const float mu = ldsr<float>(S_FIN + i16 * 4), rs = ldsr<float>(S_FIN + 64 + i16 * 4);
      v = rs * (m - mu * c4);
      off = WS_K + (R0 >> 5) * 1024 + (g4 & 1) * 512 + ((R0 & 31) + i16) * 16 + (g4 >> 1) * 8;
    }
    st8(wsr, off, 0, (u32x2){cvtpk(v[0], v[1]), cvtpk(v[2], v[3])});
    WAITV(1);
  } else {
    WAITV(0);
  }
#define JOB(i) retile_job((u32)(b * 20 + w * 5 + (i)), (u64)wo, (u64)w1, (u64)w2, (u64)w3, (u64)w4)
  u32x4 rts[5];
  if (w < 4) {
#pragma unroll
    for (int i = 0; i < 5; ++i) {
      const Job jb = JOB(i);
      rts[i] = *(const GAS u32x4*)(jb.src + (u32)((lane >> 2) * jb.prow + (lane & 3) * 16));
    }
  }
  FENCE();
  STAMP(2);
  const int q_hp = w & 1, q_rt = (w >> 1) & 1, q_kh = w >> 2;
  const int head = 2 * q_hp + q_kh;
  bf16x8 qf;
  {
    BAR();
    STAMP(3);
    {
      f32x4 as = {0, 0, 0, 0}, aq = {0, 0, 0, 0};
      const int rt = w & 3, kh = w >> 2;
      const u32 sa = L_XIMG + (16 * rt + i16) * XP + kh * 512 + g4 * 16;
#pragma unroll
      for (int t = 0; t < 8; ++t) {
        const bf16x8 xf = ldsr<bf16x8>(sa + t * 64);
        as = mfma16(one, xf, as); aq = mfma16(xf, xf, aq);
      }
      MFMA_GAP16(as); MFMA_GAP16(aq);
      if (lane < 16) ldsw<float>(S_QS + ((16 * rt + lane) * 2 + kh) * 4, as[0]);
      if ((i16 >> 2) == g4) ldsw<float>(S_QS + 512 + ((16 * rt + i16) * 2 + kh) * 4, diag_pick(aq, lane));
    }
    f32x16 acc = {}, accc = {};
    const int q = i16 >> 2, p = i16 & 3, cb = g4 & 1;
    const u32 wa = L_WQ + (256 * q_kh + 8 * h2 + q) * 128 + (((4 * q_hp + 2 * cb + (p >> 1)) ^ (4 * ((q >> 1) & 1))) * 16) + (p & 1) * 8;
    const u32 xa = L_XIMG + (32 * q_rt + r32) * XP + q_kh * 512 + h2 * 16;
#pragma unroll
    for (int t = 0; t < 16; ++t) {
      const bf16x8 af = cat(ldstr(wa + t * 2048), ldstr(wa + t * 2048 + 512));
      acc = mfma32(af, ldsr<bf16x8>(xa + t * 32), acc);
    }
    const u32 wb = wa + q_rt * 16384;
#pragma unroll
    for (int t = 0; t < 8; ++t) accc = mfma32(cat(ldstr(wb + t * 2048), ldstr(wb + t * 2048 + 512)), one, accc);
    if (w == 0) {
      WAITV(5);
      if (lane < 8) __hip_atomic_store((GAS u64*)(ws + WS_FKV + lane * 2048 + b * 8), tag, __ATOMIC_RELAXED, __HIP_MEMORY_SCOPE_AGENT);
    }
    MFMA_GAP32(acc); MFMA_GAP32(accc);
    f32x4 k0, k1, s0, s1;
    if (q_kh) {
      k0 = (f32x4){acc[8], acc[9], acc[10], acc[11]}; k1 = (f32x4){acc[12], acc[13], acc[14], acc[15]};
      s0 = (f32x4){acc[0], acc[1], acc[2], acc[3]}; s1 = (f32x4){acc[4], acc[5], acc[6], acc[7]};
    } else {
      k0 = (f32x4){acc[0], acc[1], acc[2], acc[3]}; k1 = (f32x4){acc[4], acc[5], acc[6], acc[7]};
      s0 = (f32x4){acc[8], acc[9], acc[10], acc[11]}; s1 = (f32x4){acc[12], acc[13], acc[14], acc[15]};
    }
    ldsw<f32x4>(L_WKV + w * 2048 + l16, s0); ldsw<f32x4>(L_WKV + w * 2048 + 1024 + l16, s1);
    if (r32 == 0) {
#pragma unroll
      for (int a = 0; a < 4; ++a) ldsw<f32x4>(S_QC + (((q_hp * 2 + q_rt) * 2 + q_kh) * 32 + 8 * a + 4 * h2) * 4, (f32x4){accc[4 * a], accc[4 * a + 1], accc[4 * a + 2], accc[4 * a + 3]});
    }
    BAR();
    k0 += ldsr<f32x4>(L_WKV + (w ^ 4) * 2048 + l16); k1 += ldsr<f32x4>(L_WKV + (w ^ 4) * 2048 + 1024 + l16);
    f32x4 c0 = {0, 0, 0, 0}, c1 = {0, 0, 0, 0};
#pragma unroll
    for (int s = 0; s < 4; ++s) {
      c0 += ldsr<f32x4>(S_QC + ((q_hp * 4 + s) * 32 + 16 * q_kh + 4 * h2) * 4);
      c1 += ldsr<f32x4>(S_QC + ((q_hp * 4 + s) * 32 + 16 * q_kh + 8 + 4 * h2) * 4);
    }
    const f32x2 sp = ldsr<f32x2>(S_QS + (32 * q_rt + r32) * 8), qp = ldsr<f32x2>(S_QS + 512 + (32 * q_rt + r32) * 8);
    const RowStat st = finish_stat(sp[0] + sp[1], qp[0] + qp[1]);
    const float sc = st.rstd * QSCALE;
    k0 = (k0 - st.mu * c0) * sc; k1 = (k1 - st.mu * c1) * sc;
    qf = opaque(asbf((u32x4){cvtpk(k0[0], k0[1]), cvtpk(k0[2], k0[3]), cvtpk(k1[0], k1[1]), cvtpk(k1[2], k1[3])}));
  }
  STAMP(4);
  auto blk_of = [&](int half) { return half * 32 + cl; };
  const int dlt = (j >> 1) == 0 ? 1 : (j >> 1) == 1 ? -1 : (j >> 1) == 2 ? 2 : -2;
  const u32 pc_in = (u32)((4 * j + (w >> 1)) * 1024 + (w & 1) * 512);
  auto hx_off = [&](u32 base, int half) { return base + blk_of(half) * 32768 + pc_in + lane * 8; };
  auto ts_off = [&](int half) {
    const u32 l = opaque<u32>(lane);
    const u32 R = (u32)(blk_of(half) * 32 + (int)(l >> 1) + dlt);
    return R < 2048u ? WS_TS + (R >> 5) * 32768 + pc_in + (R & 31) * 16 + (l & 1) * 8 : OOB;
  };
  auto ox_off = [&]() { return WS_OX + blk_of(q_rt) * 32768 + (4 * j + head) * 1024 + l16; };
  {
    if (w == 1) {
      wait_flags(wsr, WS_FKV + (b & 7) * 2048, taglo, taghi, lane, t0);
      if (b == 0 && lane == 0) __hip_atomic_store((GAS u64*)(ws + WS_EPOCH), tag, __ATOMIC_RELAXED, __HIP_MEMORY_SCOPE_AGENT);
    }
    BAR();
    STAMP(5);
    dma_strided<3, true, 0x2000>(ws + WS_K, w * 1024 + l16, lb + L_K + w * 1024, 8192);
    dma_strided<2, true, 0x2000>(ws + WS_V, w * 1024 + l16, lb + L_V + w * 1024, 8192);
    if (tid < 272) ldsw<u32x4>(L_ONES + tid * 16, (u32x4){0x3F803F80u, 0x3F803F80u, 0x3F803F80u, 0x3F803F80u});
    if (tid == 511) ldsw<u32x2>(L_CNT, (u32x2){0u, 0u});
    WAITV(0);
    BAR();
    STAMP(6);
  }
  u32 opk[4];
  bf16x8 a_o[8], a_1[8], a_2[8], a3[32];
  u32x2 xres[2];
  {
    u32 ka = L_K + l16;
    const bool isv = r32 < 16;
    u32 va = isv ? L_V + h2 * 256 + r32 * 16 : L_ONES + ((r32 >= 20 && r32 < 28) ? 64 : 0);
    const u32 vinc = isv ? 4096u : 0u;
    f32x16 o = {};
    f32x16 negm;
    {
      const bf16x8 kf = ldsr<bf16x8>(ka);
      f32x16 z = {};
      const f32x16 s = mfma32(kf, qf, z);
      float m = fmaxf(fmaxf(s[0], s[1]), fmaxf(s[2], s[3]));
#pragma unroll
      for (int i = 4; i < 16; i += 2) m = fmaxf(m, fmaxf(s[i], s[i + 1]));
      const auto rr = __builtin_amdgcn_permlane32_swap(__float_as_uint(m), __float_as_uint(m), false, false);
      m = fmaxf(__uint_as_float(rr[0]), __uint_as_float(rr[1]));
#pragma unroll
      for (int i = 0; i < 16; ++i) negm[i] = -m;
    }
    f32x16 s = mfma32(ldsr<bf16x8>(ka), qf, negm);
    auto tiles4 = [&]() {
#pragma unroll
      for (int u = 0; u < 4; ++u) {
        const bf16x8 v0 = ldsr<bf16x8>(va + u * 1024), v1 = ldsr<bf16x8>(va + u * 1024 + 512);
        const bf16x8 kn = ldsr<bf16x8>(ka + (u + 1) * 1024);
        const f32x16 sn = mfma32(kn, qf, negm);
        u32 pk[8];
#pragma unroll
        for (int i = 0; i < 8; ++i) pk[i] = cvtpk(__builtin_amdgcn_exp2f(s[2 * i]), __builtin_amdgcn_exp2f(s[2 * i + 1]));
        o = mfma32(v0, asbf((u32x4){pk[0], pk[1], pk[2], pk[3]}), o);
        o = mfma32(v1, asbf((u32x4){pk[4], pk[5], pk[6], pk[7]}), o);
        s = sn;
      }
      va += vinc; ka += 4096;
    };
    if (w < 4) __builtin_amdgcn_s_setprio(2); else __builtin_amdgcn_s_setprio(1);
    auto kv_rest = [&](u32 img, u32 limg, int c0, auto n) {
      dma_strided<n.value, true, 4096>(ws + img + c0 * 8192, opaque<u32>((w & 3) * 1024 + l16), lb + limg + c0 * 8192 + (w & 3) * 1024, 4096);
    };
    using N2 = std::integral_constant<int, 2>; using N4 = std::integral_constant<int, 4>;
    auto lds_count = [&](u32 off) {
      u32 n;
      asm volatile("ds_add_rtn_u32 %0, %1, %2\n\ts_waitcnt lgkmcnt(0)" : "=v"(n) : "v"(lb + off), "v"(lane == 0 ? 1u : 0u) : "memory");
      return (u32)__builtin_amdgcn_readfirstlane(n);
    };
    const u32x4 s4 = {SENT, SENT, SENT, SENT};
    u32x4 fa, fb;
    auto fetch = [&](int p, u32 l) {
      const u32 fo = (2 * j * 32 + 4 * w) * 1024;
      if (p < 24) {
        const int t = p & 7;
        const bf16x8 v = asbf(ld16(wsr, l + (t & 3) * 1024, (p < 8 ? WS_WO : p < 16 ? WS_W1 : WS_W2) + fo + (t >> 2) * 32768));
        if (p < 8) a_o[t] = v; else if (p < 16) a_1[t] = v; else a_2[t] = v;
      } else if (p < 26) {
        xres[p - 24] = *(const GAS u32x2*)((const char*)x + (u32)((blk_of(p - 24) * 32 + (l >> 5)) * 1024 + (64 * j + 8 * w) * 2 + ((l >> 4) & 1) * 8));
      } else if (p < 26 + A3_N0) {
        a3[p - 26] = asbf(ld16(wsr, w < 7 ? ((7 * j + w) * 32) * 1024 + l : OOB, WS_W3 + (p - 26) * 1024));
      } else if (p < 33 + A3_N0) {
        dma1(ws + WS_W3 + (p - 26 - A3_N0) * 1024, (((u32)(b >> 6) & 3u) * 8 + w) * 7168 + j * 229376 + l, lb + L_DUM1);
      }
    };
    static_for<0, 16>([&](auto IT) {
      constexpr int it = IT.value;
      ASTAMP(it, 0);
      if (w < 4) {
        if (it == 0) {
          const u32 l = opaque<u32>(lane);
          const u32 sb = L_STG + w * 5120;
#pragma unroll
          for (int i = 0; i < 5; ++i) ldsw<u32x4>(sb + i * 1024 + l * 16, rts[i]);
          const u32 tb = sb + ((l & 15) >> 2) * 64 + ((l >> 4) & 1) * 32 + (l & 3) * 8;
#pragma unroll
          for (int i = 0; i < 5; ++i) {
            const bool perm = JOB(i).perm;
            const u32 t0a = tb + i * 1024 + (l >> 5) * (perm ? 256 : 512);
            st16(wsr, l * 16, WS_WO + (b * 20 + w * 5 + i) * 1024, asu(cat(ldstr(t0a), ldstr(t0a + (perm ? 512 : 256)))));
          }
        } else if (it == 1) {
          const u32 l = opaque<u32>(lane);
#pragma unroll
          for (int half = 0; half < 2; ++half) {
            const u32 po = blk_of(half) * 32768 + (4 * j + w) * 1024;
            st16(wsr, l * 16, WS_OX + po, s4); st16(wsr, l * 16, WS_HX + po, s4); st16(wsr, l * 16, WS_X1 + po, s4);
            const int R = blk_of(half) * 32 + (int)(l & 31) + dlt;
            const u32 mk = (u32)R < 2048u ? SENT : 0u;
            st16(wsr, ((R & 2047) >> 5) * 32768 + (l >> 5) * 512 + (R & 31) * 16, WS_TS + (4 * j + w) * 1024, (u32x4){mk, mk, mk, mk});
          }
        } else if (it == 2) {
          const u32 uo = WS_UX + (blk_of(w >> 1) * 112 + 14 * j + 7 * (w & 1)) * 1024;
          const u32 l = opaque<u32>(l16);
#pragma unroll
          for (int i = 0; i < 7; ++i) st16(wsr, l + (i & 3) * 1024, uo + (i >> 2) * 4096, s4);
        } else if (it == 5) {
          WAITV(0);
          if (lds_count(L_CNT + 4) == 3 && lane < 8) __hip_atomic_store((GAS u64*)(ws + WS_FW + lane * 2048 + b * 8), tag, __ATOMIC_RELAXED, __HIP_MEMORY_SCOPE_AGENT);
        }
      } else {
        if (it == 0) {
          kv_rest(WS_V, L_V, 2, N4{}); kv_rest(WS_K, L_K, 3, N4{}); kv_rest(WS_V, L_V, 4, N4{}); kv_rest(WS_K, L_K, 5, N4{});
          kv_rest(WS_V, L_V, 6, N4{}); kv_rest(WS_K, L_K, 7, N2{});
        } else if (it == 1) {
          WAITV(0);
          lds_count(L_CNT);
        }
      }
      if (it == 4) {
        while (__builtin_amdgcn_readfirstlane(*(volatile LAS u32*)((LAS char*)smem + L_CNT)) != 4) {
          if (timed_out(t0)) break;
          __builtin_amdgcn_s_sleep(1);
        }
        FENCE();
      }
      if (it == 3 && w == 7 && (true)) asm volatile("buffer_wbl2 sc0 sc1" ::: "memory");
      if (it == 7) {
        const u32 l = opaque<u32>(l16);
        fa = ld16(wsr, l * 2, WS_FW + (b & 7) * 2048); fb = ld16(wsr, l * 2 + 16, WS_FW + (b & 7) * 2048);
      }
      if (it == 8) {
        if (!flags_ok(fa, fb, taglo, taghi)) wait_flags(wsr, WS_FW + (b & 7) * 2048, taglo, taghi, lane, t0);
      }
      if (it >= 8) {
        const u32 l = opaque<u32>(l16);
#pragma unroll
        for (int p = 5 * (it - 8); p < 5 * (it - 7); ++p) fetch(p, l);
      }
      if (it == 12 && w < 4) __builtin_amdgcn_s_setprio(0);
      FENCE();
      ASTAMP(it, 1);
      tiles4();
      FENCE();
      if (it == 9) STAMP(7);
    });
    STAMP(8);
    __builtin_amdgcn_s_setprio(0);
    ka = L_K + l16;
    float l = o[8];
    if (!__all(l <= 1.8446744e19f)) {
      float m = -INFINITY;
      o = (f32x16){};
      u32 vb = isv ? L_V + h2 * 256 + r32 * 16 : L_ONES;
      for (int t = 0; t < 64; ++t) {
        f32x16 z = {};
        const f32x16 sx = mfma32(ldsr<bf16x8>(ka + t * 1024), qf, z);
        float mt = sx[0];
#pragma unroll
        for (int i = 1; i < 16; ++i) mt = fmaxf(mt, sx[i]);
        const auto rr = __builtin_amdgcn_permlane32_swap(__float_as_uint(mt), __float_as_uint(mt), false, false);
        mt = fmaxf(fmaxf(__uint_as_float(rr[0]), __uint_as_float(rr[1])), m);
        const float f = __builtin_amdgcn_exp2f(m - mt);
        m = mt;
        u32 pk[8];
#pragma unroll
        for (int i = 0; i < 8; ++i) pk[i] = cvtpk(__builtin_amdgcn_exp2f(sx[2 * i] - m), __builtin_amdgcn_exp2f(sx[2 * i + 1] - m));
#pragma unroll
        for (int i = 0; i < 16; ++i) o[i] *= f;
        o = mfma32(ldsr<bf16x8>(vb), asbf((u32x4){pk[0], pk[1], pk[2], pk[3]}), o);
        o = mfma32(ldsr<bf16x8>(vb + 512), asbf((u32x4){pk[4], pk[5], pk[6], pk[7]}), o);
        vb += isv ? 1024u : 0u;
      }
      MFMA_GAP32(o);
      l = o[8];
    }
    const float rl = __builtin_amdgcn_rcpf(l);
#pragma unroll
    for (int i = 0; i < 4; ++i) opk[i] = canon(cvtpk(o[2 * i] * rl, o[2 * i + 1] * rl));
  }
  st16(wsr, ox_off(), 0, (u32x4){opk[0], opk[1], opk[2], opk[3]});
  STAMP(9);
  bf16x8 sel[4];
  auto make_sel = [&]() {
    const u32 l = opaque<u32>(lane), e = (l & 1) ? 0x3F800000u : 0x00003F80u;
#pragma unroll
    for (int q = 0; q < 4; ++q) {
      const bool on = ((l >> 2) & 3) == q;
      const u32 d0 = (on && !(l & 2)) ? e : 0u, d1 = (on && (l & 2)) ? e : 0u;
      sel[q] = asbf((u32x4){d0, d1, d0, d1});
    }
  };
  make_sel();
  const u32 gvo = w * 4096 + l16;
  auto pw_of = [&](u32 l) { return L_P + ((l >> 3) & 3) * 1024 + (w >> 1) * 256 + (l & 7) * 32 + (l >> 5) * 16 + (((w ^ (l >> 3)) & 1) * 8); };
  const u32 pw = pw_of(lane);
  const u32 pr = L_P + gvo;
  auto g_load = [&](bf16x8 (&f)[4], u32 src) {
#pragma unroll
    for (int t = 0; t < 4; ++t) f[t] = asbf(ld16(wsr, gvo + t * 1024, src));
  };
  auto g_wait = [&](bf16x8 (&f)[4], u32 src) {
    while (unwritten(f)) {
      if (timed_out(t0)) break;
      __builtin_amdgcn_s_sleep(1);
      FENCE();
      g_load(f, src);
    }
  };
  auto row_stats = [&](int half, const bf16x8 (&f)[4]) {
    float s0 = 0.f, s1 = 0.f, q0 = 0.f, q1 = 0.f;
#pragma unroll
    for (int t = 0; t < 4; ++t) {
      const u32x4 d = asu(f[t]);
      s0 = dot2(d[0], 0x3F803F80u, s0); q0 = dot2(d[0], d[0], q0); s1 = dot2(d[1], 0x3F803F80u, s1); q1 = dot2(d[1], d[1], q1);
      s0 = dot2(d[2], 0x3F803F80u, s0); q0 = dot2(d[2], d[2], q0); s1 = dot2(d[3], 0x3F803F80u, s1); q1 = dot2(d[3], d[3], q1);
    }
    const f32x2 sq = {swap32_add(s0 + s1), swap32_add(q0 + q1)};
    if (lane < 32) ldsw<f32x2>(L_STAT + half * STH + lane * STP + w * 8, sq);
  };
  auto partials = [&](int half, f32x16& acc0, f32x16& acc1, u32 pw) {
#pragma unroll
    for (int a = 0; a < 4; ++a) {
      ldsw<u32x2>(pw + half * 32768 + a * 4096, (u32x2){cvtpk(acc0[4 * a], acc0[4 * a + 1]), cvtpk(acc0[4 * a + 2], acc0[4 * a + 3])});
      ldsw<u32x2>(pw + half * 32768 + (4 + a) * 4096, (u32x2){cvtpk(acc1[4 * a], acc1[4 * a + 1]), cvtpk(acc1[4 * a + 2], acc1[4 * a + 3])});
    }
  };
  auto slice_gemm = [&](int half, const bf16x8 (&a)[8], const bf16x8 (&f)[4]) {
    f32x16 acc0 = {}, acc1 = {};
#pragma unroll
    for (int t = 0; t < 4; ++t) { acc0 = mfma32(a[t], f[t], acc0); acc1 = mfma32(a[4 + t], f[t], acc1); }
    partials(half, acc0, acc1, pw);
  };
  auto reduce_at = [&](int half, u32 pr_) -> f32x4 {
    f32x4 acc = {0, 0, 0, 0};
#pragma unroll
    for (int q = 0; q < 4; ++q) acc = mfma16(sel[q], ldsr<bf16x8>(pr_ + half * 32768 + q * 1024), acc);
    return acc;
  };
  auto reduce = [&](int half) { return reduce_at(half, pr); };
  auto src_of = [&](u32 base, int half) { return base + blk_of(half) * 32768; };
  auto pack4 = [&](f32x4 v) { return (u32x2){canon(cvtpk(v[0], v[1])), canon(cvtpk(v[2], v[3]))}; };
  const u32 a3off = w < 7 ? ((7 * j + w) * 32) * 1024 + l16 : OOB;
  auto a3_load = [&](int t0_, int t1_) {
#pragma unroll
    for (int t = 0; t < 32; ++t) if (t >= t0_ && t < t1_) a3[t] = asbf(ld16(wsr, a3off, WS_W3 + t * 1024));
  };

  f32x4 hres[2], xp[2], cs1;
  bf16x8 fA[4], fB[4];
  auto phase = [&](const bf16x8 (&a)[8], u32 src, bool stats, auto&& first, auto&& epi, auto&& put, auto&& pre, auto&& slotA, auto&& slotB, int sbase) {
    PSTAMP(sbase, 0);
    g_wait(fA, src_of(src, 0));
    STAMP(sbase);
    PSTAMP(sbase, 1);
    if (stats) row_stats(0, fA);
    first();
    PSTAMP(sbase, 2);
    slice_gemm(0, a, fA);
    PSTAMP(sbase, 3);
    g_load(fB, src_of(src, 1));
    slotA();
    PSTAMP(sbase, 4);
    BAR();
    PSTAMP(sbase, 5);
    const f32x4 rA = reduce(0);
    const u32x2 vA = epi(0, rA);
    PSTAMP(sbase, 6);
    put(0, vA);
    PSTAMP(sbase, 7);
    g_wait(fB, src_of(src, 1));
    STAMP(sbase + 1);
    PSTAMP(sbase, 8);
    if (stats) row_stats(1, fB);
    PSTAMP(sbase, 9);
    slice_gemm(1, a, fB);
    PSTAMP(sbase, 10);
    slotB();
    PSTAMP(sbase, 11);
    BAR();
    PSTAMP(sbase, 12);
    const u32x2 vB = epi(1, reduce(1));
    PSTAMP(sbase, 13);
    pre();
    PSTAMP(sbase, 14);
    put(1, vB);
    PSTAMP(sbase, 15);
  };
  auto lane_stat = [&](int half) {
    const u32 sa = L_STAT + half * STH + (lane >> 1) * STP + (lane & 1) * 32;
    const f32x4 p0 = ldsr<f32x4>(sa), p1 = ldsr<f32x4>(sa + 16);
    return finish_stat(xor1_add((p0[0] + p0[2]) + (p1[0] + p1[2])), xor1_add((p0[1] + p0[3]) + (p1[1] + p1[3])));
  };
  const u32 a4off = (2 * j * 112 + 14 * w) * 1024 + l16;
  g_load(fA, src_of(WS_OX, 0));
  {
    float c[2] = {0.f, 0.f};
#pragma unroll
    for (int t = 0; t < 8; ++t) {
      const u32x4 d = asu(a_1[t]);
#pragma unroll
      for (int e = 0; e < 4; ++e) c[t >> 2] = dot2(d[e], 0x3F803F80u, c[t >> 2]);
    }
    const float c0 = swap32_add(c[0]), c1 = swap32_add(c[1]);
    ldsw<float>(L_CS1P + w * 256 + lane * 4, lane < 32 ? c0 : c1);
  }
  phase(a_o, WS_OX, false,
        [&]() { BAR(); },
        [&](int half, f32x4 acc) {
          if (half == 0) {
            cs1 = (f32x4){0.f, 0.f, 0.f, 0.f};
#pragma unroll
            for (int v = 0; v < 8; ++v) cs1 += ldsr<f32x4>(L_CS1P + v * 256 + w * 32 + (lane & 1) * 16);
          }
          hres[half] = acc + (f32x4){bflo(xres[half][0]), bfhi(xres[half][0]), bflo(xres[half][1]), bfhi(xres[half][1])};
          return pack4(hres[half]);
        },
        [&](int half, u32x2 v) { st8(wsr, hx_off(WS_HX, half), 0, v); },
        [&]() { g_load(fA, src_of(WS_HX, 0)); },
        [&]() {
#pragma unroll
          for (int i = 0; i < 7; ++i) dma1(ws + WS_W4 + i * 1024, (((u32)(b >> 6) & 3u) * 8 + w) * 7168 + j * 229376 + l16, lb + L_DUM1);
        },
        [&]() { a3_load(A3_N0, A3_N0 + 5); }, 10);
  STAMP(12);
  phase(a_1, WS_HX, true, [&]() { },
        [&](int half, f32x4 acc) {
          const RowStat st = lane_stat(half);
          const float r2 = __builtin_amdgcn_rsqf(st.var * st.rstd * st.rstd + LN_EPS), rr = st.rstd * r2;
          xp[half] = (hres[half] - st.mu) * st.rstd;
          const f32x4 pre = (acc - st.mu * cs1) * rr;
          return pack4((f32x4){gelu(pre[0]), gelu(pre[1]), gelu(pre[2]), gelu(pre[3])});
        },
        [&](int half, u32x2 v) { st8(wsr, ts_off(half), 0, v); },
        [&]() { g_load(fA, src_of(WS_TS, 0)); },
        [&]() { a3_load(A3_N0 + 5, A3_N0 + 10); },
        [&]() { a3_load(A3_N0 + 10, A3_N0 + 13); dma4_sc1(ws + WS_W4 + 10 * 1024, a4off, lb + L_A4 + w * 8192); }, 13);
  STAMP(15);
  phase(a_2, WS_TS, false, [&]() { },
        [&](int half, f32x4 acc) {
          const f32x4 x1 = xp[half] + acc;
          ldsw<f32x4>(L_RES + half * 8192 + w * 1024 + l16, hres[half] + x1);
          return pack4(x1);
        },
        [&](int half, u32x2 v) { st8(wsr, hx_off(WS_X1, half), 0, v); },
        [&]() { dma4_sc1(ws + src_of(WS_X1, 0), gvo, lb + L_P + w * 4096); },
        [&]() { a3_load(A3_N0 + 13, A3_N0 + 16); dma4_sc1(ws + WS_W4 + 114688 + 10 * 1024, a4off, lb + L_A4 + w * 8192 + 4096); },
        [&]() { a3_load(A3_N0 + 16, 32); }, 16);
  STAMP(18);
  bf16x8 a4[10][2];
  auto a4_load = [&](int n) { a4[n >> 1][n & 1] = asbf(ld16(wsr, a4off, WS_W4 + (n & 1) * 114688 + (n >> 1) * 1024)); };
  u32x4 uf[14];
  auto u_load1 = [&](int half, int t) { uf[t] = ld16(wsr, l16, WS_UX + (blk_of(half) * 112 + w * 14 + t) * 1024); };
  {
    auto x1_finish = [&](int half) {
      for (;;) {
#pragma unroll
        for (int t = 0; t < 4; ++t) fA[t] = ldsr<bf16x8>(pr + half * 32768 + t * 1024);
        if (!unwritten(fA)) break;
        if (timed_out(t0)) break;
        __builtin_amdgcn_s_sleep(1);
        dma4_sc1(ws + src_of(WS_X1, half), gvo, lb + L_P + half * 32768 + w * 4096);
        WAITV(0);
      }
      row_stats(half, fA);
    };
    struct UOut { float rstd, nm; f32x4 cs[2]; float gv[4]; u32 up[8]; u32 l; };
    auto u_stat = [&](UOut& u, int half) {
      u.l = opaque<u32>(lane);
      const u32 sr = L_STAT + half * STH + (u.l & 31) * STP;
      const f32x4 p0 = ldsr<f32x4>(sr), p1 = ldsr<f32x4>(sr + 16), p2 = ldsr<f32x4>(sr + 32), p3 = ldsr<f32x4>(sr + 48);
      const f32x4 p = (p0 + p1) + (p2 + p3);
      const RowStat st = finish_stat(p[0] + p[2], p[1] + p[3]);
      u.rstd = st.rstd; u.nm = -st.mu * st.rstd;
    };
    auto u_cs = [&](UOut& u, int a) { u.cs[a & 1] = ldsr<f32x4>(L_CS + (32 * w + 8 * a) * 4 + (u.l >> 5) * 16); };
    auto u_val = [&](UOut& u, int half, const f32x16& acc, int v) {
      const int a = v >> 2, e = v & 3;
      u.gv[e] = gelu(acc[v] * u.rstd + u.nm * u.cs[a & 1][e]);
      if (e == 3) { u.up[2 * a] = canon(cvtpk(u.gv[0], u.gv[1])); u.up[2 * a + 1] = canon(cvtpk(u.gv[2], u.gv[3])); }
      if (v == 7 || v == 15) {
        const u32 uo = w < 7 ? WS_UX + (blk_of(half) * 112 + j * 14 + 2 * w) * 1024 + u.l * 16 : OOB;
        if (v == 7) st16(wsr, uo, 0, (u32x4){u.up[0], u.up[1], u.up[2], u.up[3]});
        else st16(wsr, uo, 1024, (u32x4){u.up[4], u.up[5], u.up[6], u.up[7]});
      }
    };
    WAITV(1);
    x1_finish(0);
    BAR();
    STAMP(19);
    dma4_sc1(ws + src_of(WS_X1, 1), gvo, lb + L_P + 32768 + w * 4096);
    const u32 xb = L_P + (w < 7 ? l16 : 0u);
    f32x16 accA = {}, accB = {};
    bf16x8 bq[4];
    {
      float c0 = 0.f, c1 = 0.f;
#pragma unroll
      for (int t = 0; t < 4; ++t) bq[t] = ldsr<bf16x8>(xb + t * 1024);
#pragma unroll
      for (int t = 0; t < 32; ++t) {
        accA = mfma32(a3[t], bq[t & 3], accA);
        if (t + 4 < 32) bq[t & 3] = ldsr<bf16x8>(xb + (t + 4) * 1024);
        const u32x4 d = asu(a3[t]);
        c0 = dot2(d[0], 0x3F803F80u, c0); c1 = dot2(d[1], 0x3F803F80u, c1); c0 = dot2(d[2], 0x3F803F80u, c0); c1 = dot2(d[3], 0x3F803F80u, c1);
        if (t >= 4 && (t & 3) == 0 && t / 4 - 1 < A4_N0) a4_load(t / 4 - 1);
        __builtin_amdgcn_sched_barrier(0);
      }
      const float c = swap32_add(c0 + c1);
      if (lane < 32) ldsw<float>(L_CS + w * 128 + lane * 4, c);
    }
    STAMP(20);
    asm volatile("s_waitcnt vmcnt(%0)" :: "n"(A4_N0) : "memory");
    x1_finish(1);
    BAR();
    STAMP(21);
    {
      UOut u;
#pragma unroll
      for (int t = 0; t < 4; ++t) bq[t] = ldsr<bf16x8>(xb + 32768 + t * 1024);
      u_stat(u, 0); u_cs(u, 0);
#pragma unroll
      for (int t = 0; t < 32; ++t) {
        if (t < 16 && (t & 3) == 1 && t + 3 < 16) u_cs(u, (t + 3) >> 2);
        accB = mfma32(a3[t], bq[t & 3], accB);
        if (t + 4 < 32) bq[t & 3] = ldsr<bf16x8>(xb + 32768 + (t + 4) * 1024);
        if (t < 16) u_val(u, 0, accA, t);
        const int n = A4_N0 + t - 1;
        if (t >= 1 && n < 20) a4_load(n);
        __builtin_amdgcn_sched_barrier(0);
      }
    }
    STAMP(22);
    {
      UOut u;
      u_stat(u, 1); u_cs(u, 0);
#pragma unroll
      for (int v = 0; v < 16; ++v) {
        if ((v & 3) == 0 && v + 4 < 16) u_cs(u, (v + 4) >> 2);
        if (v < 14) u_load1(0, v);
        u_val(u, 1, accB, v);
        __builtin_amdgcn_sched_barrier(0);
      }
    }
    STAMP(23);
  }
  {
    auto u_wait = [&](int half) {
      for (;;) {
        u32 m = 0;
#pragma unroll
        for (int t = 0; t < 14; ++t) m = umax(m, max4(uf[t]));
        if (!__any(m == SENT)) break;
        if (timed_out(t0)) break;
        __builtin_amdgcn_s_sleep(2);
        FENCE();
#pragma unroll
        for (int t = 0; t < 14; ++t) u_load1(half, t);
      }
    };
#pragma unroll
    for (int half = 0; half < 2; ++half) {
      u_wait(half);
      STAMP(24 + half);
      const u32 sa = L_A4 + w * 8192 + opaque<u32>(l16);
      f32x16 acc0 = {}, acc1 = {};
      bf16x8 sq[4][2];
#pragma unroll
      for (int t = 0; t < 14; ++t) {
        if (t >= 8 && t < 12) { sq[t - 8][0] = ldsr<bf16x8>(sa + (t - 8) * 1024); sq[t - 8][1] = ldsr<bf16x8>(sa + 4096 + (t - 8) * 1024); }
        acc0 = mfma32(t < 10 ? a4[t < 10 ? t : 0][0] : sq[t < 10 ? 0 : t - 10][0], asbf(uf[t]), acc0);
        acc1 = mfma32(t < 10 ? a4[t < 10 ? t : 0][1] : sq[t < 10 ? 0 : t - 10][1], asbf(uf[t]), acc1);
        if (half == 0 && t >= 7) { u_load1(1, 2 * (t - 7)); u_load1(1, 2 * (t - 7) + 1); }
        __builtin_amdgcn_sched_barrier(0);
      }
      if (half == 0) BAR();
      partials(half, acc0, acc1, pw_of(opaque<u32>(lane)));
      FENCE();
    }
    STAMP(26);
    make_sel();
    BAR();
#pragma unroll
    for (int half = 0; half < 2; ++half) {
      const u32 l = opaque<u32>(lane);
      const f32x4 v = reduce_at(half, L_P + w * 4096 + l * 16) + ldsr<f32x4>(L_RES + half * 8192 + w * 1024 + l * 16);
      *(GAS u32x2*)((char*)out + (u32)((blk_of(half) * 32 + (l >> 1)) * 1024 + (64 * j + 8 * w + 4 * (l & 1)) * 2)) = (u32x2){cvtpk(v[0], v[1]), cvtpk(v[2], v[3])};
    }
  }
  STAMP(27);
}

extern "C" void kernel_launch(void* const* d_in, const int* in_sizes, int n_in, void* d_out, int out_size, void* d_ws, size_t ws_size, hipStream_t stream) {
  (void)in_sizes; (void)n_in; (void)out_size; (void)ws_size;
  fused_block_kernel<<<256, 512, 0, stream>>>((const u16*)d_in[0], (const u16*)d_in[1], (const u16*)d_in[2], (const u16*)d_in[3], (const u16*)d_in[4],
                                              (const u16*)d_in[6], (const u16*)d_in[7], (const u16*)d_in[8], (const u16*)d_in[9], (u16*)d_out, (char*)d_ws);
}
```

```cpp
#include <hip/hip_runtime.h>
#include <stdint.h>
#include <type_traits>

typedef unsigned short u16;
typedef unsigned int u32;
typedef unsigned long long u64;
typedef short bf16x8 __attribute__((ext_vector_type(8)));
typedef short s16x4 __attribute__((ext_vector_type(4)));
typedef float f32x2 __attribute__((ext_vector_type(2)));
typedef float f32x4 __attribute__((ext_vector_type(4)));
typedef float f32x16 __attribute__((ext_vector_type(16)));
typedef u32 u32x2 __attribute__((ext_vector_type(2)));
typedef u32 u32x4 __attribute__((ext_vector_type(4)));
typedef __bf16 bf16x2_t __attribute__((ext_vector_type(2)));
#define LAS __attribute__((address_space(3)))
#define GAS __attribute__((address_space(1)))
#define DI __device__ __forceinline__

constexpr u32 WS_EPOCH = 0x0;
constexpr u32 WS_FKV = 0x1000;
constexpr u32 WS_FW = 0x5000;
constexpr u32 WS_K = 0x10000;
constexpr u32 WS_V = 0x20000;
constexpr u32 WS_DBG = 0x1E00000;
constexpr u32 WS_WO = 0x100000, WS_W1 = 0x180000, WS_W2 = 0x200000, WS_W3 = 0x280000, WS_W4 = 0x440000;
constexpr u32 WS_OX = 0x600000, WS_HX = 0x800000, WS_TS = 0xA00000, WS_X1 = 0xC00000, WS_UX = 0xE00000;
constexpr u32 WS_STAMP = 0x2000000;
constexpr u32 WS_END = 0x2100000;
constexpr u32 OOB = 0x80000000u;

constexpr u32 XP = 1040;
constexpr u32 L_XIMG = 0;
constexpr u32 L_WQ = 66560;
constexpr u32 L_WKV = 132096;
constexpr u32 S_PM = 148480, S_PC = 156672, S_PS = 157184, S_PQ = 157696, S_FIN = 158208, S_QS = 158336, S_QC = 159360;
constexpr u32 L_DUM1 = 160384;
constexpr u32 L_CNT = 161408;
constexpr u32 L_K = 0, L_V = 65536, L_ONES = 131072;
constexpr u32 L_STG = 135424;
constexpr u32 L_CS1P = 155904;
constexpr u32 L_P = 0;
constexpr u32 L_STAT = 65536;
constexpr u32 STP = 80, STH = 2560;
constexpr u32 L_CS = 71680;
constexpr u32 L_RES = 73728;
constexpr u32 L_A4 = 90112;
constexpr u32 L_STAMP = 161792;
constexpr u32 LDS_BYTES = 163840;

__shared__ __attribute__((aligned(16))) char smem[LDS_BYTES];

constexpr u32 SENT = 0xFFFFFFFFu;
constexpr float LN_EPS = 1e-5f;
constexpr float QSCALE = 0.25f * 1.4426950408889634f;
#define CODE_KB 43

DI f32x16 mfma32(bf16x8 a, bf16x8 b, f32x16 c) { return __builtin_amdgcn_mfma_f32_32x32x16_bf16(a, b, c, 0, 0, 0); }
DI f32x4 mfma16(bf16x8 a, bf16x8 b, f32x4 c) { return __builtin_amdgcn_mfma_f32_16x16x32_bf16(a, b, c, 0, 0, 0); }
DI u32 cvtpk(float lo, float hi) { f32x2 v = {lo, hi}; bf16x2_t b = __builtin_convertvector(v, bf16x2_t); return __builtin_bit_cast(u32, b); }
DI u32 canon(u32 d) { return d < 0xFFFFFFFEu ? d : 0xFFFFFFFEu; }
DI float bflo(u32 d) { return __uint_as_float(d << 16); }
DI float bfhi(u32 d) { return __uint_as_float(d & 0xFFFF0000u); }
DI float dot2(u32 a, u32 b, float c) { return __builtin_amdgcn_fdot2_f32_bf16(__builtin_bit_cast(bf16x2_t, a), __builtin_bit_cast(bf16x2_t, b), c, false); }
DI float gelu(float x) {
  const float c1 = -2.0f * 0.7978845608028654f * 1.4426950408889634f, c2 = c1 * 0.044715f;
  const float e = __builtin_amdgcn_exp2f(x * (c1 + c2 * x * x));
  return x * __builtin_amdgcn_rcpf(1.0f + e);
}
DI float swap32_add(float v) {
  const auto r = __builtin_amdgcn_permlane32_swap(__float_as_uint(v), __float_as_uint(v), false, false);
  return __uint_as_float(r[0]) + __uint_as_float(r[1]);
}
DI float xor1_add(float v) { return v + __int_as_float(__builtin_amdgcn_mov_dpp(__float_as_int(v), 0xB1, 0xF, 0xF, true)); }

#define BAR() asm volatile("s_waitcnt lgkmcnt(0)\n\ts_barrier" ::: "memory")
#define WAITV(n) asm volatile("s_waitcnt vmcnt(" #n ")" ::: "memory")
#define FENCE() asm volatile("" ::: "memory")
template <class T> DI T opaque(T v) { asm volatile("" : "+v"(v)); return v; }

template <class T> DI T ldsr(u32 off) { return *(const LAS T*)((LAS char*)smem + off); }
template <class T> DI void ldsw(u32 off, T v) { *(LAS T*)((LAS char*)smem + off) = v; }
DI s16x4 ldstr(u32 off) {
  typedef short v4i16_t __attribute__((ext_vector_type(4)));
  return __builtin_bit_cast(s16x4, __builtin_amdgcn_ds_read_tr16_b64_v4i16((LAS v4i16_t*)((LAS char*)smem + off)));
}
DI bf16x8 cat(s16x4 a, s16x4 b) { return __builtin_shufflevector(a, b, 0, 1, 2, 3, 4, 5, 6, 7); }
DI u32 lds_base() { return (u32)(uintptr_t)(LAS char*)smem; }

#define DMA_HEAD "s_mov_b32 %[k], m0\n\ts_mov_b32 m0, %[lds]\n\ts_nop 0\n\t"
#define DMA_TAIL "s_mov_b32 m0, %[k]"
DI void dma1(const void* sbase, u32 voff, u32 ldsdst) {
  u32 keep;
  asm volatile(DMA_HEAD "global_load_lds_dwordx4 %[v], %[s]\n\t" DMA_TAIL : [k] "=&s"(keep) : [v] "v"(voff), [s] "s"(sbase), [lds] "s"(ldsdst) : "memory");
}
DI void dma2(const void* sbase, u32 voff, u32 ldsdst) {
  u32 keep;
  asm volatile(DMA_HEAD "global_load_lds_dwordx4 %[v], %[s]\n\tglobal_load_lds_dwordx4 %[v], %[s] offset:1024\n\t" DMA_TAIL
               : [k] "=&s"(keep) : [v] "v"(voff), [s] "s"(sbase), [lds] "s"(ldsdst) : "memory");
}
DI void dma4_rows(const void* sbase, u32 v0, u32 v1, u32 v2, u32 v3, u32 ldsdst) {
  u32 keep;
  asm volatile(DMA_HEAD "global_load_lds_dwordx4 %[a], %[s]\n\tglobal_load_lds_dwordx4 %[b], %[s] offset:1040\n\t"
               "global_load_lds_dwordx4 %[c], %[s] offset:2080\n\tglobal_load_lds_dwordx4 %[d], %[s] offset:3120\n\t" DMA_TAIL
               : [k] "=&s"(keep) : [a] "v"(v0), [b] "v"(v1), [c] "v"(v2), [d] "v"(v3), [s] "s"(sbase), [lds] "s"(ldsdst) : "memory");
}
DI void dma4_sc1(const void* sbase, u32 voff, u32 ldsdst) {
  u32 keep;
  asm volatile(DMA_HEAD "global_load_lds_dwordx4 %[v], %[s] sc1\n\tglobal_load_lds_dwordx4 %[v], %[s] offset:1024 sc1\n\t"
               "global_load_lds_dwordx4 %[v], %[s] offset:2048 sc1\n\tglobal_load_lds_dwordx4 %[v], %[s] offset:3072 sc1\n\t" DMA_TAIL
               : [k] "=&s"(keep) : [v] "v"(voff), [s] "s"(sbase), [lds] "s"(ldsdst) : "memory");
}
#define DMA_STEP(SC) "global_load_lds_dwordx4 %[v], %[s]" SC "\n\ts_add_u32 m0, m0, %[ls]\n\tv_add_u32 %[v], %[gs], %[v]\n\t"
template <int N, bool SC1, u32 GS>
DI void dma_strided(const void* sbase, u32 voff, u32 ldsdst, u32 lstep) {
  u32 keep;
  static_assert(N == 2 || N == 3 || N == 4 || N == 8, "");
#define DMA_RUN(BODY) asm volatile(DMA_HEAD BODY DMA_TAIL : [k] "=&s"(keep), [v] "+v"(voff) : [s] "s"(sbase), [lds] "s"(ldsdst), [ls] "s"(lstep), [gs] "n"(GS) : "memory", "scc")
  if constexpr (SC1) {
    if constexpr (N == 2) DMA_RUN(DMA_STEP(" sc1") DMA_STEP(" sc1"));
    if constexpr (N == 3) DMA_RUN(DMA_STEP(" sc1") DMA_STEP(" sc1") DMA_STEP(" sc1"));
    if constexpr (N == 4) DMA_RUN(DMA_STEP(" sc1") DMA_STEP(" sc1") DMA_STEP(" sc1") DMA_STEP(" sc1"));
  } else {
    if constexpr (N == 8) DMA_RUN(DMA_STEP("") DMA_STEP("") DMA_STEP("") DMA_STEP("") DMA_STEP("") DMA_STEP("") DMA_STEP("") DMA_STEP(""));
  }
#undef DMA_RUN
}

typedef __amdgpu_buffer_rsrc_t rsrc_t;
DI rsrc_t mkrsrc(const void* p, u32 bytes) { return __builtin_amdgcn_make_buffer_rsrc((void*)p, 0, bytes, 0x00020000); }
DI u32x4 ld16(rsrc_t r, u32 voff, u32 soff) { return __builtin_amdgcn_raw_buffer_load_b128(r, voff, soff, 16); }
DI void st16(rsrc_t r, u32 voff, u32 soff, u32x4 v) { __builtin_amdgcn_raw_buffer_store_b128(v, r, voff, soff, 16); }
DI void st8(rsrc_t r, u32 voff, u32 soff, u32x2 v) { __builtin_amdgcn_raw_buffer_store_b64(v, r, voff, soff, 16); }
DI void st4(rsrc_t r, u32 voff, u32 soff, u32 v) { __builtin_amdgcn_raw_buffer_store_b32(v, r, voff, soff, 16); }
DI bf16x8 asbf(u32x4 v) { return __builtin_bit_cast(bf16x8, v); }
DI u32x4 asu(bf16x8 v) { return __builtin_bit_cast(u32x4, v); }
DI u32 umax(u32 a, u32 b) { return a > b ? a : b; }
DI u32 max4(u32x4 v) { return umax(umax(v[0], v[1]), umax(v[2], v[3])); }

constexpr u64 SPIN_TICKS = 400000ull;
DI bool timed_out(u64 t0) { return __builtin_amdgcn_s_memrealtime() - t0 > SPIN_TICKS; }

#define STAMP(i) do { } while (0)
#define ASTAMP(it, pos) do { } while (0)
#define PSTAMP(sb, i) do { } while (0)
#define MFMA_GAP16(v) asm volatile("s_nop 7" : "+v"(v))
#define MFMA_GAP32(v) asm volatile("s_nop 7\n\ts_nop 3" : "+v"(v))

DI bool flags_ok(u32x4 a, u32x4 b, u32 lo, u32 hi) {
  return __all(a[0] == lo && a[1] == hi && a[2] == lo && a[3] == hi && b[0] == lo && b[1] == hi && b[2] == lo && b[3] == hi);
}
DI void wait_flags(rsrc_t wsr, u32 base, u32 taglo, u32 taghi, int lane, u64 t0) {
  for (;;) {
    const u32x4 a = ld16(wsr, lane * 32, base), b = ld16(wsr, lane * 32 + 16, base);
    if (flags_ok(a, b, taglo, taghi)) break;
    if (timed_out(t0)) break;
    __builtin_amdgcn_s_sleep(1);
    FENCE();
  }
}

DI bf16x8 ones_frag() { return asbf((u32x4){0x3F803F80u, 0x3F803F80u, 0x3F803F80u, 0x3F803F80u}); }
DI float diag_pick(f32x4 g, int lane) { const int r = lane & 3; float a = (r & 1) ? g[1] : g[0], b = (r & 1) ? g[3] : g[2]; return (r & 2) ? b : a; }

struct RowStat { float mu, rstd, var; };
DI RowStat finish_stat(float s, float q) {
  RowStat r; r.mu = s * (1.0f / 512.0f);
  r.var = fmaxf(q * (1.0f / 512.0f) - r.mu * r.mu, 0.0f);
  r.rstd = __builtin_amdgcn_rsqf(r.var + LN_EPS);
  return r;
}
DI bool unwritten(const bf16x8 (&f)[4]) {
  return __any(umax(umax(max4(asu(f[0])), max4(asu(f[1]))), umax(max4(asu(f[2])), max4(asu(f[3])))) == SENT);
}

template <int I, int N, class F> DI void static_for(F&& f) {
  if constexpr (I < N) { f(std::integral_constant<int, I>{}); static_for<I + 1, N>(f); }
}
struct Job { const char* src; u32 prow; bool perm; };
DI Job retile_job(u32 f, u64 wo, u64 w1, u64 w2, u64 w3, u64 w4) {
  const bool big = f >= 1536, last = f >= 3328;
  const u32 g = f - 3328, T = last ? g / 112 : big ? (f - 1536) >> 5 : (f >> 5) & 15, ks = last ? g % 112 : f & 31;
  const u32 prow = big && !last ? 3584 : 1024;
  u64 m = f < 1024 ? w1 : w2;
  m = f < 512 ? wo : m; m = big ? w3 : m; m = last ? w4 : m;
  return Job{(const char*)(m + (u64)(16 * ks * prow + T * 64)), prow, last || f < 512};
}

#define A3_N0 6
#define A4_N0 6

__global__ __launch_bounds__(512, 2) void fused_block_kernel(const u16* __restrict__ x, const u16* __restrict__ wq, const u16* __restrict__ wk,
                                                             const u16* __restrict__ wv, const u16* __restrict__ wo, const u16* __restrict__ w1,
                                                             const u16* __restrict__ w2, const u16* __restrict__ w3, const u16* __restrict__ w4,
                                                             u16* __restrict__ out, char* ws) {
  const int tid = threadIdx.x, lane = tid & 63, w = __builtin_amdgcn_readfirstlane(tid >> 6);
  const int b = blockIdx.x;
  const int cl = (b & 7) * 4 + (b >> 6), j = (b >> 3) & 7;
  const u32 lb = lds_base();
  const u32 l16 = lane * 16;
  {
    u64 pc;
    asm volatile("s_getpc_b64 %0" : "=s"(pc));
    dma1((const void*)(pc & ~63ull), (u32)((((b >> 3) * 8 + w) % CODE_KB) * 1024) + l16, lb + L_DUM1);
  }
  asm volatile("" :: "s"(x), "s"(wq), "s"(wk), "s"(wv), "s"(wo), "s"(w1), "s"(w2), "s"(w3), "s"(w4), "s"(out), "s"(ws));
  const u64 t0 = __builtin_amdgcn_s_memrealtime();
  const int i16 = lane & 15, g4 = lane >> 4, r32 = lane & 31, h2 = lane >> 5;
  STAMP(0);

  const int kvrt = j >> 1, kvmat = j & 1;
  auto grow = [&](int rho) { return (rho < 32 ? 0 : 1024 - 32) + 32 * cl + rho; };
  const int R0 = grow(16 * kvrt);
  u32x4 xk0, xk1;
  u64 tag;
  {
    const u32 xo = (u32)(i16 * 1024 + g4 * 32);
    asm volatile("global_load_dwordx4 %0, %2, %3\n\tglobal_load_dwordx4 %1, %2, %3 offset:16"
                 : "=&v"(xk0), "=&v"(xk1) : "v"(xo), "s"((const char*)x + R0 * 1024 + w * 128) : "memory");
    dma2((const char*)(kvmat ? wv : wk) + w * 2048, l16, lb + L_WKV + w * 2048);
    asm volatile("s_load_dwordx2 %0, %1, 0x0" : "=s"(tag) : "s"(ws + WS_EPOCH) : "memory");
    const char* xr = (const char*)x + grow(8 * w) * 1024 - 48;
    dma4_rows(xr, l16 + 48, l16 + 32, l16 + 16, l16, lb + L_XIMG + 8 * w * XP);
    dma4_rows(xr + 4096, l16 + 48, l16 + 32, l16 + 16, l16, lb + L_XIMG + (8 * w + 4) * XP);
    dma_strided<8, false, 0x2000>((const char*)wq + w * 65536 + j * 128, (u32)((lane >> 3) * 1024 + (((lane & 7) ^ (4 * ((lane >> 4) & 1))) * 16)), lb + L_WQ + w * 8192, 1024);
    asm volatile("s_waitcnt vmcnt(16) lgkmcnt(0)" : "+v"(xk0), "+v"(xk1), "+s"(tag) :: "memory");
  }
  tag += 1ull;
  const u32 taglo = (u32)tag, taghi = (u32)(tag >> 32);
  const rsrc_t wsr = mkrsrc(ws, WS_END);
  const bf16x8 one = ones_frag();
  STAMP(1);

  {
    f32x4 am = {0, 0, 0, 0}, ac = {0, 0, 0, 0}, as = {0, 0, 0, 0}, aq = {0, 0, 0, 0};
    const u32 wa = L_WKV + w * 2048 + (16 * g4 + (i16 >> 2)) * 32 + (i16 & 3) * 8;
    const bf16x8 wf0 = cat(ldstr(wa), ldstr(wa + 128)), wf1 = cat(ldstr(wa + 256), ldstr(wa + 384));
    const bf16x8 xf0 = asbf(xk0), xf1 = asbf(xk1);
    as = mfma16(one, xf0, as); aq = mfma16(xf0, xf0, aq); as = mfma16(one, xf1, as); aq = mfma16(xf1, xf1, aq);
    if (kvmat) {
      am = mfma16(xf0, wf0, am); ac = mfma16(one, wf0, ac); am = mfma16(xf1, wf1, am); ac = mfma16(one, wf1, ac);
      MFMA_GAP16(am); MFMA_GAP16(ac);
      if (lane < 16) ldsw<float>(S_PC + (w * 16 + lane) * 4, ac[0]);
    } else {
      am = mfma16(wf0, xf0, am); ac = mfma16(wf0, one, ac); am = mfma16(wf1, xf1, am); ac = mfma16(wf1, one, ac);
      MFMA_GAP16(am); MFMA_GAP16(ac);
      if (i16 == 0) ldsw<f32x4>(S_PC + (w * 16 + 4 * g4) * 4, ac);
    }
    ldsw<f32x4>(S_PM + w * 1024 + l16, am);
    if (lane < 16) ldsw<float>(S_PS + (lane * 8 + w) * 4, as[0]);
    if ((i16 >> 2) == g4) ldsw<float>(S_PQ + (i16 * 8 + w) * 4, diag_pick(aq, lane));
    BAR();
  }
  if (w == 0) {
    {
      const f32x4 s0 = ldsr<f32x4>(S_PS + i16 * 32), s1 = ldsr<f32x4>(S_PS + i16 * 32 + 16);
      const f32x4 q0 = ldsr<f32x4>(S_PQ + i16 * 32), q1 = ldsr<f32x4>(S_PQ + i16 * 32 + 16);
      const f32x4 s = s0 + s1, q = q0 + q1;
      const RowStat st = finish_stat((s[0] + s[1]) + (s[2] + s[3]), (q[0] + q[1]) + (q[2] + q[3]));
      if (lane < 16) { ldsw<float>(S_FIN + lane * 4, st.mu); ldsw<float>(S_FIN + 64 + lane * 4, st.rstd); }
    }
    f32x4 m = {0, 0, 0, 0};
#pragma unroll
    for (int ww = 0; ww < 8; ++ww) m += ldsr<f32x4>(S_PM + ww * 1024 + l16);
    f32x4 v;
    u32 off;
    if (kvmat) {
      float c1 = 0.f;
#pragma unroll
      for (int ww = 0; ww < 8; ++ww) c1 += ldsr<float>(S_PC + (ww * 16 + i16) * 4);
      asm volatile("s_waitcnt lgkmcnt(0)" ::: "memory");
      const f32x4 mu = ldsr<f32x4>(S_FIN + g4 * 16), rs = ldsr<f32x4>(S_FIN + 64 + g4 * 16);
      v = rs * (m - mu * c1);
      off = WS_V + (R0 >> 5) * 1024 + ((R0 >> 4) & 1) * 512 + (g4 & 1) * 256 + i16 * 16 + (g4 >> 1) * 8;
    } else {
      f32x4 c4 = {0, 0, 0, 0};
#pragma unroll
      for (int ww = 0; ww < 8; ++ww) c4 += ldsr<f32x4>(S_PC + (ww * 16 + 4 * g4) * 4);
      asm volatile("s_waitcnt lgkmcnt(0)" ::: "memory");
      const float mu = ldsr<float>(S_FIN + i16 * 4), rs = ldsr<float>(S_FIN + 64 + i16 * 4);
      v = rs * (m - mu * c4);
      off = WS_K + (R0 >> 5) * 1024 + (g4 & 1) * 512 + ((R0 & 31) + i16) * 16 + (g4 >> 1) * 8;
    }
    st8(wsr, off, 0, (u32x2){cvtpk(v[0], v[1]), cvtpk(v[2], v[3])});
    WAITV(1);
  } else {
    WAITV(0);
  }
#define JOB(i) retile_job((u32)(b * 20 + w * 5 + (i)), (u64)wo, (u64)w1, (u64)w2, (u64)w3, (u64)w4)
  u32x4 rts[5];
  if (w < 4) {
#pragma unroll
    for (int i = 0; i < 5; ++i) {
      const Job jb = JOB(i);
      rts[i] = *(const GAS u32x4*)(jb.src + (u32)((lane >> 2) * jb.prow + (lane & 3) * 16));
    }
  }
  FENCE();
  STAMP(2);
  const int q_hp = w & 1, q_rt = (w >> 1) & 1, q_kh = w >> 2;
  const int head = 2 * q_hp + q_kh;
  bf16x8 qf;
  {
    BAR();
    STAMP(3);
    {
      f32x4 as = {0, 0, 0, 0}, aq = {0, 0, 0, 0};
      const int rt = w & 3, kh = w >> 2;
      const u32 sa = L_XIMG + (16 * rt + i16) * XP + kh * 512 + g4 * 16;
#pragma unroll
      for (int t = 0; t < 8; ++t) {
        const bf16x8 xf = ldsr<bf16x8>(sa + t * 64);
        as = mfma16(one, xf, as); aq = mfma16(xf, xf, aq);
      }
      MFMA_GAP16(as); MFMA_GAP16(aq);
      if (lane < 16) ldsw<float>(S_QS + ((16 * rt + lane) * 2 + kh) * 4, as[0]);
      if ((i16 >> 2) == g4) ldsw<float>(S_QS + 512 + ((16 * rt + i16) * 2 + kh) * 4, diag_pick(aq, lane));
    }
    f32x16 acc = {}, accc = {};
    const int q = i16 >> 2, p = i16 & 3, cb = g4 & 1;
    const u32 wa = L_WQ + (256 * q_kh + 8 * h2 + q) * 128 + (((4 * q_hp + 2 * cb + (p >> 1)) ^ (4 * ((q >> 1) & 1))) * 16) + (p & 1) * 8;
    const u32 xa = L_XIMG + (32 * q_rt + r32) * XP + q_kh * 512 + h2 * 16;
#pragma unroll
    for (int t = 0; t < 16; ++t) {
      const bf16x8 af = cat(ldstr(wa + t * 2048), ldstr(wa + t * 2048 + 512));
      acc = mfma32(af, ldsr<bf16x8>(xa + t * 32), acc);
    }
    const u32 wb = wa + q_rt * 16384;
#pragma unroll
    for (int t = 0; t < 8; ++t) accc = mfma32(cat(ldstr(wb + t * 2048), ldstr(wb + t * 2048 + 512)), one, accc);
    if (w == 0) {
      WAITV(5);
      if (lane < 8) __hip_atomic_store((GAS u64*)(ws + WS_FKV + lane * 2048 + b * 8), tag, __ATOMIC_RELAXED, __HIP_MEMORY_SCOPE_AGENT);
    }
    MFMA_GAP32(acc); MFMA_GAP32(accc);
    f32x4 k0, k1, s0, s1;
    if (q_kh) {
      k0 = (f32x4){acc[8], acc[9], acc[10], acc[11]}; k1 = (f32x4){acc[12], acc[13], acc[14], acc[15]};
      s0 = (f32x4){acc[0], acc[1], acc[2], acc[3]}; s1 = (f32x4){acc[4], acc[5], acc[6], acc[7]};
    } else {
      k0 = (f32x4){acc[0], acc[1], acc[2], acc[3]}; k1 = (f32x4){acc[4], acc[5], acc[6], acc[7]};
      s0 = (f32x4){acc[8], acc[9], acc[10], acc[11]}; s1 = (f32x4){acc[12], acc[13], acc[14], acc[15]};
    }
    ldsw<f32x4>(L_WKV + w * 2048 + l16, s0); ldsw<f32x4>(L_WKV + w * 2048 + 1024 + l16, s1);
    if (r32 == 0) {
#pragma unroll
      for (int a = 0; a < 4; ++a) ldsw<f32x4>(S_QC + (((q_hp * 2 + q_rt) * 2 + q_kh) * 32 + 8 * a + 4 * h2) * 4, (f32x4){accc[4 * a], accc[4 * a + 1], accc[4 * a + 2], accc[4 * a + 3]});
    }
    BAR();
    k0 += ldsr<f32x4>(L_WKV + (w ^ 4) * 2048 + l16); k1 += ldsr<f32x4>(L_WKV + (w ^ 4) * 2048 + 1024 + l16);
    f32x4 c0 = {0, 0, 0, 0}, c1 = {0, 0, 0, 0};
#pragma unroll
    for (int s = 0; s < 4; ++s) {
      c0 += ldsr<f32x4>(S_QC + ((q_hp * 4 + s) * 32 + 16 * q_kh + 4 * h2) * 4);
      c1 += ldsr<f32x4>(S_QC + ((q_hp * 4 + s) * 32 + 16 * q_kh + 8 + 4 * h2) * 4);
    }
    const f32x2 sp = ldsr<f32x2>(S_QS + (32 * q_rt + r32) * 8), qp = ldsr<f32x2>(S_QS + 512 + (32 * q_rt + r32) * 8);
    const RowStat st = finish_stat(sp[0] + sp[1], qp[0] + qp[1]);
    const float sc = st.rstd * QSCALE;
    k0 = (k0 - st.mu * c0) * sc; k1 = (k1 - st.mu * c1) * sc;
    qf = opaque(asbf((u32x4){cvtpk(k0[0], k0[1]), cvtpk(k0[2], k0[3]), cvtpk(k1[0], k1[1]), cvtpk(k1[2], k1[3])}));
  }
  STAMP(4);
  auto blk_of = [&](int half) { return half * 32 + cl; };
  const int dlt = (j >> 1) == 0 ? 1 : (j >> 1) == 1 ? -1 : (j >> 1) == 2 ? 2 : -2;
  const u32 pc_in = (u32)((4 * j + (w >> 1)) * 1024 + (w & 1) * 512);
  auto hx_off = [&](u32 base, int half) { return base + blk_of(half) * 32768 + pc_in + lane * 8; };
  auto ts_off = [&](int half) {
    const u32 l = opaque<u32>(lane);
    const u32 R = (u32)(blk_of(half) * 32 + (int)(l >> 1) + dlt);
    return R < 2048u ? WS_TS + (R >> 5) * 32768 + pc_in + (R & 31) * 16 + (l & 1) * 8 : OOB;
  };
  auto ox_off = [&]() { return WS_OX + blk_of(q_rt) * 32768 + (4 * j + head) * 1024 + l16; };
  {
    if (w == 1) {
      wait_flags(wsr, WS_FKV + (b & 7) * 2048, taglo, taghi, lane, t0);
      if (b == 0 && lane == 0) __hip_atomic_store((GAS u64*)(ws + WS_EPOCH), tag, __ATOMIC_RELAXED, __HIP_MEMORY_SCOPE_AGENT);
    }
    BAR();
    STAMP(5);
    dma_strided<3, true, 0x2000>(ws + WS_K, w * 1024 + l16, lb + L_K + w * 1024, 8192);
    dma_strided<2, true, 0x2000>(ws + WS_V, w * 1024 + l16, lb + L_V + w * 1024, 8192);
    if (tid < 272) ldsw<u32x4>(L_ONES + tid * 16, (u32x4){0x3F803F80u, 0x3F803F80u, 0x3F803F80u, 0x3F803F80u});
    if (tid == 511) ldsw<u32x2>(L_CNT, (u32x2){0u, 0u});
    WAITV(0);
    BAR();
    STAMP(6);
  }
  u32 opk[4];
  bf16x8 a_o[8], a_1[8], a_2[8], a3[32];
  u32x2 xres[2];
  {
    u32 ka = L_K + l16;
    const bool isv = r32 < 16;
    u32 va = isv ? L_V + h2 * 256 + r32 * 16 : L_ONES + ((r32 >= 20 && r32 < 28) ? 64 : 0);
    const u32 vinc = isv ? 4096u : 0u;
    f32x16 o = {};
    f32x16 negm;
    {
      const bf16x8 kf = ldsr<bf16x8>(ka);
      f32x16 z = {};
      const f32x16 s = mfma32(kf, qf, z);
      float m = fmaxf(fmaxf(s[0], s[1]), fmaxf(s[2], s[3]));
#pragma unroll
      for (int i = 4; i < 16; i += 2) m = fmaxf(m, fmaxf(s[i], s[i + 1]));
      const auto rr = __builtin_amdgcn_permlane32_swap(__float_as_uint(m), __float_as_uint(m), false, false);
      m = fmaxf(__uint_as_float(rr[0]), __uint_as_float(rr[1]));
#pragma unroll
      for (int i = 0; i < 16; ++i) negm[i] = -m;
    }
    f32x16 s = mfma32(ldsr<bf16x8>(ka), qf, negm);
    auto tiles4 = [&]() {
#pragma unroll
      for (int u = 0; u < 4; ++u) {
        const bf16x8 v0 = ldsr<bf16x8>(va + u * 1024), v1 = ldsr<bf16x8>(va + u * 1024 + 512);
        const bf16x8 kn = ldsr<bf16x8>(ka + (u + 1) * 1024);
        const f32x16 sn = mfma32(kn, qf, negm);
        u32 pk[8];
#pragma unroll
        for (int i = 0; i < 8; ++i) pk[i] = cvtpk(__builtin_amdgcn_exp2f(s[2 * i]), __builtin_amdgcn_exp2f(s[2 * i + 1]));
        o = mfma32(v0, asbf((u32x4){pk[0], pk[1], pk[2], pk[3]}), o);
        o = mfma32(v1, asbf((u32x4){pk[4], pk[5], pk[6], pk[7]}), o);
        s = sn;
      }
      va += vinc; ka += 4096;
    };
    if (w < 4) __builtin_amdgcn_s_setprio(2); else __builtin_amdgcn_s_setprio(1);
    auto kv_rest = [&](u32 img, u32 limg, int c0, auto n) {
      dma_strided<n.value, true, 4096>(ws + img + c0 * 8192, opaque<u32>((w & 3) * 1024 + l16), lb + limg + c0 * 8192 + (w & 3) * 1024, 4096);
    };
    using N2 = std::integral_constant<int, 2>; using N4 = std::integral_constant<int, 4>;
    auto lds_count = [&](u32 off) {
      u32 n;
      asm volatile("ds_add_rtn_u32 %0, %1, %2\n\ts_waitcnt lgkmcnt(0)" : "=v"(n) : "v"(lb + off), "v"(lane == 0 ? 1u : 0u) : "memory");
      return (u32)__builtin_amdgcn_readfirstlane(n);
    };
    const u32x4 s4 = {SENT, SENT, SENT, SENT};
    u32x4 fa, fb;
    auto fetch = [&](int p, u32 l) {
      const u32 fo = (2 * j * 32 + 4 * w) * 1024;
      if (p < 24) {
        const int t = p & 7;
        const bf16x8 v = asbf(ld16(wsr, l + (t & 3) * 1024, (p < 8 ? WS_WO : p < 16 ? WS_W1 : WS_W2) + fo + (t >> 2) * 32768));
        if (p < 8) a_o[t] = v; else if (p < 16) a_1[t] = v; else a_2[t] = v;
      } else if (p < 26) {
        xres[p - 24] = *(const GAS u32x2*)((const char*)x + (u32)((blk_of(p - 24) * 32 + (l >> 5)) * 1024 + (64 * j + 8 * w) * 2 + ((l >> 4) & 1) * 8));
      } else if (p < 26 + A3_N0) {
        a3[p - 26] = asbf(ld16(wsr, w < 7 ? ((7 * j + w) * 32) * 1024 + l : OOB, WS_W3 + (p - 26) * 1024));
      } else if (p < 33 + A3_N0) {
        dma1(ws + WS_W3 + (p - 26 - A3_N0) * 1024, (((u32)(b >> 6) & 3u) * 8 + w) * 7168 + j * 229376 + l, lb + L_DUM1);
      }
    };
    static_for<0, 16>([&](auto IT) {
      constexpr int it = IT.value;
      ASTAMP(it, 0);
      if (w < 4) {
        if (it == 0) {
          const u32 l = opaque<u32>(lane);
          const u32 sb = L_STG + w * 5120;
#pragma unroll
          for (int i = 0; i < 5; ++i) ldsw<u32x4>(sb + i * 1024 + l * 16, rts[i]);
          const u32 tb = sb + ((l & 15) >> 2) * 64 + ((l >> 4) & 1) * 32 + (l & 3) * 8;
#pragma unroll
          for (int i = 0; i < 5; ++i) {
            const bool perm = JOB(i).perm;
            const u32 t0a = tb + i * 1024 + (l >> 5) * (perm ? 256 : 512);
            st16(wsr, l * 16, WS_WO + (b * 20 + w * 5 + i) * 1024, asu(cat(ldstr(t0a), ldstr(t0a + (perm ? 512 : 256)))));
          }
        } else if (it == 1) {
          const u32 l = opaque<u32>(lane);
#pragma unroll
          for (int half = 0; half < 2; ++half) {
            const u32 po = blk_of(half) * 32768 + (4 * j + w) * 1024;
            st16(wsr, l * 16, WS_OX + po, s4); st16(wsr, l * 16, WS_HX + po, s4); st16(wsr, l * 16, WS_X1 + po, s4);
            const int R = blk_of(half) * 32 + (int)(l & 31) + dlt;
            const u32 mk = (u32)R < 2048u ? SENT : 0u;
            st16(wsr, ((R & 2047) >> 5) * 32768 + (l >> 5) * 512 + (R & 31) * 16, WS_TS + (4 * j + w) * 1024, (u32x4){mk, mk, mk, mk});
          }
        } else if (it == 2) {
          const u32 uo = WS_UX + (blk_of(w >> 1) * 112 + 14 * j + 7 * (w & 1)) * 1024;
          const u32 l = opaque<u32>(l16);
#pragma unroll
          for (int i = 0; i < 7; ++i) st16(wsr, l + (i & 3) * 1024, uo + (i >> 2) * 4096, s4);
        } else if (it == 5) {
          WAITV(0);
          if (lds_count(L_CNT + 4) == 3 && lane < 8) __hip_atomic_store((GAS u64*)(ws + WS_FW + lane * 2048 + b * 8), tag, __ATOMIC_RELAXED, __HIP_MEMORY_SCOPE_AGENT);
        }
      } else {
        if (it == 0) {
          kv_rest(WS_V, L_V, 2, N4{}); kv_rest(WS_K, L_K, 3, N4{}); kv_rest(WS_V, L_V, 4, N4{}); kv_rest(WS_K, L_K, 5, N4{});
          kv_rest(WS_V, L_V, 6, N4{}); kv_rest(WS_K, L_K, 7, N2{});
        } else if (it == 1) {
          WAITV(0);
          lds_count(L_CNT);
        }
      }
      if (it == 4) {
        while (__builtin_amdgcn_readfirstlane(*(volatile LAS u32*)((LAS char*)smem + L_CNT)) != 4) {
          if (timed_out(t0)) break;
          __builtin_amdgcn_s_sleep(1);
        }
        FENCE();
      }
      if (it == 7) {
        const u32 l = opaque<u32>(l16);
        fa = ld16(wsr, l * 2, WS_FW + (b & 7) * 2048); fb = ld16(wsr, l * 2 + 16, WS_FW + (b & 7) * 2048);
      }
      if (it == 8) {
        if (!flags_ok(fa, fb, taglo, taghi)) wait_flags(wsr, WS_FW + (b & 7) * 2048, taglo, taghi, lane, t0);
      }
      if (it >= 8) {
        const u32 l = opaque<u32>(l16);
#pragma unroll
        for (int p = 5 * (it - 8); p < 5 * (it - 7); ++p) fetch(p, l);
      }
      if (it == 12 && w < 4) __builtin_amdgcn_s_setprio(0);
      FENCE();
      ASTAMP(it, 1);
      tiles4();
      FENCE();
      if (it == 9) STAMP(7);
    });
    STAMP(8);
    __builtin_amdgcn_s_setprio(0);
    ka = L_K + l16;
    float l = o[8];
    if (!__all(l <= 1.8446744e19f)) {
      float m = -INFINITY;
      o = (f32x16){};
      u32 vb = isv ? L_V + h2 * 256 + r32 * 16 : L_ONES;
      for (int t = 0; t < 64; ++t) {
        f32x16 z = {};
        const f32x16 sx = mfma32(ldsr<bf16x8>(ka + t * 1024), qf, z);
        float mt = sx[0];
#pragma unroll
        for (int i = 1; i < 16; ++i) mt = fmaxf(mt, sx[i]);
        const auto rr = __builtin_amdgcn_permlane32_swap(__float_as_uint(mt), __float_as_uint(mt), false, false);
        mt = fmaxf(fmaxf(__uint_as_float(rr[0]), __uint_as_float(rr[1])), m);
        const float f = __builtin_amdgcn_exp2f(m - mt);
        m = mt;
        u32 pk[8];
#pragma unroll
        for (int i = 0; i < 8; ++i) pk[i] = cvtpk(__builtin_amdgcn_exp2f(sx[2 * i] - m), __builtin_amdgcn_exp2f(sx[2 * i + 1] - m));
#pragma unroll
        for (int i = 0; i < 16; ++i) o[i] *= f;
        o = mfma32(ldsr<bf16x8>(vb), asbf((u32x4){pk[0], pk[1], pk[2], pk[3]}), o);
        o = mfma32(ldsr<bf16x8>(vb + 512), asbf((u32x4){pk[4], pk[5], pk[6], pk[7]}), o);
        vb += isv ? 1024u : 0u;
      }
      MFMA_GAP32(o);
      l = o[8];
    }
    const float rl = __builtin_amdgcn_rcpf(l);
#pragma unroll
    for (int i = 0; i < 4; ++i) opk[i] = canon(cvtpk(o[2 * i] * rl, o[2 * i + 1] * rl));
  }
  st16(wsr, ox_off(), 0, (u32x4){opk[0], opk[1], opk[2], opk[3]});
  STAMP(9);
  bf16x8 sel[4];
  auto make_sel = [&]() {
    const u32 l = opaque<u32>(lane), e = (l & 1) ? 0x3F800000u : 0x00003F80u;
#pragma unroll
    for (int q = 0; q < 4; ++q) {
      const bool on = ((l >> 2) & 3) == q;
      const u32 d0 = (on && !(l & 2)) ? e : 0u, d1 = (on && (l & 2)) ? e : 0u;
      sel[q] = asbf((u32x4){d0, d1, d0, d1});
    }
  };
  make_sel();
  const u32 gvo = w * 4096 + l16;
  auto pw_of = [&](u32 l) { return L_P + ((l >> 3) & 3) * 1024 + (w >> 1) * 256 + (l & 7) * 32 + (l >> 5) * 16 + (((w ^ (l >> 3)) & 1) * 8); };
  const u32 pw = pw_of(lane);
  const u32 pr = L_P + gvo;
  auto g_load = [&](bf16x8 (&f)[4], u32 src) {
#pragma unroll
    for (int t = 0; t < 4; ++t) f[t] = asbf(ld16(wsr, gvo + t * 1024, src));
  };
  auto g_wait = [&](bf16x8 (&f)[4], u32 src) {
    while (unwritten(f)) {
      if (timed_out(t0)) break;
      __builtin_amdgcn_s_sleep(1);
      FENCE();
      g_load(f, src);
    }
  };
  auto row_stats = [&](int half, const bf16x8 (&f)[4]) {
    float s0 = 0.f, s1 = 0.f, q0 = 0.f, q1 = 0.f;
#pragma unroll
    for (int t = 0; t < 4; ++t) {
      const u32x4 d = asu(f[t]);
      s0 = dot2(d[0], 0x3F803F80u, s0); q0 = dot2(d[0], d[0], q0); s1 = dot2(d[1], 0x3F803F80u, s1); q1 = dot2(d[1], d[1], q1);
      s0 = dot2(d[2], 0x3F803F80u, s0); q0 = dot2(d[2], d[2], q0); s1 = dot2(d[3], 0x3F803F80u, s1); q1 = dot2(d[3], d[3], q1);
    }
    const f32x2 sq = {swap32_add(s0 + s1), swap32_add(q0 + q1)};
    if (lane < 32) ldsw<f32x2>(L_STAT + half * STH + lane * STP + w * 8, sq);
  };
  auto partials = [&](int half, f32x16& acc0, f32x16& acc1, u32 pw) {
#pragma unroll
    for (int a = 0; a < 4; ++a) {
      ldsw<u32x2>(pw + half * 32768 + a * 4096, (u32x2){cvtpk(acc0[4 * a], acc0[4 * a + 1]), cvtpk(acc0[4 * a + 2], acc0[4 * a + 3])});
      ldsw<u32x2>(pw + half * 32768 + (4 + a) * 4096, (u32x2){cvtpk(acc1[4 * a], acc1[4 * a + 1]), cvtpk(acc1[4 * a + 2], acc1[4 * a + 3])});
    }
  };
  auto slice_gemm = [&](int half, const bf16x8 (&a)[8], const bf16x8 (&f)[4]) {
    f32x16 acc0 = {}, acc1 = {};
#pragma unroll
    for (int t = 0; t < 4; ++t) { acc0 = mfma32(a[t], f[t], acc0); acc1 = mfma32(a[4 + t], f[t], acc1); }
    partials(half, acc0, acc1, pw);
  };
  auto reduce_at = [&](int half, u32 pr_) -> f32x4 {
    f32x4 acc = {0, 0, 0, 0};
#pragma unroll
    for (int q = 0; q < 4; ++q) acc = mfma16(sel[q], ldsr<bf16x8>(pr_ + half * 32768 + q * 1024), acc);
    return acc;
  };
  auto reduce = [&](int half) { return reduce_at(half, pr); };
  auto src_of = [&](u32 base, int half) { return base + blk_of(half) * 32768; };
  auto pack4 = [&](f32x4 v) { return (u32x2){canon(cvtpk(v[0], v[1])), canon(cvtpk(v[2], v[3]))}; };
  const u32 a3off = w < 7 ? ((7 * j + w) * 32) * 1024 + l16 : OOB;
  auto a3_load = [&](int t0_, int t1_) {
#pragma unroll
    for (int t = 0; t < 32; ++t) if (t >= t0_ && t < t1_) a3[t] = asbf(ld16(wsr, a3off, WS_W3 + t * 1024));
  };

  f32x4 hres[2], xp[2], cs1;
  bf16x8 fA[4], fB[4];
  auto phase = [&](const bf16x8 (&a)[8], u32 src, bool stats, auto&& first, auto&& epi, auto&& put, auto&& pre, auto&& slotA, auto&& slotB, int sbase) {
    PSTAMP(sbase, 0);
    g_wait(fA, src_of(src, 0));
    STAMP(sbase);
    PSTAMP(sbase, 1);
    if (stats) row_stats(0, fA);
    first();
    PSTAMP(sbase, 2);
    slice_gemm(0, a, fA);
    PSTAMP(sbase, 3);
    g_load(fB, src_of(src, 1));
    slotA();
    PSTAMP(sbase, 4);
    BAR();
    PSTAMP(sbase, 5);
    const f32x4 rA = reduce(0);
    const u32x2 vA = epi(0, rA);
    PSTAMP(sbase, 6);
    put(0, vA);
    PSTAMP(sbase, 7);
    g_wait(fB, src_of(src, 1));
    STAMP(sbase + 1);
    PSTAMP(sbase, 8);
    if (stats) row_stats(1, fB);
    PSTAMP(sbase, 9);
    slice_gemm(1, a, fB);
    PSTAMP(sbase, 10);
    slotB();
    PSTAMP(sbase, 11);
    BAR();
    PSTAMP(sbase, 12);
    const u32x2 vB = epi(1, reduce(1));
    PSTAMP(sbase, 13);
    pre();
    PSTAMP(sbase, 14);
    put(1, vB);
    PSTAMP(sbase, 15);
  };
  auto lane_stat = [&](int half) {
    const u32 sa = L_STAT + half * STH + (lane >> 1) * STP + (lane & 1) * 32;
    const f32x4 p0 = ldsr<f32x4>(sa), p1 = ldsr<f32x4>(sa + 16);
    return finish_stat(xor1_add((p0[0] + p0[2]) + (p1[0] + p1[2])), xor1_add((p0[1] + p0[3]) + (p1[1] + p1[3])));
  };
  const u32 a4off = (2 * j * 112 + 14 * w) * 1024 + l16;
  g_load(fA, src_of(WS_OX, 0));
  {
    float c[2] = {0.f, 0.f};
#pragma unroll
    for (int t = 0; t < 8; ++t) {
      const u32x4 d = asu(a_1[t]);
#pragma unroll
      for (int e = 0; e < 4; ++e) c[t >> 2] = dot2(d[e], 0x3F803F80u, c[t >> 2]);
    }
    const float c0 = swap32_add(c[0]), c1 = swap32_add(c[1]);
    ldsw<float>(L_CS1P + w * 256 + lane * 4, lane < 32 ? c0 : c1);
  }
  phase(a_o, WS_OX, false,
        [&]() { BAR(); },
        [&](int half, f32x4 acc) {
          if (half == 0) {
            cs1 = (f32x4){0.f, 0.f, 0.f, 0.f};
#pragma unroll
            for (int v = 0; v < 8; ++v) cs1 += ldsr<f32x4>(L_CS1P + v * 256 + w * 32 + (lane & 1) * 16);
          }
          hres[half] = acc + (f32x4){bflo(xres[half][0]), bfhi(xres[half][0]), bflo(xres[half][1]), bfhi(xres[half][1])};
          return pack4(hres[half]);
        },
        [&](int half, u32x2 v) { st8(wsr, hx_off(WS_HX, half), 0, v); },
        [&]() { g_load(fA, src_of(WS_HX, 0)); },
        [&]() {
#pragma unroll
          for (int i = 0; i < 7; ++i) dma1(ws + WS_W4 + i * 1024, (((u32)(b >> 6) & 3u) * 8 + w) * 7168 + j * 229376 + l16, lb + L_DUM1);
        },
        [&]() { a3_load(A3_N0, A3_N0 + 5); }, 10);
  STAMP(12);
  phase(a_1, WS_HX, true, [&]() { },
        [&](int half, f32x4 acc) {
          const RowStat st = lane_stat(half);
          const float r2 = __builtin_amdgcn_rsqf(st.var * st.rstd * st.rstd + LN_EPS), rr = st.rstd * r2;
          xp[half] = (hres[half] - st.mu) * st.rstd;
          const f32x4 pre = (acc - st.mu * cs1) * rr;
          return pack4((f32x4){gelu(pre[0]), gelu(pre[1]), gelu(pre[2]), gelu(pre[3])});
        },
        [&](int half, u32x2 v) { st8(wsr, ts_off(half), 0, v); },
        [&]() { g_load(fA, src_of(WS_TS, 0)); },
        [&]() { a3_load(A3_N0 + 5, A3_N0 + 10); },
        [&]() { a3_load(A3_N0 + 10, A3_N0 + 13); dma4_sc1(ws + WS_W4 + 10 * 1024, a4off, lb + L_A4 + w * 8192); }, 13);
  STAMP(15);
  phase(a_2, WS_TS, false, [&]() { },
        [&](int half, f32x4 acc) {
          const f32x4 x1 = xp[half] + acc;
          ldsw<f32x4>(L_RES + half * 8192 + w * 1024 + l16, hres[half] + x1);
          return pack4(x1);
        },
        [&](int half, u32x2 v) { st8(wsr, hx_off(WS_X1, half), 0, v); },
        [&]() { dma4_sc1(ws + src_of(WS_X1, 0), gvo, lb + L_P + w * 4096); },
        [&]() { a3_load(A3_N0 + 13, A3_N0 + 16); dma4_sc1(ws + WS_W4 + 114688 + 10 * 1024, a4off, lb + L_A4 + w * 8192 + 4096); },
        [&]() { a3_load(A3_N0 + 16, 32); }, 16);
  STAMP(18);
  bf16x8 a4[10][2];
  auto a4_load = [&](int n) { a4[n >> 1][n & 1] = asbf(ld16(wsr, a4off, WS_W4 + (n & 1) * 114688 + (n >> 1) * 1024)); };
  u32x4 uf[14];
  auto u_load1 = [&](int half, int t) { uf[t] = ld16(wsr, l16, WS_UX + (blk_of(half) * 112 + w * 14 + t) * 1024); };
  {
    auto x1_finish = [&](int half) {
      for (;;) {
#pragma unroll
        for (int t = 0; t < 4; ++t) fA[t] = ldsr<bf16x8>(pr + half * 32768 + t * 1024);
        if (!unwritten(fA)) break;
        if (timed_out(t0)) break;
        __builtin_amdgcn_s_sleep(1);
        dma4_sc1(ws + src_of(WS_X1, half), gvo, lb + L_P + half * 32768 + w * 4096);
        WAITV(0);
      }
      row_stats(half, fA);
    };
    struct UOut { float rstd, nm; f32x4 cs[2]; float gv[4]; u32 up[8]; u32 l; };
    auto u_stat = [&](UOut& u, int half) {
      u.l = opaque<u32>(lane);
      const u32 sr = L_STAT + half * STH + (u.l & 31) * STP;
      const f32x4 p0 = ldsr<f32x4>(sr), p1 = ldsr<f32x4>(sr + 16), p2 = ldsr<f32x4>(sr + 32), p3 = ldsr<f32x4>(sr + 48);
      const f32x4 p = (p0 + p1) + (p2 + p3);
      const RowStat st = finish_stat(p[0] + p[2], p[1] + p[3]);
      u.rstd = st.rstd; u.nm = -st.mu * st.rstd;
    };
    auto u_cs = [&](UOut& u, int a) { u.cs[a & 1] = ldsr<f32x4>(L_CS + (32 * w + 8 * a) * 4 + (u.l >> 5) * 16); };
    auto u_val = [&](UOut& u, int half, const f32x16& acc, int v) {
      const int a = v >> 2, e = v & 3;
      u.gv[e] = gelu(acc[v] * u.rstd + u.nm * u.cs[a & 1][e]);
      if (e == 3) { u.up[2 * a] = canon(cvtpk(u.gv[0], u.gv[1])); u.up[2 * a + 1] = canon(cvtpk(u.gv[2], u.gv[3])); }
      if (v == 7 || v == 15) {
        const u32 uo = w < 7 ? WS_UX + (blk_of(half) * 112 + j * 14 + 2 * w) * 1024 + u.l * 16 : OOB;
        if (v == 7) st16(wsr, uo, 0, (u32x4){u.up[0], u.up[1], u.up[2], u.up[3]});
        else st16(wsr, uo, 1024, (u32x4){u.up[4], u.up[5], u.up[6], u.up[7]});
      }
    };
    WAITV(1);
    x1_finish(0);
    BAR();
    STAMP(19);
    dma4_sc1(ws + src_of(WS_X1, 1), gvo, lb + L_P + 32768 + w * 4096);
    const u32 xb = L_P + (w < 7 ? l16 : 0u);
    f32x16 accA = {}, accB = {};
    bf16x8 bq[4];
    {
      float c0 = 0.f, c1 = 0.f;
#pragma unroll
      for (int t = 0; t < 4; ++t) bq[t] = ldsr<bf16x8>(xb + t * 1024);
#pragma unroll
      for (int t = 0; t < 32; ++t) {
        accA = mfma32(a3[t], bq[t & 3], accA);
        if (t + 4 < 32) bq[t & 3] = ldsr<bf16x8>(xb + (t + 4) * 1024);
        const u32x4 d = asu(a3[t]);
        c0 = dot2(d[0], 0x3F803F80u, c0); c1 = dot2(d[1], 0x3F803F80u, c1); c0 = dot2(d[2], 0x3F803F80u, c0); c1 = dot2(d[3], 0x3F803F80u, c1);
        if (t >= 4 && (t & 3) == 0 && t / 4 - 1 < A4_N0) a4_load(t / 4 - 1);
        __builtin_amdgcn_sched_barrier(0);
      }
      const float c = swap32_add(c0 + c1);
      if (lane < 32) ldsw<float>(L_CS + w * 128 + lane * 4, c);
    }
    STAMP(20);
    asm volatile("s_waitcnt vmcnt(%0)" :: "n"(A4_N0) : "memory");
    x1_finish(1);
    BAR();
    STAMP(21);
    {
      UOut u;
#pragma unroll
      for (int t = 0; t < 4; ++t) bq[t] = ldsr<bf16x8>(xb + 32768 + t * 1024);
      u_stat(u, 0); u_cs(u, 0);
#pragma unroll
      for (int t = 0; t < 32; ++t) {
        if (t < 16 && (t & 3) == 1 && t + 3 < 16) u_cs(u, (t + 3) >> 2);
        accB = mfma32(a3[t], bq[t & 3], accB);
        if (t + 4 < 32) bq[t & 3] = ldsr<bf16x8>(xb + 32768 + (t + 4) * 1024);
        if (t < 16) u_val(u, 0, accA, t);
        const int n = A4_N0 + t - 1;
        if (t >= 1 && n < 20) a4_load(n);
        __builtin_amdgcn_sched_barrier(0);
      }
    }
    STAMP(22);
    {
      UOut u;
      u_stat(u, 1); u_cs(u, 0);
#pragma unroll
      for (int v = 0; v < 16; ++v) {
        if ((v & 3) == 0 && v + 4 < 16) u_cs(u, (v + 4) >> 2);
        if (v < 14) u_load1(0, v);
        u_val(u, 1, accB, v);
        __builtin_amdgcn_sched_barrier(0);
      }
    }
    STAMP(23);
  }
  {
    auto u_wait = [&](int half) {
      for (;;) {
        u32 m = 0;
#pragma unroll
        for (int t = 0; t < 14; ++t) m = umax(m, max4(uf[t]));
        if (!__any(m == SENT)) break;
        if (timed_out(t0)) break;
        __builtin_amdgcn_s_sleep(2);
        FENCE();
#pragma unroll
        for (int t = 0; t < 14; ++t) u_load1(half, t);
      }
    };
#pragma unroll
    for (int half = 0; half < 2; ++half) {
      u_wait(half);
      STAMP(24 + half);
      const u32 sa = L_A4 + w * 8192 + opaque<u32>(l16);
      f32x16 acc0 = {}, acc1 = {};
      bf16x8 sq[4][2];
#pragma unroll
      for (int t = 0; t < 14; ++t) {
        if (t >= 8 && t < 12) { sq[t - 8][0] = ldsr<bf16x8>(sa + (t - 8) * 1024); sq[t - 8][1] = ldsr<bf16x8>(sa + 4096 + (t - 8) * 1024); }
        acc0 = mfma32(t < 10 ? a4[t < 10 ? t : 0][0] : sq[t < 10 ? 0 : t - 10][0], asbf(uf[t]), acc0);
        acc1 = mfma32(t < 10 ? a4[t < 10 ? t : 0][1] : sq[t < 10 ? 0 : t - 10][1], asbf(uf[t]), acc1);
        if (half == 0 && t >= 7) { u_load1(1, 2 * (t - 7)); u_load1(1, 2 * (t - 7) + 1); }
        __builtin_amdgcn_sched_barrier(0);
      }
      if (half == 0) BAR();
      partials(half, acc0, acc1, pw_of(opaque<u32>(lane)));
      FENCE();
    }
    STAMP(26);
    make_sel();
    BAR();
#pragma unroll
    for (int half = 0; half < 2; ++half) {
      const u32 l = opaque<u32>(lane);
      const f32x4 v = reduce_at(half, L_P + w * 4096 + l * 16) + ldsr<f32x4>(L_RES + half * 8192 + w * 1024 + l * 16);
      *(GAS u32x2*)((char*)out + (u32)((blk_of(half) * 32 + (l >> 1)) * 1024 + (64 * j + 8 * w + 4 * (l & 1)) * 2)) = (u32x2){cvtpk(v[0], v[1]), cvtpk(v[2], v[3])};
    }
  }
  STAMP(27);
}

__shared__ char smem2[163840];
__global__ __launch_bounds__(512, 2) void probe_kernel() {
  const u64 t0 = __builtin_amdgcn_s_memrealtime();
  while (__builtin_amdgcn_s_memrealtime() - t0 < 3000ull) __builtin_amdgcn_s_sleep(1);
  asm volatile("v_mov_b32 v255, 0" ::: "v255");
  if (t0 == 12345) smem2[threadIdx.x] = 1;
}

extern "C" void kernel_launch(void* const* d_in, const int* in_sizes, int n_in, void* d_out, int out_size, void* d_ws, size_t ws_size, hipStream_t stream) {
  (void)in_sizes; (void)n_in; (void)out_size; (void)ws_size;
  probe_kernel<<<256, 512, 0, stream>>>();
  fused_block_kernel<<<256, 512, 0, stream>>>((const u16*)d_in[0], (const u16*)d_in[1], (const u16*)d_in[2], (const u16*)d_in[3], (const u16*)d_in[4],
                                              (const u16*)d_in[6], (const u16*)d_in[7], (const u16*)d_in[8], (const u16*)d_in[9], (u16*)d_out, (char*)d_ws);
}
```

```cpp
#include <hip/hip_runtime.h>
#include <stdint.h>
#include <type_traits>

typedef unsigned short u16;
typedef unsigned int u32;
typedef unsigned long long u64;
typedef short bf16x8 __attribute__((ext_vector_type(8)));
typedef short s16x4 __attribute__((ext_vector_type(4)));
typedef float f32x2 __attribute__((ext_vector_type(2)));
typedef float f32x4 __attribute__((ext_vector_type(4)));
typedef float f32x16 __attribute__((ext_vector_type(16)));
typedef u32 u32x2 __attribute__((ext_vector_type(2)));
typedef u32 u32x4 __attribute__((ext_vector_type(4)));
typedef __bf16 bf16x2_t __attribute__((ext_vector_type(2)));
#define LAS __attribute__((address_space(3)))
#define GAS __attribute__((address_space(1)))
#define DI __device__ __forceinline__

constexpr u32 WS_EPOCH = 0x0;
constexpr u32 WS_FKV = 0x1000;
constexpr u32 WS_FW = 0x5000;
constexpr u32 WS_K = 0x10000;
constexpr u32 WS_V = 0x20000;
constexpr u32 WS_DBG = 0x1E00000;
constexpr u32 WS_WO = 0x100000, WS_W1 = 0x180000, WS_W2 = 0x200000, WS_W3 = 0x280000, WS_W4 = 0x440000;
constexpr u32 WS_OX = 0x600000, WS_HX = 0x800000, WS_TS = 0xA00000, WS_X1 = 0xC00000, WS_UX = 0xE00000;
constexpr u32 WS_STAMP = 0x2000000;
constexpr u32 WS_END = 0x2100000;
constexpr u32 OOB = 0x80000000u;

constexpr u32 XP = 1040;
constexpr u32 L_XIMG = 0;
constexpr u32 L_WQ = 66560;
constexpr u32 L_WKV = 132096;
constexpr u32 S_PM = 148480, S_PC = 156672, S_PS = 157184, S_PQ = 157696, S_FIN = 158208, S_QS = 158336, S_QC = 159360;
constexpr u32 L_DUM1 = 160384;
constexpr u32 L_CNT = 161408;
constexpr u32 L_K = 0, L_V = 65536, L_ONES = 131072;
constexpr u32 L_STG = 135424;
constexpr u32 L_CS1P = 155904;
constexpr u32 L_P = 0;
constexpr u32 L_STAT = 65536;
constexpr u32 STP = 80, STH = 2560;
constexpr u32 L_CS = 71680;
constexpr u32 L_RES = 73728;
constexpr u32 L_A4 = 90112;
constexpr u32 L_STAMP = 161792;
constexpr u32 LDS_BYTES = 163840;

__shared__ __attribute__((aligned(16))) char smem[LDS_BYTES];

constexpr u32 SENT = 0xFFFFFFFFu;
constexpr float LN_EPS = 1e-5f;
constexpr float QSCALE = 0.25f * 1.4426950408889634f;
#define CODE_KB 43

DI f32x16 mfma32(bf16x8 a, bf16x8 b, f32x16 c) { return __builtin_amdgcn_mfma_f32_32x32x16_bf16(a, b, c, 0, 0, 0); }
DI f32x4 mfma16(bf16x8 a, bf16x8 b, f32x4 c) { return __builtin_amdgcn_mfma_f32_16x16x32_bf16(a, b, c, 0, 0, 0); }
DI u32 cvtpk(float lo, float hi) { f32x2 v = {lo, hi}; bf16x2_t b = __builtin_convertvector(v, bf16x2_t); return __builtin_bit_cast(u32, b); }
DI u32 canon(u32 d) { return d < 0xFFFFFFFEu ? d : 0xFFFFFFFEu; }
DI float bflo(u32 d) { return __uint_as_float(d << 16); }
DI float bfhi(u32 d) { return __uint_as_float(d & 0xFFFF0000u); }
DI float dot2(u32 a, u32 b, float c) { return __builtin_amdgcn_fdot2_f32_bf16(__builtin_bit_cast(bf16x2_t, a), __builtin_bit_cast(bf16x2_t, b), c, false); }
DI float gelu(float x) {
  const float c1 = -2.0f * 0.7978845608028654f * 1.4426950408889634f, c2 = c1 * 0.044715f;
  const float e = __builtin_amdgcn_exp2f(x * (c1 + c2 * x * x));
  return x * __builtin_amdgcn_rcpf(1.0f + e);
}
DI f32x2 gelu2(f32x2 x) {
  const float c1 = -2.0f * 0.7978845608028654f * 1.4426950408889634f, c2 = c1 * 0.044715f;
  const f32x2 a = x * ((x * x) * c2 + c1);
  const f32x2 d = (f32x2){__builtin_amdgcn_exp2f(a[0]), __builtin_amdgcn_exp2f(a[1])} + 1.0f;
  return x * (f32x2){__builtin_amdgcn_rcpf(d[0]), __builtin_amdgcn_rcpf(d[1])};
}
DI float swap32_add(float v) {
  const auto r = __builtin_amdgcn_permlane32_swap(__float_as_uint(v), __float_as_uint(v), false, false);
  return __uint_as_float(r[0]) + __uint_as_float(r[1]);
}
DI float xor1_add(float v) { return v + __int_as_float(__builtin_amdgcn_mov_dpp(__float_as_int(v), 0xB1, 0xF, 0xF, true)); }

#define BAR() asm volatile("s_waitcnt lgkmcnt(0)\n\ts_barrier" ::: "memory")
#define WAITV(n) asm volatile("s_waitcnt vmcnt(" #n ")" ::: "memory")
#define FENCE() asm volatile("" ::: "memory")
template <class T> DI T opaque(T v) { asm volatile("" : "+v"(v)); return v; }

template <class T> DI T ldsr(u32 off) { return *(const LAS T*)((LAS char*)smem + off); }
template <class T> DI void ldsw(u32 off, T v) { *(LAS T*)((LAS char*)smem + off) = v; }
DI s16x4 ldstr(u32 off) {
  typedef short v4i16_t __attribute__((ext_vector_type(4)));
  return __builtin_bit_cast(s16x4, __builtin_amdgcn_ds_read_tr16_b64_v4i16((LAS v4i16_t*)((LAS char*)smem + off)));
}
DI bf16x8 cat(s16x4 a, s16x4 b) { return __builtin_shufflevector(a, b, 0, 1, 2, 3, 4, 5, 6, 7); }
DI u32 lds_base() { return (u32)(uintptr_t)(LAS char*)smem; }

#define DMA_HEAD "s_mov_b32 %[k], m0\n\ts_mov_b32 m0, %[lds]\n\ts_nop 0\n\t"
#define DMA_TAIL "s_mov_b32 m0, %[k]"
DI void dma1(const void* sbase, u32 voff, u32 ldsdst) {
  u32 keep;
  asm volatile(DMA_HEAD "global_load_lds_dwordx4 %[v], %[s]\n\t" DMA_TAIL : [k] "=&s"(keep) : [v] "v"(voff), [s] "s"(sbase), [lds] "s"(ldsdst) : "memory");
}
DI void dma2(const void* sbase, u32 voff, u32 ldsdst) {
  u32 keep;
  asm volatile(DMA_HEAD "global_load_lds_dwordx4 %[v], %[s]\n\tglobal_load_lds_dwordx4 %[v], %[s] offset:1024\n\t" DMA_TAIL
               : [k] "=&s"(keep) : [v] "v"(voff), [s] "s"(sbase), [lds] "s"(ldsdst) : "memory");
}
DI void dma4_rows(const void* sbase, u32 v0, u32 v1, u32 v2, u32 v3, u32 ldsdst) {
  u32 keep;
  asm volatile(DMA_HEAD "global_load_lds_dwordx4 %[a], %[s]\n\tglobal_load_lds_dwordx4 %[b], %[s] offset:1040\n\t"
               "global_load_lds_dwordx4 %[c], %[s] offset:2080\n\tglobal_load_lds_dwordx4 %[d], %[s] offset:3120\n\t" DMA_TAIL
               : [k] "=&s"(keep) : [a] "v"(v0), [b] "v"(v1), [c] "v"(v2), [d] "v"(v3), [s] "s"(sbase), [lds] "s"(ldsdst) : "memory");
}
DI void dma4_sc1(const void* sbase, u32 voff, u32 ldsdst) {
  u32 keep;
  asm volatile(DMA_HEAD "global_load_lds_dwordx4 %[v], %[s] sc1\n\tglobal_load_lds_dwordx4 %[v], %[s] offset:1024 sc1\n\t"
               "global_load_lds_dwordx4 %[v], %[s] offset:2048 sc1\n\tglobal_load_lds_dwordx4 %[v], %[s] offset:3072 sc1\n\t" DMA_TAIL
               : [k] "=&s"(keep) : [v] "v"(voff), [s] "s"(sbase), [lds] "s"(ldsdst) : "memory");
}
#define DMA_STEP(SC) "global_load_lds_dwordx4 %[v], %[s]" SC "\n\ts_add_u32 m0, m0, %[ls]\n\tv_add_u32 %[v], %[gs], %[v]\n\t"
template <int N, bool SC1, u32 GS>
DI void dma_strided(const void* sbase, u32 voff, u32 ldsdst, u32 lstep) {
  u32 keep;
  static_assert(N == 2 || N == 3 || N == 4 || N == 8, "");
#define DMA_RUN(BODY) asm volatile(DMA_HEAD BODY DMA_TAIL : [k] "=&s"(keep), [v] "+v"(voff) : [s] "s"(sbase), [lds] "s"(ldsdst), [ls] "s"(lstep), [gs] "n"(GS) : "memory", "scc")
  if constexpr (SC1) {
    if constexpr (N == 2) DMA_RUN(DMA_STEP(" sc1") DMA_STEP(" sc1"));
    if constexpr (N == 3) DMA_RUN(DMA_STEP(" sc1") DMA_STEP(" sc1") DMA_STEP(" sc1"));
    if constexpr (N == 4) DMA_RUN(DMA_STEP(" sc1") DMA_STEP(" sc1") DMA_STEP(" sc1") DMA_STEP(" sc1"));
  } else {
    if constexpr (N == 8) DMA_RUN(DMA_STEP("") DMA_STEP("") DMA_STEP("") DMA_STEP("") DMA_STEP("") DMA_STEP("") DMA_STEP("") DMA_STEP(""));
  }
#undef DMA_RUN
}

typedef __amdgpu_buffer_rsrc_t rsrc_t;
DI rsrc_t mkrsrc(const void* p, u32 bytes) { return __builtin_amdgcn_make_buffer_rsrc((void*)p, 0, bytes, 0x00020000); }
DI u32x4 ld16(rsrc_t r, u32 voff, u32 soff) { return __builtin_amdgcn_raw_buffer_load_b128(r, voff, soff, 16); }
DI void st16(rsrc_t r, u32 voff, u32 soff, u32x4 v) {
  __builtin_amdgcn_raw_buffer_store_b128(v, r, voff, soff, 16);
  asm volatile("s_nop 1" :: "v"(v) : "memory");
}
DI void st8(rsrc_t r, u32 voff, u32 soff, u32x2 v) { __builtin_amdgcn_raw_buffer_store_b64(v, r, voff, soff, 16); }
DI void st4(rsrc_t r, u32 voff, u32 soff, u32 v) { __builtin_amdgcn_raw_buffer_store_b32(v, r, voff, soff, 16); }
DI bf16x8 asbf(u32x4 v) { return __builtin_bit_cast(bf16x8, v); }
DI u32x4 asu(bf16x8 v) { return __builtin_bit_cast(u32x4, v); }
DI void regather4(u32x4 srd, u32 voff, u32 soff, bf16x8& f0, bf16x8& f1, bf16x8& f2, bf16x8& f3) {
  asm volatile("s_sleep 1\n\tbuffer_load_dwordx4 %0, %4, %5, %6 offen sc1\n\tbuffer_load_dwordx4 %1, %4, %5, %6 offen offset:1024 sc1\n\t"
               "buffer_load_dwordx4 %2, %4, %5, %6 offen offset:2048 sc1\n\tbuffer_load_dwordx4 %3, %4, %5, %6 offen offset:3072 sc1\n\ts_waitcnt vmcnt(0)"
               : "+v"(f0), "+v"(f1), "+v"(f2), "+v"(f3) : "v"(voff), "s"(srd), "s"(soff) : "memory");
}
DI void regather2(u32x4 srd, u32 voff, u32 soff, u32x4& f0, u32x4& f1) {
  asm volatile("buffer_load_dwordx4 %0, %2, %3, %4 offen sc1\n\tbuffer_load_dwordx4 %1, %2, %3, %4 offen offset:1024 sc1"
               : "+v"(f0), "+v"(f1) : "v"(voff), "s"(srd), "s"(soff) : "memory");
}
DI u32 umax(u32 a, u32 b) { return a > b ? a : b; }
DI u32 max4(u32x4 v) { return umax(umax(v[0], v[1]), umax(v[2], v[3])); }

constexpr u64 SPIN_TICKS = 400000ull;
DI bool timed_out(u64 t0) { return __builtin_amdgcn_s_memrealtime() - t0 > SPIN_TICKS; }

#define STAMP(i) do { } while (0)
#define ASTAMP(it, pos) do { } while (0)
#define PSTAMP(sb, i) do { } while (0)
#define MFMA_GAP16(v) asm volatile("s_nop 7" : "+v"(v))
#define MFMA_GAP32(v) asm volatile("s_nop 7\n\ts_nop 3" : "+v"(v))

DI bool flags_ok(u32x4 a, u32x4 b, u32 lo, u32 hi) {
  return __all(a[0] == lo && a[1] == hi && a[2] == lo && a[3] == hi && b[0] == lo && b[1] == hi && b[2] == lo && b[3] == hi);
}
DI void wait_flags(rsrc_t wsr, u32 base, u32 taglo, u32 taghi, int lane, u64 t0) {
  for (;;) {
    const u32x4 a = ld16(wsr, lane * 32, base), b = ld16(wsr, lane * 32 + 16, base);
    if (flags_ok(a, b, taglo, taghi)) break;
    if (timed_out(t0)) break;
    __builtin_amdgcn_s_sleep(1);
    FENCE();
  }
}

DI bf16x8 ones_frag() { return asbf((u32x4){0x3F803F80u, 0x3F803F80u, 0x3F803F80u, 0x3F803F80u}); }
DI float diag_pick(f32x4 g, int lane) { const int r = lane & 3; float a = (r & 1) ? g[1] : g[0], b = (r & 1) ? g[3] : g[2]; return (r & 2) ? b : a; }

struct RowStat { float mu, rstd, var; };
DI RowStat finish_stat(float s, float q) {
  RowStat r; r.mu = s * (1.0f / 512.0f);
  r.var = fmaxf(q * (1.0f / 512.0f) - r.mu * r.mu, 0.0f);
  r.rstd = __builtin_amdgcn_rsqf(r.var + LN_EPS);
  return r;
}
DI bool unwritten(const bf16x8 (&f)[4]) {
  return __any(umax(umax(max4(asu(f[0])), max4(asu(f[1]))), umax(max4(asu(f[2])), max4(asu(f[3])))) == SENT);
}

template <int I, int N, class F> DI void static_for(F&& f) {
  if constexpr (I < N) { f(std::integral_constant<int, I>{}); static_for<I + 1, N>(f); }
}
struct Job { const char* src; u32 prow; bool perm; };
DI Job retile_job(u32 f, u64 wo, u64 w1, u64 w2, u64 w3, u64 w4) {
  const bool big = f >= 1536, last = f >= 3328;
  const u32 g = f - 3328, T = last ? g / 112 : big ? (f - 1536) >> 5 : (f >> 5) & 15, ks = last ? g % 112 : f & 31;
  const u32 prow = big && !last ? 3584 : 1024;
  u64 m = f < 1024 ? w1 : w2;
  m = f < 512 ? wo : m; m = big ? w3 : m; m = last ? w4 : m;
  return Job{(const char*)(m + (u64)(16 * ks * prow + T * 64)), prow, last || f < 512};
}

#define PRE_POS 0
#define PUT16 0
#define GB_POS 1
#define O_SLEEP 16
#define SLOT_POS 0
#define W4_WARM 0
#define FPI 6
#define A3_N0 14
static_assert(33 + A3_N0 + (W4_WARM == 1 ? 7 : 0) <= 8 * FPI, "fetch schedule");
#define A4_N0 6

__global__ __launch_bounds__(512, 2) void fused_block_kernel(const u16* __restrict__ x, const u16* __restrict__ wq, const u16* __restrict__ wk,
                                                             const u16* __restrict__ wv, const u16* __restrict__ wo, const u16* __restrict__ w1,
                                                             const u16* __restrict__ w2, const u16* __restrict__ w3, const u16* __restrict__ w4,
                                                             u16* __restrict__ out, char* ws) {
  const int tid = threadIdx.x, lane = tid & 63, w = __builtin_amdgcn_readfirstlane(tid >> 6);
  const int b = blockIdx.x;
  const int cl = (b & 7) * 4 + (b >> 6), j = (b >> 3) & 7;
  const u32 lb = lds_base();
  const u32 l16 = lane * 16;
  {
    u64 pc;
    asm volatile("s_getpc_b64 %0" : "=s"(pc));
    dma1((const void*)(pc & ~63ull), (u32)((((b >> 3) * 8 + w) % CODE_KB) * 1024) + l16, lb + L_DUM1);
  }
  asm volatile("" :: "s"(x), "s"(wq), "s"(wk), "s"(wv), "s"(wo), "s"(w1), "s"(w2), "s"(w3), "s"(w4), "s"(out), "s"(ws));
  const u64 t0 = __builtin_amdgcn_s_memrealtime();
  const int i16 = lane & 15, g4 = lane >> 4, r32 = lane & 31, h2 = lane >> 5;
  STAMP(0);

  const int kvrt = j >> 1, kvmat = j & 1;
  auto grow = [&](int rho) { return (rho < 32 ? 0 : 1024 - 32) + 32 * cl + rho; };
  const int R0 = grow(16 * kvrt);
  u32x4 xk0, xk1;
  u64 tag;
  {
    const u32 xo = (u32)(i16 * 1024 + g4 * 32);
    asm volatile("global_load_dwordx4 %0, %2, %3\n\tglobal_load_dwordx4 %1, %2, %3 offset:16"
                 : "=&v"(xk0), "=&v"(xk1) : "v"(xo), "s"((const char*)x + R0 * 1024 + w * 128) : "memory");
    dma2((const char*)(kvmat ? wv : wk) + w * 2048, l16, lb + L_WKV + w * 2048);
    asm volatile("s_load_dwordx2 %0, %1, 0x0" : "=s"(tag) : "s"(ws + WS_EPOCH) : "memory");
    const char* xr = (const char*)x + grow(8 * w) * 1024 - 48;
    dma4_rows(xr, l16 + 48, l16 + 32, l16 + 16, l16, lb + L_XIMG + 8 * w * XP);
    dma4_rows(xr + 4096, l16 + 48, l16 + 32, l16 + 16, l16, lb + L_XIMG + (8 * w + 4) * XP);
    dma_strided<8, false, 0x2000>((const char*)wq + w * 65536 + j * 128, (u32)((lane >> 3) * 1024 + (((lane & 7) ^ (4 * ((lane >> 4) & 1))) * 16)), lb + L_WQ + w * 8192, 1024);
    asm volatile("s_waitcnt vmcnt(16) lgkmcnt(0)" : "+v"(xk0), "+v"(xk1), "+s"(tag) :: "memory");
  }
  tag += 1ull;
  const u32 taglo = (u32)tag, taghi = (u32)(tag >> 32);
  const rsrc_t wsr = mkrsrc(ws, WS_END);
  const u32x4 srd = {(u32)(uintptr_t)ws, (u32)((uintptr_t)ws >> 32) & 0xFFFFu, WS_END, 0x00020000u};
  const bf16x8 one = ones_frag();
  STAMP(1);

  {
    f32x4 am = {0, 0, 0, 0}, ac = {0, 0, 0, 0}, as = {0, 0, 0, 0}, aq = {0, 0, 0, 0};
    const u32 wa = L_WKV + w * 2048 + (16 * g4 + (i16 >> 2)) * 32 + (i16 & 3) * 8;
    const bf16x8 wf0 = cat(ldstr(wa), ldstr(wa + 128)), wf1 = cat(ldstr(wa + 256), ldstr(wa + 384));
    const bf16x8 xf0 = asbf(xk0), xf1 = asbf(xk1);
    as = mfma16(one, xf0, as); aq = mfma16(xf0, xf0, aq); as = mfma16(one, xf1, as); aq = mfma16(xf1, xf1, aq);
    if (kvmat) {
      am = mfma16(xf0, wf0, am); ac = mfma16(one, wf0, ac); am = mfma16(xf1, wf1, am); ac = mfma16(one, wf1, ac);
      MFMA_GAP16(am); MFMA_GAP16(ac);
      if (lane < 16) ldsw<float>(S_PC + (w * 16 + lane) * 4, ac[0]);
    } else {
      am = mfma16(wf0, xf0, am); ac = mfma16(wf0, one, ac); am = mfma16(wf1, xf1, am); ac = mfma16(wf1, one, ac);
      MFMA_GAP16(am); MFMA_GAP16(ac);
      if (i16 == 0) ldsw<f32x4>(S_PC + (w * 16 + 4 * g4) * 4, ac);
    }
    ldsw<f32x4>(S_PM + w * 1024 + l16, am);
    if (lane < 16) ldsw<float>(S_PS + (lane * 8 + w) * 4, as[0]);
    if ((i16 >> 2) == g4) ldsw<float>(S_PQ + (i16 * 8 + w) * 4, diag_pick(aq, lane));
    BAR();
  }
  if (w == 0) {
    {
      const f32x4 s0 = ldsr<f32x4>(S_PS + i16 * 32), s1 = ldsr<f32x4>(S_PS + i16 * 32 + 16);
      const f32x4 q0 = ldsr<f32x4>(S_PQ + i16 * 32), q1 = ldsr<f32x4>(S_PQ + i16 * 32 + 16);
      const f32x4 s = s0 + s1, q = q0 + q1;
      const RowStat st = finish_stat((s[0] + s[1]) + (s[2] + s[3]), (q[0] + q[1]) + (q[2] + q[3]));
      if (lane < 16) { ldsw<float>(S_FIN + lane * 4, st.mu); ldsw<float>(S_FIN + 64 + lane * 4, st.rstd); }
    }
    f32x4 m = {0, 0, 0, 0};
#pragma unroll
    for (int ww = 0; ww < 8; ++ww) m += ldsr<f32x4>(S_PM + ww * 1024 + l16);
    f32x4 v;
    u32 off;
    if (kvmat) {
      float c1 = 0.f;
#pragma unroll
      for (int ww = 0; ww < 8; ++ww) c1 += ldsr<float>(S_PC + (ww * 16 + i16) * 4);
      asm volatile("s_waitcnt lgkmcnt(0)" ::: "memory");
      const f32x4 mu = ldsr<f32x4>(S_FIN + g4 * 16), rs = ldsr<f32x4>(S_FIN + 64 + g4 * 16);
      v = rs * (m - mu * c1);
      off = WS_V + (R0 >> 5) * 1024 + ((R0 >> 4) & 1) * 512 + (g4 & 1) * 256 + i16 * 16 + (g4 >> 1) * 8;
    } else {
      f32x4 c4 = {0, 0, 0, 0};
#pragma unroll
      for (int ww = 0; ww < 8; ++ww) c4 += ldsr<f32x4>(S_PC + (ww * 16 + 4 * g4) * 4);
      asm volatile("s_waitcnt lgkmcnt(0)" ::: "memory");
      const float mu = ldsr<float>(S_FIN + i16 * 4), rs = ldsr<float>(S_FIN + 64 + i16 * 4);
      v = rs * (m - mu * c4);
      off = WS_K + (R0 >> 5) * 1024 + (g4 & 1) * 512 + ((R0 & 31) + i16) * 16 + (g4 >> 1) * 8;
    }
    st8(wsr, off, 0, (u32x2){cvtpk(v[0], v[1]), cvtpk(v[2], v[3])});
    WAITV(1);
  } else {
    WAITV(0);
  }
#define JOB(i) retile_job((u32)(b * 20 + w * 5 + (i)), (u64)wo, (u64)w1, (u64)w2, (u64)w3, (u64)w4)
  u32x4 rts[5];
  if (w < 4) {
#pragma unroll
    for (int i = 0; i < 5; ++i) {
      const Job jb = JOB(i);
      rts[i] = *(const GAS u32x4*)(jb.src + (u32)((lane >> 2) * jb.prow + (lane & 3) * 16));
    }
  }
  FENCE();
  STAMP(2);
  const int q_hp = w & 1, q_rt = (w >> 1) & 1, q_kh = w >> 2;
  const int head = 2 * q_hp + q_kh;
  bf16x8 qf;
  {
    BAR();
    STAMP(3);
    {
      f32x4 as = {0, 0, 0, 0}, aq = {0, 0, 0, 0};
      const int rt = w & 3, kh = w >> 2;
      const u32 sa = L_XIMG + (16 * rt + i16) * XP + kh * 512 + g4 * 16;
#pragma unroll
      for (int t = 0; t < 8; ++t) {
        const bf16x8 xf = ldsr<bf16x8>(sa + t * 64);
        as = mfma16(one, xf, as); aq = mfma16(xf, xf, aq);
      }
      MFMA_GAP16(as); MFMA_GAP16(aq);
      if (lane < 16) ldsw<float>(S_QS + ((16 * rt + lane) * 2 + kh) * 4, as[0]);
      if ((i16 >> 2) == g4) ldsw<float>(S_QS + 512 + ((16 * rt + i16) * 2 + kh) * 4, diag_pick(aq, lane));
    }
    f32x16 acc = {}, accc = {};
    const int q = i16 >> 2, p = i16 & 3, cb = g4 & 1;
    const u32 wa = L_WQ + (256 * q_kh + 8 * h2 + q) * 128 + (((4 * q_hp + 2 * cb + (p >> 1)) ^ (4 * ((q >> 1) & 1))) * 16) + (p & 1) * 8;
    const u32 xa = L_XIMG + (32 * q_rt + r32) * XP + q_kh * 512 + h2 * 16;
#pragma unroll
    for (int t = 0; t < 16; ++t) {
      const bf16x8 af = cat(ldstr(wa + t * 2048), ldstr(wa + t * 2048 + 512));
      acc = mfma32(af, ldsr<bf16x8>(xa + t * 32), acc);
    }
    const u32 wb = wa + q_rt * 16384;
#pragma unroll
    for (int t = 0; t < 8; ++t) accc = mfma32(cat(ldstr(wb + t * 2048), ldstr(wb + t * 2048 + 512)), one, accc);
    if (w == 0) {
      WAITV(5);
      if (lane < 8) __hip_atomic_store((GAS u64*)(ws + WS_FKV + lane * 2048 + b * 8), tag, __ATOMIC_RELAXED, __HIP_MEMORY_SCOPE_AGENT);
    }
    MFMA_GAP32(acc); MFMA_GAP32(accc);
    f32x4 k0, k1, s0, s1;
    if (q_kh) {
      k0 = (f32x4){acc[8], acc[9], acc[10], acc[11]}; k1 = (f32x4){acc[12], acc[13], acc[14], acc[15]};
      s0 = (f32x4){acc[0], acc[1], acc[2], acc[3]}; s1 = (f32x4){acc[4], acc[5], acc[6], acc[7]};
    } else {
      k0 = (f32x4){acc[0], acc[1], acc[2], acc[3]}; k1 = (f32x4){acc[4], acc[5], acc[6], acc[7]};
      s0 = (f32x4){acc[8], acc[9], acc[10], acc[11]}; s1 = (f32x4){acc[12], acc[13], acc[14], acc[15]};
    }
    ldsw<f32x4>(L_WKV + w * 2048 + l16, s0); ldsw<f32x4>(L_WKV + w * 2048 + 1024 + l16, s1);
    if (r32 == 0) {
#pragma unroll
      for (int a = 0; a < 4; ++a) ldsw<f32x4>(S_QC + (((q_hp * 2 + q_rt) * 2 + q_kh) * 32 + 8 * a + 4 * h2) * 4, (f32x4){accc[4 * a], accc[4 * a + 1], accc[4 * a + 2], accc[4 * a + 3]});
    }
    BAR();
    k0 += ldsr<f32x4>(L_WKV + (w ^ 4) * 2048 + l16); k1 += ldsr<f32x4>(L_WKV + (w ^ 4) * 2048 + 1024 + l16);
    f32x4 c0 = {0, 0, 0, 0}, c1 = {0, 0, 0, 0};
#pragma unroll
    for (int s = 0; s < 4; ++s) {
      c0 += ldsr<f32x4>(S_QC + ((q_hp * 4 + s) * 32 + 16 * q_kh + 4 * h2) * 4);
      c1 += ldsr<f32x4>(S_QC + ((q_hp * 4 + s) * 32 + 16 * q_kh + 8 + 4 * h2) * 4);
    }
    const f32x2 sp = ldsr<f32x2>(S_QS + (32 * q_rt + r32) * 8), qp = ldsr<f32x2>(S_QS + 512 + (32 * q_rt + r32) * 8);
    const RowStat st = finish_stat(sp[0] + sp[1], qp[0] + qp[1]);
    const float sc = st.rstd * QSCALE;
    k0 = (k0 - st.mu * c0) * sc; k1 = (k1 - st.mu * c1) * sc;
    qf = opaque(asbf((u32x4){cvtpk(k0[0], k0[1]), cvtpk(k0[2], k0[3]), cvtpk(k1[0], k1[1]), cvtpk(k1[2], k1[3])}));
  }
  STAMP(4);
  auto blk_of = [&](int half) { return half * 32 + cl; };
  const int dlt = (j >> 1) == 0 ? 1 : (j >> 1) == 1 ? -1 : (j >> 1) == 2 ? 2 : -2;
  const u32 pc_in = (u32)((4 * j + (w >> 1)) * 1024 + (w & 1) * 512);
  auto hx_so = [&](u32 base, int half) { return base + blk_of(half) * 32768 + pc_in; };
  auto ts_vo = [&](int half) {
    const u32 l = opaque<u32>(lane);
    const int rr = (int)(l >> 1) + dlt;
    const u32 R = (u32)(blk_of(half) * 32 + rr);
    const u32 o = (u32)((rr >> 5) + 1) * 32768 + (u32)(rr & 31) * 16 + (l & 1) * 8;
    return R < 2048u && !(PUT16 && (l & 1)) ? o : OOB;
  };
  auto ox_off = [&]() { return WS_OX + blk_of(q_rt) * 32768 + (4 * j + head) * 1024 + l16; };
  {
    if (w == 1) {
      wait_flags(wsr, WS_FKV + (b & 7) * 2048, taglo, taghi, lane, t0);
      if (b == 0 && lane == 0) __hip_atomic_store((GAS u64*)(ws + WS_EPOCH), tag, __ATOMIC_RELAXED, __HIP_MEMORY_SCOPE_AGENT);
    }
    BAR();
    STAMP(5);
    dma_strided<3, true, 0x2000>(ws + WS_K, w * 1024 + l16, lb + L_K + w * 1024, 8192);
    dma_strided<2, true, 0x2000>(ws + WS_V, w * 1024 + l16, lb + L_V + w * 1024, 8192);
    if (tid < 272) ldsw<u32x4>(L_ONES + tid * 16, (u32x4){0x3F803F80u, 0x3F803F80u, 0x3F803F80u, 0x3F803F80u});
    if (tid == 511) ldsw<u32x2>(L_CNT, (u32x2){0u, 0u});
    WAITV(0);
    BAR();
    STAMP(6);
  }
  u32 opk[4];
  bf16x8 a_o[8], a_1[8], a_2[8], a3[32];
  u32x2 xres[2];
  {
    u32 ka = L_K + l16;
    const bool isv = r32 < 16;
    u32 va = isv ? L_V + h2 * 256 + r32 * 16 : L_ONES + ((r32 >= 20 && r32 < 28) ? 64 : 0);
    const u32 vinc = isv ? 4096u : 0u;
    f32x16 o = {};
    f32x16 negm;
    {
      const bf16x8 kf = ldsr<bf16x8>(ka);
      f32x16 z = {};
      const f32x16 s = mfma32(kf, qf, z);
      float m = fmaxf(fmaxf(s[0], s[1]), fmaxf(s[2], s[3]));
#pragma unroll
      for (int i = 4; i < 16; i += 2) m = fmaxf(m, fmaxf(s[i], s[i + 1]));
      const auto rr = __builtin_amdgcn_permlane32_swap(__float_as_uint(m), __float_as_uint(m), false, false);
      m = fmaxf(__uint_as_float(rr[0]), __uint_as_float(rr[1]));
#pragma unroll
      for (int i = 0; i < 16; ++i) negm[i] = -m;
    }
    f32x16 s = mfma32(ldsr<bf16x8>(ka), qf, negm);
    auto tiles4 = [&]() {
#pragma unroll
      for (int u = 0; u < 4; ++u) {
        const bf16x8 v0 = ldsr<bf16x8>(va + u * 1024), v1 = ldsr<bf16x8>(va + u * 1024 + 512);
        const bf16x8 kn = ldsr<bf16x8>(ka + (u + 1) * 1024);
        const f32x16 sn = mfma32(kn, qf, negm);
        u32 pk[8];
#pragma unroll
        for (int i = 0; i < 8; ++i) pk[i] = cvtpk(__builtin_amdgcn_exp2f(s[2 * i]), __builtin_amdgcn_exp2f(s[2 * i + 1]));
        o = mfma32(v0, asbf((u32x4){pk[0], pk[1], pk[2], pk[3]}), o);
        o = mfma32(v1, asbf((u32x4){pk[4], pk[5], pk[6], pk[7]}), o);
        s = sn;
      }
      va += vinc; ka += 4096;
    };
    if (w < 4) __builtin_amdgcn_s_setprio(2); else __builtin_amdgcn_s_setprio(1);
    auto kv_rest = [&](u32 img, u32 limg, int c0, auto n) {
      dma_strided<n.value, true, 4096>(ws + img + c0 * 8192, opaque<u32>((w & 3) * 1024 + l16), lb + limg + c0 * 8192 + (w & 3) * 1024, 4096);
    };
    using N2 = std::integral_constant<int, 2>; using N4 = std::integral_constant<int, 4>;
    auto lds_count = [&](u32 off) {
      u32 n;
      asm volatile("ds_add_rtn_u32 %0, %1, %2\n\ts_waitcnt lgkmcnt(0)" : "=v"(n) : "v"(lb + off), "v"(lane == 0 ? 1u : 0u) : "memory");
      return (u32)__builtin_amdgcn_readfirstlane(n);
    };
    const u32x4 s4 = {SENT, SENT, SENT, SENT};
    u32x4 fa, fb;
    auto fetch = [&](int p, u32 l) {
      const u32 fo = (2 * j * 32 + 4 * w) * 1024;
      if (p < 24) {
        const int t = p & 7;
        const bf16x8 v = asbf(ld16(wsr, l + (t & 3) * 1024, (p < 8 ? WS_WO : p < 16 ? WS_W1 : WS_W2) + fo + (t >> 2) * 32768));
        if (p < 8) a_o[t] = v; else if (p < 16) a_1[t] = v; else a_2[t] = v;
      } else if (p < 26) {
        xres[p - 24] = *(const GAS u32x2*)((const char*)x + (u32)((blk_of(p - 24) * 32 + (l >> 5)) * 1024 + (64 * j + 8 * w) * 2 + ((l >> 4) & 1) * 8));
      } else if (p < 26 + A3_N0) {
        a3[p - 26] = asbf(ld16(wsr, w < 7 ? ((7 * j + w) * 32) * 1024 + l : OOB, WS_W3 + (p - 26) * 1024));
      } else if (p < 33 + A3_N0) {
        dma1(ws + WS_W3 + (p - 26 - A3_N0) * 1024, (((u32)(b >> 6) & 3u) * 8 + w) * 7168 + j * 229376 + l, lb + L_DUM1);
      } else if (W4_WARM == 1 && p < 40 + A3_N0) {
        dma1(ws + WS_W4 + (p - 33 - A3_N0) * 1024, (((u32)(b >> 6) & 3u) * 8 + w) * 7168 + j * 229376 + l, lb + L_DUM1);
      }
    };
    static_for<0, 16>([&](auto IT) {
      constexpr int it = IT.value;
      ASTAMP(it, 0);
      if (w < 4) {
        if (it == 0) {
          const u32 l = opaque<u32>(lane);
          const u32 sb = L_STG + w * 5120;
#pragma unroll
          for (int i = 0; i < 5; ++i) ldsw<u32x4>(sb + i * 1024 + l * 16, rts[i]);
          const u32 tb = sb + ((l & 15) >> 2) * 64 + ((l >> 4) & 1) * 32 + (l & 3) * 8;
#pragma unroll
          for (int i = 0; i < 5; ++i) {
            const bool perm = JOB(i).perm;
            const u32 t0a = tb + i * 1024 + (l >> 5) * (perm ? 256 : 512);
            st16(wsr, l * 16, WS_WO + (b * 20 + w * 5 + i) * 1024, asu(cat(ldstr(t0a), ldstr(t0a + (perm ? 512 : 256)))));
          }
        } else if (it == 1) {
          const u32 l = opaque<u32>(lane);
#pragma unroll
          for (int half = 0; half < 2; ++half) {
            const u32 po = blk_of(half) * 32768 + (4 * j + w) * 1024;
            st16(wsr, l * 16, WS_OX + po, s4); st16(wsr, l * 16, WS_HX + po, s4); st16(wsr, l * 16, WS_X1 + po, s4);
            const int R = blk_of(half) * 32 + (int)(l & 31) + dlt;
            const u32 mk = (u32)R < 2048u ? SENT : 0u;
            st16(wsr, ((R & 2047) >> 5) * 32768 + (l >> 5) * 512 + (R & 31) * 16, WS_TS + (4 * j + w) * 1024, (u32x4){mk, mk, mk, mk});
          }
        } else if (it == 2) {
          const u32 uo = WS_UX + (blk_of(w >> 1) * 112 + 14 * j + 7 * (w & 1)) * 1024;
          const u32 l = opaque<u32>(l16);
#pragma unroll
          for (int i = 0; i < 7; ++i) st16(wsr, l + (i & 3) * 1024, uo + (i >> 2) * 4096, s4);
        } else if (it == 5) {
          WAITV(0);
          if (lds_count(L_CNT + 4) == 3 && lane < 8) __hip_atomic_store((GAS u64*)(ws + WS_FW + lane * 2048 + b * 8), tag, __ATOMIC_RELAXED, __HIP_MEMORY_SCOPE_AGENT);
        }
      } else {
        if (it == 0) {
          kv_rest(WS_V, L_V, 2, N4{}); kv_rest(WS_K, L_K, 3, N4{}); kv_rest(WS_V, L_V, 4, N4{}); kv_rest(WS_K, L_K, 5, N4{});
          kv_rest(WS_V, L_V, 6, N4{}); kv_rest(WS_K, L_K, 7, N2{});
        } else if (it == 1) {
          WAITV(0);
          lds_count(L_CNT);
        }
      }
      if (it == 4) {
        while (__builtin_amdgcn_readfirstlane(*(volatile LAS u32*)((LAS char*)smem + L_CNT)) != 4) {
          if (timed_out(t0)) break;
          __builtin_amdgcn_s_sleep(1);
        }
        FENCE();
      }
      if (it == 7) {
        const u32 l = opaque<u32>(l16);
        fa = ld16(wsr, l * 2, WS_FW + (b & 7) * 2048); fb = ld16(wsr, l * 2 + 16, WS_FW + (b & 7) * 2048);
      }
      if (it == 8) {
        if (!flags_ok(fa, fb, taglo, taghi)) wait_flags(wsr, WS_FW + (b & 7) * 2048, taglo, taghi, lane, t0);
      }
      if (it >= 8) {
        const u32 l = opaque<u32>(l16);
#pragma unroll
        for (int p = FPI * (it - 8); p < FPI * (it - 7); ++p) fetch(p, l);
      }
      if (it == 12 && w < 4) __builtin_amdgcn_s_setprio(0);
      FENCE();
      ASTAMP(it, 1);
      tiles4();
      FENCE();
      if (it == 9) STAMP(7);
    });
    STAMP(8);
    __builtin_amdgcn_s_setprio(0);
    ka = L_K + l16;
    float l = o[8];
    if (!__all(l <= 1.8446744e19f)) {
      float m = -INFINITY;
      o = (f32x16){};
      u32 vb = isv ? L_V + h2 * 256 + r32 * 16 : L_ONES;
      for (int t = 0; t < 64; ++t) {
        f32x16 z = {};
        const f32x16 sx = mfma32(ldsr<bf16x8>(ka + t * 1024), qf, z);
        float mt = sx[0];
#pragma unroll
        for (int i = 1; i < 16; ++i) mt = fmaxf(mt, sx[i]);
        const auto rr = __builtin_amdgcn_permlane32_swap(__float_as_uint(mt), __float_as_uint(mt), false, false);
        mt = fmaxf(fmaxf(__uint_as_float(rr[0]), __uint_as_float(rr[1])), m);
        const float f = __builtin_amdgcn_exp2f(m - mt);
        m = mt;
        u32 pk[8];
#pragma unroll
        for (int i = 0; i < 8; ++i) pk[i] = cvtpk(__builtin_amdgcn_exp2f(sx[2 * i] - m), __builtin_amdgcn_exp2f(sx[2 * i + 1] - m));
#pragma unroll
        for (int i = 0; i < 16; ++i) o[i] *= f;
        o = mfma32(ldsr<bf16x8>(vb), asbf((u32x4){pk[0], pk[1], pk[2], pk[3]}), o);
        o = mfma32(ldsr<bf16x8>(vb + 512), asbf((u32x4){pk[4], pk[5], pk[6], pk[7]}), o);
        vb += isv ? 1024u : 0u;
      }
      MFMA_GAP32(o);
      l = o[8];
    }
    const float rl = __builtin_amdgcn_rcpf(l);
#pragma unroll
    for (int i = 0; i < 4; ++i) opk[i] = canon(cvtpk(o[2 * i] * rl, o[2 * i + 1] * rl));
  }
  st16(wsr, ox_off(), 0, (u32x4){opk[0], opk[1], opk[2], opk[3]});
  STAMP(9);
  bf16x8 sel;
  auto make_sel = [&]() {
    const u32 l = opaque<u32>(lane), e = ((l >> 2) & 3) != (l >> 4) ? 0u : (l & 1) ? 0x3F800000u : 0x00003F80u;
    const u32 d0 = (l & 2) ? 0u : e, d1 = (l & 2) ? e : 0u;
    sel = asbf((u32x4){d0, d1, d0, d1});
  };
  make_sel();
  const u32 gvo = opaque<u32>(w * 4096 + l16) & 0x73F0u;
  auto pw_of = [&](u32 l) { return L_P + ((l >> 3) & 3) * 256 + (w >> 1) * 1024 + (l & 7) * 32 + (l >> 5) * 16 + (((w ^ (l >> 3)) & 1) * 8); };
  const u32 pw = pw_of(lane);
  const u32 pr = L_P + gvo;
  auto g_load = [&](bf16x8 (&f)[4], u32 src) {
#pragma unroll
    for (int t = 0; t < 4; ++t) f[t] = asbf(ld16(wsr, gvo | (t * 1024), src));
  };
  auto g_wait = [&](bf16x8 (&f)[4], u32 src) {
    if (__builtin_expect(unwritten(f), 0)) {
      do {
        if (timed_out(t0)) break;
        regather4(srd, gvo, src, f[0], f[1], f[2], f[3]);
      } while (unwritten(f));
    }
    asm volatile("" : "+v"(f[0]), "+v"(f[1]), "+v"(f[2]), "+v"(f[3]));
  };
  auto row_stats = [&](int half, const bf16x8 (&f)[4]) {
    float s0 = 0.f, s1 = 0.f, q0 = 0.f, q1 = 0.f;
#pragma unroll
    for (int t = 0; t < 4; ++t) {
      const u32x4 d = asu(f[t]);
      s0 = dot2(d[0], 0x3F803F80u, s0); q0 = dot2(d[0], d[0], q0); s1 = dot2(d[1], 0x3F803F80u, s1); q1 = dot2(d[1], d[1], q1);
      s0 = dot2(d[2], 0x3F803F80u, s0); q0 = dot2(d[2], d[2], q0); s1 = dot2(d[3], 0x3F803F80u, s1); q1 = dot2(d[3], d[3], q1);
    }
    const f32x2 sq = {swap32_add(s0 + s1), swap32_add(q0 + q1)};
    if (lane < 32) ldsw<f32x2>(L_STAT + half * STH + lane * STP + w * 8, sq);
  };
  auto partials = [&](int half, f32x16& acc0, f32x16& acc1, u32 pw) {
#pragma unroll
    for (int a = 0; a < 4; ++a) {
      ldsw<u32x2>(pw + half * 32768 + a * 4096, (u32x2){cvtpk(acc0[4 * a], acc0[4 * a + 1]), cvtpk(acc0[4 * a + 2], acc0[4 * a + 3])});
      ldsw<u32x2>(pw + half * 32768 + (4 + a) * 4096, (u32x2){cvtpk(acc1[4 * a], acc1[4 * a + 1]), cvtpk(acc1[4 * a + 2], acc1[4 * a + 3])});
    }
  };
  const u32 stw = opaque<u32>(L_STAT + r32 * STP + w * 8);
  auto slice_gemm = [&](int half, const bf16x8 (&a)[8], const bf16x8 (&f)[4], bool stats, auto&& mid) {
    f32x16 acc0 = {}, acc1 = {};
    float s0 = 0.f, q0 = 0.f;
#pragma unroll
    for (int t = 0; t < 4; ++t) {
      const u32x4 d = asu(f[t]);
      acc0 = mfma32(a[t], f[t], acc0);
      if (stats) { s0 = dot2(d[0], 0x3F803F80u, s0); q0 = dot2(d[0], d[0], q0); s0 = dot2(d[1], 0x3F803F80u, s0); q0 = dot2(d[1], d[1], q0); }
      __builtin_amdgcn_sched_barrier(0);
      acc1 = mfma32(a[4 + t], f[t], acc1);
      if (stats) { s0 = dot2(d[2], 0x3F803F80u, s0); q0 = dot2(d[2], d[2], q0); s0 = dot2(d[3], 0x3F803F80u, s0); q0 = dot2(d[3], d[3], q0); }
      __builtin_amdgcn_sched_barrier(0);
    }
    if (stats) ldsw<f32x2>(stw + half * STH, (f32x2){swap32_add(s0), swap32_add(q0)});
    mid();
    __builtin_amdgcn_sched_barrier(0);
    partials(half, acc0, acc1, pw);
  };
  auto reduce_at = [&](int half, u32 pr_) -> f32x4 {
    f32x4 acc = {0, 0, 0, 0};
#pragma unroll
    for (int q = 0; q < 4; ++q) acc = mfma16(sel, ldsr<bf16x8>(pr_ + half * 32768 + q * 1024), acc);
    return acc;
  };
  auto reduce = [&](int half) { return reduce_at(half, pr); };
  auto src_of = [&](u32 base, int half) { return base + blk_of(half) * 32768; };
  const u32 hvo = opaque<u32>(PUT16 && (lane & 1) ? OOB : (u32)lane * 8);
  auto put_at = [&](u32 vo, u32 so, u32x2 v) {
    if (PUT16) {
      const u32 n0 = __builtin_amdgcn_mov_dpp(v[0], 0xB1, 0xF, 0xF, true), n1 = __builtin_amdgcn_mov_dpp(v[1], 0xB1, 0xF, 0xF, true);
      st16(wsr, vo, so, (u32x4){v[0], v[1], n0, n1});
    } else st8(wsr, vo, so, v);
  };
  auto pack4 = [&](f32x4 v) { return (u32x2){canon(cvtpk(v[0], v[1])), canon(cvtpk(v[2], v[3]))}; };
  const u32 a3off = w < 7 ? ((7 * j + w) * 32) * 1024 + l16 : OOB;
  auto a3_load = [&](int t0_, int t1_) {
#pragma unroll
    for (int t = 0; t < 32; ++t) if (t >= t0_ && t < t1_) a3[t] = asbf(ld16(wsr, a3off, WS_W3 + t * 1024));
  };

  auto a3_slot = [&](int k) {
    constexpr int n = 32 - A3_N0;
    a3_load(A3_N0 + k * n / 6, A3_N0 + (k + 1) * n / 6);
  };
  f32x4 hres[2], xp[2], cs1;
  bf16x8 fA[4], fB[4];
  const u32 str = opaque<u32>(L_STAT + (lane >> 1) * STP + (lane & 1) * 32);
  auto back = [&](int half, bool stats, auto&& epi, auto&& hook) {
    bf16x8 p[4];
    f32x4 p0 = {0, 0, 0, 0}, p1 = {0, 0, 0, 0};
#pragma unroll
    for (int q = 0; q < 4; ++q) p[q] = ldsr<bf16x8>(pr + half * 32768 + q * 1024);
    if (stats) { p0 = ldsr<f32x4>(str + half * STH); p1 = ldsr<f32x4>(str + half * STH + 16); }
    hook();
    __builtin_amdgcn_sched_barrier(0);
    f32x4 acc = {0, 0, 0, 0};
#pragma unroll
    for (int q = 0; q < 4; ++q) acc = mfma16(sel, p[q], acc);
    RowStat st = {0.f, 0.f, 0.f};
    if (stats) { const f32x4 p = p0 + p1; st = finish_stat(xor1_add(p[0] + p[2]), xor1_add(p[1] + p[3])); }
    return epi(half, acc, st);
  };
  auto phase = [&](const bf16x8 (&a)[8], u32 src, bool stats, int gb, auto&& first, auto&& epi, auto&& put, auto&& pre, auto&& slotA, auto&& slotB, int sbase) {
    PSTAMP(sbase, 0);
    g_wait(fA, src_of(src, 0));
    STAMP(sbase);
    PSTAMP(sbase, 1);
    slice_gemm(0, a, fA, stats, [&]() {
      if (gb == 0) g_load(fB, src_of(src, 1));
      if (SLOT_POS == 0) slotA();
      first();
    });
    PSTAMP(sbase, 4);
    BAR();
    PSTAMP(sbase, 5);
    const u32x2 vA = back(0, stats, epi, [&]() { if (gb == 1) g_load(fB, src_of(src, 1)); });
    PSTAMP(sbase, 6);
    if (gb == 2) g_load(fB, src_of(src, 1));
    __builtin_amdgcn_sched_barrier(0);
    put(0, vA);
    if (SLOT_POS == 1) slotA();
    __builtin_amdgcn_sched_barrier(0);
    PSTAMP(sbase, 7);
    g_wait(fB, src_of(src, 1));
    STAMP(sbase + 1);
    PSTAMP(sbase, 8);
    slice_gemm(1, a, fB, stats, [&]() { if (SLOT_POS == 0) slotB(); });
    PSTAMP(sbase, 11);
    BAR();
    PSTAMP(sbase, 12);
    const u32x2 vB = back(1, stats, epi, [&]() {
      pre();
    });
    PSTAMP(sbase, 13);
    PSTAMP(sbase, 14);
    __builtin_amdgcn_sched_barrier(0);
    put(1, vB);
    if (SLOT_POS == 1) slotB();
    __builtin_amdgcn_sched_barrier(0);
    PSTAMP(sbase, 15);
  };
  const u32 a4off = (2 * j * 112 + 14 * w) * 1024 + l16;
  {
    float c[2] = {0.f, 0.f};
#pragma unroll
    for (int t = 0; t < 8; ++t) {
      const u32x4 d = asu(a_1[t]);
#pragma unroll
      for (int e = 0; e < 4; ++e) c[t >> 2] = dot2(d[e], 0x3F803F80u, c[t >> 2]);
    }
    const float c0 = swap32_add(c[0]), c1 = swap32_add(c[1]);
    ldsw<float>(L_CS1P + w * 256 + lane * 4, lane < 32 ? c0 : c1);
  }
  __builtin_amdgcn_sched_barrier(0);
  if (O_SLEEP) __builtin_amdgcn_s_sleep(O_SLEEP);
  g_load(fA, src_of(WS_OX, 0));
  g_load(fB, src_of(WS_OX, 1));
  __builtin_amdgcn_sched_barrier(0);
  phase(a_o, WS_OX, false, -1,
        [&]() { BAR(); },
        [&](int half, f32x4 acc, RowStat) {
          if (half == 1) {
            cs1 = (f32x4){0.f, 0.f, 0.f, 0.f};
#pragma unroll
            for (int v = 0; v < 8; ++v) cs1 += ldsr<f32x4>(L_CS1P + v * 256 + w * 32 + (lane & 1) * 16);
            cs1 = opaque(cs1);
          }
          hres[half] = acc + (f32x4){bflo(xres[half][0]), bfhi(xres[half][0]), bflo(xres[half][1]), bfhi(xres[half][1])};
          return pack4(hres[half]);
        },
        [&](int half, u32x2 v) { put_at(hvo, hx_so(WS_HX, half), v); },
        [&]() { g_load(fA, src_of(WS_HX, 0)); },
        [&]() {
          if (W4_WARM == 2) {
#pragma unroll
            for (int i = 0; i < 7; ++i) dma1(ws + WS_W4 + i * 1024, (((u32)(b >> 6) & 3u) * 8 + w) * 7168 + j * 229376 + l16, lb + L_DUM1);
          }
          a3_slot(0);
        },
        [&]() { a3_slot(1); }, 10);
  STAMP(12);
  const u32 tso[2] = {opaque<u32>(ts_vo(0)), opaque<u32>(ts_vo(1))};
  phase(a_1, WS_HX, true, GB_POS, [&]() { },
        [&](int half, f32x4 acc, RowStat st) {
          const float r2 = __builtin_amdgcn_rsqf(st.var * st.rstd * st.rstd + LN_EPS), rr = st.rstd * r2;
          const f32x4 pre = (acc - st.mu * cs1) * rr;
          const f32x2 g0 = gelu2((f32x2){pre[0], pre[1]}), g1 = gelu2((f32x2){pre[2], pre[3]});
          xp[half] = (hres[half] - st.mu) * st.rstd;
          return (u32x2){canon(cvtpk(g0[0], g0[1])), canon(cvtpk(g1[0], g1[1]))};
        },
        [&](int half, u32x2 v) { put_at(tso[half], hx_so(WS_TS, half) - 32768, v); },
        [&]() { g_load(fA, src_of(WS_TS, 0)); },
        [&]() { a3_slot(2); },
        [&]() { a3_slot(3); dma4_sc1(ws + WS_W4 + 10 * 1024, a4off, lb + L_A4 + w * 8192); }, 13);
  STAMP(15);
  phase(a_2, WS_TS, false, GB_POS, [&]() { },
        [&](int half, f32x4 acc, RowStat) {
          const f32x4 x1 = xp[half] + acc;
          ldsw<f32x4>(L_RES + half * 8192 + w * 1024 + l16, hres[half] + x1);
          return pack4(x1);
        },
        [&](int half, u32x2 v) { put_at(hvo, hx_so(WS_X1, half), v); },
        [&]() { dma4_sc1(ws + src_of(WS_X1, 0), gvo, lb + L_P + w * 4096); },
        [&]() { a3_slot(4); dma4_sc1(ws + WS_W4 + 114688 + 10 * 1024, a4off, lb + L_A4 + w * 8192 + 4096); },
        [&]() { a3_slot(5); }, 16);
  STAMP(18);
  bf16x8 a4[10][2];
  auto a4_load = [&](int n) { a4[n >> 1][n & 1] = asbf(ld16(wsr, a4off, WS_W4 + (n & 1) * 114688 + (n >> 1) * 1024)); };
  u32x4 uf[14];
  auto u_load1 = [&](int half, int t) { uf[t] = ld16(wsr, l16, WS_UX + (blk_of(half) * 112 + w * 14 + t) * 1024); };
  {
    auto x1_finish = [&](int half) {
#pragma unroll
      for (int t = 0; t < 4; ++t) fA[t] = ldsr<bf16x8>(pr + half * 32768 + t * 1024);
      if (__builtin_expect(unwritten(fA), 0)) {
        do {
          if (timed_out(t0)) break;
          __builtin_amdgcn_s_sleep(1);
          dma4_sc1(ws + src_of(WS_X1, half), gvo, lb + L_P + half * 32768 + w * 4096);
          WAITV(0);
#pragma unroll
          for (int t = 0; t < 4; ++t) fA[t] = ldsr<bf16x8>(pr + half * 32768 + t * 1024);
        } while (unwritten(fA));
      }
      row_stats(half, fA);
    };
    struct UOut { float rstd, nm; f32x4 cs[2]; float gv[4]; u32 up[8]; u32 l; };
    auto u_stat = [&](UOut& u, int half) {
      u.l = opaque<u32>(lane);
      const u32 sr = L_STAT + half * STH + (u.l & 31) * STP;
      const f32x4 p0 = ldsr<f32x4>(sr), p1 = ldsr<f32x4>(sr + 16), p2 = ldsr<f32x4>(sr + 32), p3 = ldsr<f32x4>(sr + 48);
      const f32x4 p = (p0 + p1) + (p2 + p3);
      const RowStat st = finish_stat(p[0] + p[2], p[1] + p[3]);
      u.rstd = st.rstd; u.nm = -st.mu * st.rstd;
    };
    auto u_cs = [&](UOut& u, int a) { u.cs[a & 1] = ldsr<f32x4>(L_CS + (32 * w + 8 * a) * 4 + (u.l >> 5) * 16); };
    auto u_val = [&](UOut& u, int half, const f32x16& acc, int v) {
      const int a = v >> 2, e = v & 3;
      u.gv[e] = gelu(acc[v] * u.rstd + u.nm * u.cs[a & 1][e]);
      if (e == 3) { u.up[2 * a] = canon(cvtpk(u.gv[0], u.gv[1])); u.up[2 * a + 1] = canon(cvtpk(u.gv[2], u.gv[3])); }
      if (v == 7 || v == 15) {
        const u32 uo = w < 7 ? WS_UX + (blk_of(half) * 112 + j * 14 + 2 * w) * 1024 + u.l * 16 : OOB;
        if (v == 7) st16(wsr, uo, 0, (u32x4){u.up[0], u.up[1], u.up[2], u.up[3]});
        else st16(wsr, uo, 1024, (u32x4){u.up[4], u.up[5], u.up[6], u.up[7]});
      }
    };
    WAITV(1);
    x1_finish(0);
    BAR();
    STAMP(19);
    dma4_sc1(ws + src_of(WS_X1, 1), gvo, lb + L_P + 32768 + w * 4096);
    const u32 xb = L_P + (w < 7 ? l16 : 0u);
    f32x16 accA = {}, accB = {};
    bf16x8 bq[4];
    {
      float c0 = 0.f, c1 = 0.f;
#pragma unroll
      for (int t = 0; t < 4; ++t) bq[t] = ldsr<bf16x8>(xb + t * 1024);
#pragma unroll
      for (int t = 0; t < 32; ++t) {
        accA = mfma32(a3[t], bq[t & 3], accA);
        if (t + 4 < 32) bq[t & 3] = ldsr<bf16x8>(xb + (t + 4) * 1024);
        const u32x4 d = asu(a3[t]);
        c0 = dot2(d[0], 0x3F803F80u, c0); c1 = dot2(d[1], 0x3F803F80u, c1); c0 = dot2(d[2], 0x3F803F80u, c0); c1 = dot2(d[3], 0x3F803F80u, c1);
        if (t >= 4 && (t & 3) == 0 && t / 4 - 1 < A4_N0) a4_load(t / 4 - 1);
        __builtin_amdgcn_sched_barrier(0);
      }
      const float c = swap32_add(c0 + c1);
      if (lane < 32) ldsw<float>(L_CS + w * 128 + lane * 4, c);
    }
    STAMP(20);
    asm volatile("s_waitcnt vmcnt(%0)" :: "n"(A4_N0) : "memory");
    x1_finish(1);
    BAR();
    STAMP(21);
    {
      UOut u;
#pragma unroll
      for (int t = 0; t < 4; ++t) bq[t] = ldsr<bf16x8>(xb + 32768 + t * 1024);
      u_stat(u, 0); u_cs(u, 0);
#pragma unroll
      for (int t = 0; t < 32; ++t) {
        if (t < 16 && (t & 3) == 1 && t + 3 < 16) u_cs(u, (t + 3) >> 2);
        accB = mfma32(a3[t], bq[t & 3], accB);
        if (t + 4 < 32) bq[t & 3] = ldsr<bf16x8>(xb + 32768 + (t + 4) * 1024);
        if (t < 16) u_val(u, 0, accA, t);
        const int n = A4_N0 + t - 1;
        if (t >= 1 && n < 20) a4_load(n);
        __builtin_amdgcn_sched_barrier(0);
      }
    }
    STAMP(22);
    {
      UOut u;
      u_stat(u, 1); u_cs(u, 0);
#pragma unroll
      for (int v = 0; v < 16; ++v) {
        if ((v & 3) == 0 && v + 4 < 16) u_cs(u, (v + 4) >> 2);
        if (v < 14) u_load1(0, v);
        u_val(u, 1, accB, v);
        __builtin_amdgcn_sched_barrier(0);
      }
    }
    STAMP(23);
  }
  {
    auto u_bad = [&]() {
      u32 m = 0;
#pragma unroll
      for (int t = 0; t < 14; ++t) m = umax(m, max4(uf[t]));
      return __any(m == SENT);
    };
    auto u_wait = [&](int half) {
      if (__builtin_expect(u_bad(), 0)) {
        do {
          if (timed_out(t0)) break;
          __builtin_amdgcn_s_sleep(2);
          const u32 so = WS_UX + (blk_of(half) * 112 + w * 14) * 1024, l = opaque<u32>(l16);
#pragma unroll
          for (int t = 0; t < 14; t += 2) regather2(srd, l, so + t * 1024, uf[t], uf[t + 1]);
          asm volatile("s_waitcnt vmcnt(0)" : "+v"(uf[0]), "+v"(uf[1]), "+v"(uf[2]), "+v"(uf[3]), "+v"(uf[4]), "+v"(uf[5]), "+v"(uf[6]), "+v"(uf[7]), "+v"(uf[8]), "+v"(uf[9]),
                       "+v"(uf[10]), "+v"(uf[11]), "+v"(uf[12]), "+v"(uf[13]) :: "memory");
        } while (u_bad());
      }
    };
#pragma unroll
    for (int half = 0; half < 2; ++half) {
      u_wait(half);
      STAMP(24 + half);
      const u32 sa = L_A4 + w * 8192 + opaque<u32>(l16);
      f32x16 acc0 = {}, acc1 = {};
      bf16x8 sq[4][2];
#pragma unroll
      for (int t = 0; t < 14; ++t) {
        if (t >= 8 && t < 12) { sq[t - 8][0] = ldsr<bf16x8>(sa + (t - 8) * 1024); sq[t - 8][1] = ldsr<bf16x8>(sa + 4096 + (t - 8) * 1024); }
        acc0 = mfma32(t < 10 ? a4[t < 10 ? t : 0][0] : sq[t < 10 ? 0 : t - 10][0], asbf(uf[t]), acc0);
        acc1 = mfma32(t < 10 ? a4[t < 10 ? t : 0][1] : sq[t < 10 ? 0 : t - 10][1], asbf(uf[t]), acc1);
        if (half == 0 && t >= 7) { u_load1(1, 2 * (t - 7)); u_load1(1, 2 * (t - 7) + 1); }
        __builtin_amdgcn_sched_barrier(0);
      }
      if (half == 0) BAR();
      partials(half, acc0, acc1, pw_of(opaque<u32>(lane)));
      FENCE();
    }
    STAMP(26);
    make_sel();
    BAR();
#pragma unroll
    for (int half = 0; half < 2; ++half) {
      const u32 l = opaque<u32>(lane);
      const f32x4 v = reduce_at(half, L_P + w * 4096 + l * 16) + ldsr<f32x4>(L_RES + half * 8192 + w * 1024 + l * 16);
      *(GAS u32x2*)((char*)out + (u32)((blk_of(half) * 32 + (l >> 1)) * 1024 + (64 * j + 8 * w + 4 * (l & 1)) * 2)) = (u32x2){cvtpk(v[0], v[1]), cvtpk(v[2], v[3])};
    }
  }
  STAMP(27);
}

extern "C" void kernel_launch(void* const* d_in, const int* in_sizes, int n_in, void* d_out, int out_size, void* d_ws, size_t ws_size, hipStream_t stream) {
  (void)in_sizes; (void)n_in; (void)out_size; (void)ws_size;
  fused_block_kernel<<<256, 512, 0, stream>>>((const u16*)d_in[0], (const u16*)d_in[1], (const u16*)d_in[2], (const u16*)d_in[3], (const u16*)d_in[4],
                                              (const u16*)d_in[6], (const u16*)d_in[7], (const u16*)d_in[8], (const u16*)d_in[9], (u16*)d_out, (char*)d_ws);
}
```

```cpp
#include <hip/hip_runtime.h>
#include <stdint.h>
#include <type_traits>

typedef unsigned short u16;
typedef unsigned int u32;
typedef unsigned long long u64;
typedef short bf16x8 __attribute__((ext_vector_type(8)));
typedef short s16x4 __attribute__((ext_vector_type(4)));
typedef float f32x2 __attribute__((ext_vector_type(2)));
typedef float f32x4 __attribute__((ext_vector_type(4)));
typedef float f32x16 __attribute__((ext_vector_type(16)));
typedef u32 u32x2 __attribute__((ext_vector_type(2)));
typedef u32 u32x4 __attribute__((ext_vector_type(4)));
typedef __bf16 bf16x2_t __attribute__((ext_vector_type(2)));
#define LAS __attribute__((address_space(3)))
#define GAS __attribute__((address_space(1)))
#define DI __device__ __forceinline__

constexpr u32 WS_EPOCH = 0x0;
constexpr u32 WS_FKV = 0x1000;
constexpr u32 WS_FW = 0x5000;
constexpr u32 WS_K = 0x10000;
constexpr u32 WS_V = 0x20000;
constexpr u32 WS_DBG = 0x1E00000;
constexpr u32 WS_WO = 0x100000, WS_W1 = 0x180000, WS_W2 = 0x200000, WS_W3 = 0x280000, WS_W4 = 0x440000;
constexpr u32 WS_OX = 0x600000, WS_HX = 0x800000, WS_TS = 0xA00000, WS_X1 = 0xC00000, WS_UX = 0xE00000;
constexpr u32 WS_STAMP = 0x2000000;
constexpr u32 WS_END = 0x2100000;
constexpr u32 OOB = 0x80000000u;

constexpr u32 XP = 1040;
constexpr u32 L_XIMG = 0;
constexpr u32 L_WQ = 66560;
constexpr u32 L_WKV = 132096;
constexpr u32 S_PM = 148480, S_PC = 156672, S_PS = 157184, S_PQ = 157696, S_FIN = 158208, S_QS = 158336, S_QC = 159360;
constexpr u32 L_DUM1 = 160384;
constexpr u32 L_CNT = 161408;
constexpr u32 L_K = 0, L_V = 65536, L_ONES = 131072;
constexpr u32 L_STG = 135424;
constexpr u32 L_CS1P = 155904;
constexpr u32 L_P = 0;
constexpr u32 L_STAT = 65536;
constexpr u32 STP = 80, STH = 2560;
constexpr u32 L_CS = 71680;
constexpr u32 L_RES = 73728;
constexpr u32 L_A4 = 90112;
constexpr u32 L_STAMP = 161792;
constexpr u32 LDS_BYTES = 163840;

__shared__ __attribute__((aligned(16))) char smem[LDS_BYTES];

constexpr u32 SENT = 0xFFFFFFFFu;
constexpr float LN_EPS = 1e-5f;
constexpr float QSCALE = 0.25f * 1.4426950408889634f;
#define CODE_KB 43

DI f32x16 mfma32(bf16x8 a, bf16x8 b, f32x16 c) { return __builtin_amdgcn_mfma_f32_32x32x16_bf16(a, b, c, 0, 0, 0); }
DI f32x4 mfma16(bf16x8 a, bf16x8 b, f32x4 c) { return __builtin_amdgcn_mfma_f32_16x16x32_bf16(a, b, c, 0, 0, 0); }
DI u32 cvtpk(float lo, float hi) { f32x2 v = {lo, hi}; bf16x2_t b = __builtin_convertvector(v, bf16x2_t); return __builtin_bit_cast(u32, b); }
DI u32 canon(u32 d) { return d < 0xFFFFFFFEu ? d : 0xFFFFFFFEu; }
DI float bflo(u32 d) { return __uint_as_float(d << 16); }
DI float bfhi(u32 d) { return __uint_as_float(d & 0xFFFF0000u); }
DI float dot2(u32 a, u32 b, float c) { return __builtin_amdgcn_fdot2_f32_bf16(__builtin_bit_cast(bf16x2_t, a), __builtin_bit_cast(bf16x2_t, b), c, false); }
DI float gelu(float x) {
  const float c1 = -2.0f * 0.7978845608028654f * 1.4426950408889634f, c2 = c1 * 0.044715f;
  const float e = __builtin_amdgcn_exp2f(x * (c1 + c2 * x * x));
  return x * __builtin_amdgcn_rcpf(1.0f + e);
}
DI f32x2 gelu2(f32x2 x) {
  const float c1 = -2.0f * 0.7978845608028654f * 1.4426950408889634f, c2 = c1 * 0.044715f;
  const f32x2 a = x * ((x * x) * c2 + c1);
  const f32x2 d = (f32x2){__builtin_amdgcn_exp2f(a[0]), __builtin_amdgcn_exp2f(a[1])} + 1.0f;
  return x * (f32x2){__builtin_amdgcn_rcpf(d[0]), __builtin_amdgcn_rcpf(d[1])};
}
DI float swap32_add(float v) {
  const auto r = __builtin_amdgcn_permlane32_swap(__float_as_uint(v), __float_as_uint(v), false, false);
  return __uint_as_float(r[0]) + __uint_as_float(r[1]);
}
DI float xor1_add(float v) { return v + __int_as_float(__builtin_amdgcn_mov_dpp(__float_as_int(v), 0xB1, 0xF, 0xF, true)); }

#define BAR() asm volatile("s_waitcnt lgkmcnt(0)\n\ts_barrier" ::: "memory")
#define WAITV(n) asm volatile("s_waitcnt vmcnt(" #n ")" ::: "memory")
#define FENCE() asm volatile("" ::: "memory")
template <class T> DI T opaque(T v) { asm volatile("" : "+v"(v)); return v; }

template <class T> DI T ldsr(u32 off) { return *(const LAS T*)((LAS char*)smem + off); }
template <class T> DI void ldsw(u32 off, T v) { *(LAS T*)((LAS char*)smem + off) = v; }
DI s16x4 ldstr(u32 off) {
  typedef short v4i16_t __attribute__((ext_vector_type(4)));
  return __builtin_bit_cast(s16x4, __builtin_amdgcn_ds_read_tr16_b64_v4i16((LAS v4i16_t*)((LAS char*)smem + off)));
}
DI bf16x8 cat(s16x4 a, s16x4 b) { return __builtin_shufflevector(a, b, 0, 1, 2, 3, 4, 5, 6, 7); }
DI u32 lds_base() { return (u32)(uintptr_t)(LAS char*)smem; }

#define DMA_HEAD "s_mov_b32 %[k], m0\n\ts_mov_b32 m0, %[lds]\n\ts_nop 0\n\t"
#define DMA_TAIL "s_mov_b32 m0, %[k]"
DI void dma1(const void* sbase, u32 voff, u32 ldsdst) {
  u32 keep;
  asm volatile(DMA_HEAD "global_load_lds_dwordx4 %[v], %[s]\n\t" DMA_TAIL : [k] "=&s"(keep) : [v] "v"(voff), [s] "s"(sbase), [lds] "s"(ldsdst) : "memory");
}
DI void dma2(const void* sbase, u32 voff, u32 ldsdst) {
  u32 keep;
  asm volatile(DMA_HEAD "global_load_lds_dwordx4 %[v], %[s]\n\tglobal_load_lds_dwordx4 %[v], %[s] offset:1024\n\t" DMA_TAIL
               : [k] "=&s"(keep) : [v] "v"(voff), [s] "s"(sbase), [lds] "s"(ldsdst) : "memory");
}
DI void dma4_rows(const void* sbase, u32 v0, u32 v1, u32 v2, u32 v3, u32 ldsdst) {
  u32 keep;
  asm volatile(DMA_HEAD "global_load_lds_dwordx4 %[a], %[s]\n\tglobal_load_lds_dwordx4 %[b], %[s] offset:1040\n\t"
               "global_load_lds_dwordx4 %[c], %[s] offset:2080\n\tglobal_load_lds_dwordx4 %[d], %[s] offset:3120\n\t" DMA_TAIL
               : [k] "=&s"(keep) : [a] "v"(v0), [b] "v"(v1), [c] "v"(v2), [d] "v"(v3), [s] "s"(sbase), [lds] "s"(ldsdst) : "memory");
}
DI void dma4_sc1(const void* sbase, u32 voff, u32 ldsdst) {
  u32 keep;
  asm volatile(DMA_HEAD "global_load_lds_dwordx4 %[v], %[s] sc1\n\tglobal_load_lds_dwordx4 %[v], %[s] offset:1024 sc1\n\t"
               "global_load_lds_dwordx4 %[v], %[s] offset:2048 sc1\n\tglobal_load_lds_dwordx4 %[v], %[s] offset:3072 sc1\n\t" DMA_TAIL
               : [k] "=&s"(keep) : [v] "v"(voff), [s] "s"(sbase), [lds] "s"(ldsdst) : "memory");
}
#define DMA_STEP(SC) "global_load_lds_dwordx4 %[v], %[s]" SC "\n\ts_add_u32 m0, m0, %[ls]\n\tv_add_u32 %[v], %[gs], %[v]\n\t"
template <int N, bool SC1, u32 GS>
DI void dma_strided(const void* sbase, u32 voff, u32 ldsdst, u32 lstep) {
  u32 keep;
  static_assert(N == 2 || N == 3 || N == 4 || N == 8, "");
#define DMA_RUN(BODY) asm volatile(DMA_HEAD BODY DMA_TAIL : [k] "=&s"(keep), [v] "+v"(voff) : [s] "s"(sbase), [lds] "s"(ldsdst), [ls] "s"(lstep), [gs] "n"(GS) : "memory", "scc")
  if constexpr (SC1) {
    if constexpr (N == 2) DMA_RUN(DMA_STEP(" sc1") DMA_STEP(" sc1"));
    if constexpr (N == 3) DMA_RUN(DMA_STEP(" sc1") DMA_STEP(" sc1") DMA_STEP(" sc1"));
    if constexpr (N == 4) DMA_RUN(DMA_STEP(" sc1") DMA_STEP(" sc1") DMA_STEP(" sc1") DMA_STEP(" sc1"));
  } else {
    if constexpr (N == 8) DMA_RUN(DMA_STEP("") DMA_STEP("") DMA_STEP("") DMA_STEP("") DMA_STEP("") DMA_STEP("") DMA_STEP("") DMA_STEP(""));
  }
#undef DMA_RUN
}

typedef __amdgpu_buffer_rsrc_t rsrc_t;
DI rsrc_t mkrsrc(const void* p, u32 bytes) { return __builtin_amdgcn_make_buffer_rsrc((void*)p, 0, bytes, 0x00020000); }
DI u32x4 ld16(rsrc_t r, u32 voff, u32 soff) { return __builtin_amdgcn_raw_buffer_load_b128(r, voff, soff, 16); }
DI void st16(rsrc_t r, u32 voff, u32 soff, u32x4 v) {
  __builtin_amdgcn_raw_buffer_store_b128(v, r, voff, soff, 16);
  asm volatile("s_nop 1" :: "v"(v) : "memory");
}
DI void st8(rsrc_t r, u32 voff, u32 soff, u32x2 v) { __builtin_amdgcn_raw_buffer_store_b64(v, r, voff, soff, 16); }
DI void st4(rsrc_t r, u32 voff, u32 soff, u32 v) { __builtin_amdgcn_raw_buffer_store_b32(v, r, voff, soff, 16); }
DI bf16x8 asbf(u32x4 v) { return __builtin_bit_cast(bf16x8, v); }
DI u32x4 asu(bf16x8 v) { return __builtin_bit_cast(u32x4, v); }
DI void regather4(u32x4 srd, u32 voff, u32 soff, bf16x8& f0, bf16x8& f1, bf16x8& f2, bf16x8& f3) {
  asm volatile("s_sleep 1\n\tbuffer_load_dwordx4 %0, %4, %5, %6 offen sc1\n\tbuffer_load_dwordx4 %1, %4, %5, %6 offen offset:1024 sc1\n\t"
               "buffer_load_dwordx4 %2, %4, %5, %6 offen offset:2048 sc1\n\tbuffer_load_dwordx4 %3, %4, %5, %6 offen offset:3072 sc1\n\ts_waitcnt vmcnt(0)"
               : "+v"(f0), "+v"(f1), "+v"(f2), "+v"(f3) : "v"(voff), "s"(srd), "s"(soff) : "memory");
}
DI void regather1(u32x4 srd, u32 voff, u32 soff, u32x4& f0) {
  asm volatile("buffer_load_dwordx4 %0, %1, %2, %3 offen sc1" : "=&v"(f0) : "v"(voff), "s"(srd), "s"(soff) : "memory");
}
DI u32 umax(u32 a, u32 b) { return a > b ? a : b; }
DI u32 max4(u32x4 v) { return umax(umax(v[0], v[1]), umax(v[2], v[3])); }

constexpr u64 SPIN_TICKS = 400000ull;
DI bool timed_out(u64 t0) { return __builtin_amdgcn_s_memrealtime() - t0 > SPIN_TICKS; }

#define STAMP(i) do { } while (0)
#define ASTAMP(it, pos) do { } while (0)
#define PSTAMP(sb, i) do { } while (0)
#define MFMA_GAP16(v) asm volatile("s_nop 7" : "+v"(v))
#define MFMA_GAP32(v) asm volatile("s_nop 7\n\ts_nop 3" : "+v"(v))

DI bool flags_ok(u32x4 a, u32x4 b, u32 lo, u32 hi) {
  return __all(a[0] == lo && a[1] == hi && a[2] == lo && a[3] == hi && b[0] == lo && b[1] == hi && b[2] == lo && b[3] == hi);
}
DI void wait_flags(rsrc_t wsr, u32 base, u32 taglo, u32 taghi, int lane, u64 t0) {
  for (;;) {
    const u32x4 a = ld16(wsr, lane * 32, base), b = ld16(wsr, lane * 32 + 16, base);
    if (flags_ok(a, b, taglo, taghi)) break;
    if (timed_out(t0)) break;
    __builtin_amdgcn_s_sleep(1);
    FENCE();
  }
}

DI bf16x8 ones_frag() { return asbf((u32x4){0x3F803F80u, 0x3F803F80u, 0x3F803F80u, 0x3F803F80u}); }
DI float diag_pick(f32x4 g, int lane) { const int r = lane & 3; float a = (r & 1) ? g[1] : g[0], b = (r & 1) ? g[3] : g[2]; return (r & 2) ? b : a; }

struct RowStat { float mu, rstd, var; };
DI RowStat finish_stat(float s, float q) {
  RowStat r; r.mu = s * (1.0f / 512.0f);
  r.var = fmaxf(q * (1.0f / 512.0f) - r.mu * r.mu, 0.0f);
  r.rstd = __builtin_amdgcn_rsqf(r.var + LN_EPS);
  return r;
}
DI bool unwritten(const bf16x8 (&f)[4]) {
  return __any(umax(umax(max4(asu(f[0])), max4(asu(f[1]))), umax(max4(asu(f[2])), max4(asu(f[3])))) == SENT);
}

template <int I, int N, class F> DI void static_for(F&& f) {
  if constexpr (I < N) { f(std::integral_constant<int, I>{}); static_for<I + 1, N>(f); }
}
struct Job { const char* src; u32 prow; bool perm; };
DI Job retile_job(u32 f, u64 wo, u64 w1, u64 w2, u64 w3, u64 w4) {
  const bool big = f >= 1536, last = f >= 3328;
  const u32 g = f - 3328, T = last ? g / 112 : big ? (f - 1536) >> 5 : (f >> 5) & 15, ks = last ? g % 112 : f & 31;
  const u32 prow = big && !last ? 3584 : 1024;
  u64 m = f < 1024 ? w1 : w2;
  m = f < 512 ? wo : m; m = big ? w3 : m; m = last ? w4 : m;
  return Job{(const char*)(m + (u64)(16 * ks * prow + T * 64)), prow, last || f < 512};
}

#define PRE_POS 0
#define PUT16 0
#define GB_POS 1
#define O_SLEEP 16
#define SLOT_POS 0
#define W4_WARM 0
#define FPI 6
#define U_IN 0
#define W4_PRIO Q_PRIO
#define A4_SPREAD 1
#define W3B_TOG 0
#define GB_PRIO Q_PRIO
#define UE_P 0
#define U_WIN 5
#define OUT_EARLY 1
#define MID_BAR 0
#define PQ 1
#define Q_PRIO 1
#ifndef M_PRIO
#define M_PRIO 1
#endif
#define U_INIT U_WIN
#define A4_LATE 0
#define STAT_POS 0
#define GB_PK 0
#define A3_N0 14
static_assert(33 + A3_N0 + (W4_WARM == 1 ? 7 : 0) <= 8 * FPI, "fetch schedule");
#define A4_N0 6

__global__ __launch_bounds__(512, 2) void fused_block_kernel(const u16* __restrict__ x, const u16* __restrict__ wq, const u16* __restrict__ wk,
                                                             const u16* __restrict__ wv, const u16* __restrict__ wo, const u16* __restrict__ w1,
                                                             const u16* __restrict__ w2, const u16* __restrict__ w3, const u16* __restrict__ w4,
                                                             u16* __restrict__ out, char* ws) {
  const int tid = threadIdx.x, lane = tid & 63, w = __builtin_amdgcn_readfirstlane(tid >> 6);
  const int b = blockIdx.x;
  const int cl = (b & 7) * 4 + (b >> 6), j = (b >> 3) & 7;
  const u32 lb = lds_base();
  const u32 l16 = lane * 16;
  {
    u64 pc;
    asm volatile("s_getpc_b64 %0" : "=s"(pc));
    dma1((const void*)(pc & ~63ull), (u32)((((b >> 3) * 8 + w) % CODE_KB) * 1024) + l16, lb + L_DUM1);
  }
  asm volatile("" :: "s"(x), "s"(wq), "s"(wk), "s"(wv), "s"(wo), "s"(w1), "s"(w2), "s"(w3), "s"(w4), "s"(out), "s"(ws));
  const u64 t0 = __builtin_amdgcn_s_memrealtime();
  const int i16 = lane & 15, g4 = lane >> 4, r32 = lane & 31, h2 = lane >> 5;
  STAMP(0);

  const int kvrt = j >> 1, kvmat = j & 1;
  auto grow = [&](int rho) { return (rho < 32 ? 0 : 1024 - 32) + 32 * cl + rho; };
  const int R0 = grow(16 * kvrt);
  u32x4 xk0, xk1;
  u64 tag;
  {
    const u32 xo = (u32)(i16 * 1024 + g4 * 32);
    asm volatile("global_load_dwordx4 %0, %2, %3\n\tglobal_load_dwordx4 %1, %2, %3 offset:16"
                 : "=&v"(xk0), "=&v"(xk1) : "v"(xo), "s"((const char*)x + R0 * 1024 + w * 128) : "memory");
    dma2((const char*)(kvmat ? wv : wk) + w * 2048, l16, lb + L_WKV + w * 2048);
    asm volatile("s_load_dwordx2 %0, %1, 0x0" : "=s"(tag) : "s"(ws + WS_EPOCH) : "memory");
    const char* xr = (const char*)x + grow(8 * w) * 1024 - 48;
    dma4_rows(xr, l16 + 48, l16 + 32, l16 + 16, l16, lb + L_XIMG + 8 * w * XP);
    dma4_rows(xr + 4096, l16 + 48, l16 + 32, l16 + 16, l16, lb + L_XIMG + (8 * w + 4) * XP);
    dma_strided<8, false, 0x2000>((const char*)wq + w * 65536 + j * 128, (u32)((lane >> 3) * 1024 + (((lane & 7) ^ (4 * ((lane >> 4) & 1))) * 16)), lb + L_WQ + w * 8192, 1024);
    asm volatile("s_waitcnt vmcnt(16) lgkmcnt(0)" : "+v"(xk0), "+v"(xk1), "+s"(tag) :: "memory");
  }
  tag += 1ull;
  const u32 taglo = (u32)tag, taghi = (u32)(tag >> 32);
  const rsrc_t wsr = mkrsrc(ws, WS_END);
  const u32x4 srd = {(u32)(uintptr_t)ws, (u32)((uintptr_t)ws >> 32) & 0xFFFFu, WS_END, 0x00020000u};
  const bf16x8 one = ones_frag();
  STAMP(1);

  {
    f32x4 am = {0, 0, 0, 0}, ac = {0, 0, 0, 0}, as = {0, 0, 0, 0}, aq = {0, 0, 0, 0};
    const u32 wa = L_WKV + w * 2048 + (16 * g4 + (i16 >> 2)) * 32 + (i16 & 3) * 8;
    const bf16x8 wf0 = cat(ldstr(wa), ldstr(wa + 128)), wf1 = cat(ldstr(wa + 256), ldstr(wa + 384));
    const bf16x8 xf0 = asbf(xk0), xf1 = asbf(xk1);
    as = mfma16(one, xf0, as); aq = mfma16(xf0, xf0, aq); as = mfma16(one, xf1, as); aq = mfma16(xf1, xf1, aq);
    if (kvmat) {
      am = mfma16(xf0, wf0, am); ac = mfma16(one, wf0, ac); am = mfma16(xf1, wf1, am); ac = mfma16(one, wf1, ac);
      MFMA_GAP16(am); MFMA_GAP16(ac);
      if (lane < 16) ldsw<float>(S_PC + (w * 16 + lane) * 4, ac[0]);
    } else {
      am = mfma16(wf0, xf0, am); ac = mfma16(wf0, one, ac); am = mfma16(wf1, xf1, am); ac = mfma16(wf1, one, ac);
      MFMA_GAP16(am); MFMA_GAP16(ac);
      if (i16 == 0) ldsw<f32x4>(S_PC + (w * 16 + 4 * g4) * 4, ac);
    }
    ldsw<f32x4>(S_PM + w * 1024 + l16, am);
    if (lane < 16) ldsw<float>(S_PS + (lane * 8 + w) * 4, as[0]);
    if ((i16 >> 2) == g4) ldsw<float>(S_PQ + (i16 * 8 + w) * 4, diag_pick(aq, lane));
    BAR();
  }
  if (w == 0) {
    {
      const f32x4 s0 = ldsr<f32x4>(S_PS + i16 * 32), s1 = ldsr<f32x4>(S_PS + i16 * 32 + 16);
      const f32x4 q0 = ldsr<f32x4>(S_PQ + i16 * 32), q1 = ldsr<f32x4>(S_PQ + i16 * 32 + 16);
      const f32x4 s = s0 + s1, q = q0 + q1;
      const RowStat st = finish_stat((s[0] + s[1]) + (s[2] + s[3]), (q[0] + q[1]) + (q[2] + q[3]));
      if (lane < 16) { ldsw<float>(S_FIN + lane * 4, st.mu); ldsw<float>(S_FIN + 64 + lane * 4, st.rstd); }
    }
    f32x4 m = {0, 0, 0, 0};
#pragma unroll
    for (int ww = 0; ww < 8; ++ww) m += ldsr<f32x4>(S_PM + ww * 1024 + l16);
    f32x4 v;
    u32 off;
    if (kvmat) {
      float c1 = 0.f;
#pragma unroll
      for (int ww = 0; ww < 8; ++ww) c1 += ldsr<float>(S_PC + (ww * 16 + i16) * 4);
      asm volatile("s_waitcnt lgkmcnt(0)" ::: "memory");
      const f32x4 mu = ldsr<f32x4>(S_FIN + g4 * 16), rs = ldsr<f32x4>(S_FIN + 64 + g4 * 16);
      v = rs * (m - mu * c1);
      off = WS_V + (R0 >> 5) * 1024 + ((R0 >> 4) & 1) * 512 + (g4 & 1) * 256 + i16 * 16 + (g4 >> 1) * 8;
    } else {
      f32x4 c4 = {0, 0, 0, 0};
#pragma unroll
      for (int ww = 0; ww < 8; ++ww) c4 += ldsr<f32x4>(S_PC + (ww * 16 + 4 * g4) * 4);
      asm volatile("s_waitcnt lgkmcnt(0)" ::: "memory");
      const float mu = ldsr<float>(S_FIN + i16 * 4), rs = ldsr<float>(S_FIN + 64 + i16 * 4);
      v = rs * (m - mu * c4);
      off = WS_K + (R0 >> 5) * 1024 + (g4 & 1) * 512 + ((R0 & 31) + i16) * 16 + (g4 >> 1) * 8;
    }
    st8(wsr, off, 0, (u32x2){cvtpk(v[0], v[1]), cvtpk(v[2], v[3])});
    WAITV(1);
  } else {
    WAITV(0);
  }
#define JOB(i) retile_job((u32)(b * 20 + w * 5 + (i)), (u64)wo, (u64)w1, (u64)w2, (u64)w3, (u64)w4)
  u32x4 rts[5];
  if (w < 4) {
#pragma unroll
    for (int i = 0; i < 5; ++i) {
      const Job jb = JOB(i);
      rts[i] = *(const GAS u32x4*)(jb.src + (u32)((lane >> 2) * jb.prow + (lane & 3) * 16));
    }
  }
  FENCE();
  STAMP(2);
  const int q_hp = w & 1, q_rt = (w >> 1) & 1, q_kh = w >> 2;
  const int head = 2 * q_hp + q_kh;
  bf16x8 qf;
  {
    BAR();
    STAMP(3);
    {
      f32x4 as = {0, 0, 0, 0}, aq = {0, 0, 0, 0};
      const int rt = w & 3, kh = w >> 2;
      const u32 sa = L_XIMG + (16 * rt + i16) * XP + kh * 512 + g4 * 16;
#pragma unroll
      for (int t = 0; t < 8; ++t) {
        const bf16x8 xf = ldsr<bf16x8>(sa + t * 64);
        as = mfma16(one, xf, as); aq = mfma16(xf, xf, aq);
      }
      MFMA_GAP16(as); MFMA_GAP16(aq);
      if (lane < 16) ldsw<float>(S_QS + ((16 * rt + lane) * 2 + kh) * 4, as[0]);
      if ((i16 >> 2) == g4) ldsw<float>(S_QS + 512 + ((16 * rt + i16) * 2 + kh) * 4, diag_pick(aq, lane));
    }
    f32x16 acc = {}, accc = {};
    const int q = i16 >> 2, p = i16 & 3, cb = g4 & 1;
    const u32 wa = L_WQ + (256 * q_kh + 8 * h2 + q) * 128 + (((4 * q_hp + 2 * cb + (p >> 1)) ^ (4 * ((q >> 1) & 1))) * 16) + (p & 1) * 8;
    const u32 xa = L_XIMG + (32 * q_rt + r32) * XP + q_kh * 512 + h2 * 16;
#pragma unroll
    for (int t = 0; t < 16; ++t) {
      const bf16x8 af = cat(ldstr(wa + t * 2048), ldstr(wa + t * 2048 + 512));
      acc = mfma32(af, ldsr<bf16x8>(xa + t * 32), acc);
    }
    const u32 wb = wa + q_rt * 16384;
#pragma unroll
    for (int t = 0; t < 8; ++t) accc = mfma32(cat(ldstr(wb + t * 2048), ldstr(wb + t * 2048 + 512)), one, accc);
    if (w == 0) {
      WAITV(5);
      if (lane < 8) __hip_atomic_store((GAS u64*)(ws + WS_FKV + lane * 2048 + b * 8), tag, __ATOMIC_RELAXED, __HIP_MEMORY_SCOPE_AGENT);
    }
    MFMA_GAP32(acc); MFMA_GAP32(accc);
    f32x4 k0, k1, s0, s1;
    if (q_kh) {
      k0 = (f32x4){acc[8], acc[9], acc[10], acc[11]}; k1 = (f32x4){acc[12], acc[13], acc[14], acc[15]};
      s0 = (f32x4){acc[0], acc[1], acc[2], acc[3]}; s1 = (f32x4){acc[4], acc[5], acc[6], acc[7]};
    } else {
      k0 = (f32x4){acc[0], acc[1], acc[2], acc[3]}; k1 = (f32x4){acc[4], acc[5], acc[6], acc[7]};
      s0 = (f32x4){acc[8], acc[9], acc[10], acc[11]}; s1 = (f32x4){acc[12], acc[13], acc[14], acc[15]};
    }
    ldsw<f32x4>(L_WKV + w * 2048 + l16, s0); ldsw<f32x4>(L_WKV + w * 2048 + 1024 + l16, s1);
    if (r32 == 0) {
#pragma unroll
      for (int a = 0; a < 4; ++a) ldsw<f32x4>(S_QC + (((q_hp * 2 + q_rt) * 2 + q_kh) * 32 + 8 * a + 4 * h2) * 4, (f32x4){accc[4 * a], accc[4 * a + 1], accc[4 * a + 2], accc[4 * a + 3]});
    }
    BAR();
    k0 += ldsr<f32x4>(L_WKV + (w ^ 4) * 2048 + l16); k1 += ldsr<f32x4>(L_WKV + (w ^ 4) * 2048 + 1024 + l16);
    f32x4 c0 = {0, 0, 0, 0}, c1 = {0, 0, 0, 0};
#pragma unroll
    for (int s = 0; s < 4; ++s) {
      c0 += ldsr<f32x4>(S_QC + ((q_hp * 4 + s) * 32 + 16 * q_kh + 4 * h2) * 4);
      c1 += ldsr<f32x4>(S_QC + ((q_hp * 4 + s) * 32 + 16 * q_kh + 8 + 4 * h2) * 4);
    }
    const f32x2 sp = ldsr<f32x2>(S_QS + (32 * q_rt + r32) * 8), qp = ldsr<f32x2>(S_QS + 512 + (32 * q_rt + r32) * 8);
    const RowStat st = finish_stat(sp[0] + sp[1], qp[0] + qp[1]);
    const float sc = st.rstd * QSCALE;
    k0 = (k0 - st.mu * c0) * sc; k1 = (k1 - st.mu * c1) * sc;
    qf = opaque(asbf((u32x4){cvtpk(k0[0], k0[1]), cvtpk(k0[2], k0[3]), cvtpk(k1[0], k1[1]), cvtpk(k1[2], k1[3])}));
  }
  STAMP(4);
  auto blk_of = [&](int half) { return half * 32 + cl; };
  const int dlt = (j >> 1) == 0 ? 1 : (j >> 1) == 1 ? -1 : (j >> 1) == 2 ? 2 : -2;
  const u32 pc_in = (u32)((4 * j + (w >> 1)) * 1024 + (w & 1) * 512);
  auto hx_so = [&](u32 base, int half) { return base + blk_of(half) * 32768 + pc_in; };
  auto ts_vo = [&](int half) {
    const u32 l = opaque<u32>(lane);
    const int rr = (int)(l >> 1) + dlt;
    const u32 R = (u32)(blk_of(half) * 32 + rr);
    const u32 o = (u32)((rr >> 5) + 1) * 32768 + (u32)(rr & 31) * 16 + (l & 1) * 8;
    return R < 2048u && !(PUT16 && (l & 1)) ? o : OOB;
  };
  auto ox_off = [&]() { return WS_OX + blk_of(q_rt) * 32768 + (4 * j + head) * 1024 + l16; };
  {
    if (w == 1) {
      wait_flags(wsr, WS_FKV + (b & 7) * 2048, taglo, taghi, lane, t0);
      if (b == 0 && lane == 0) __hip_atomic_store((GAS u64*)(ws + WS_EPOCH), tag, __ATOMIC_RELAXED, __HIP_MEMORY_SCOPE_AGENT);
    }
    BAR();
    STAMP(5);
    dma_strided<3, true, 0x2000>(ws + WS_K, w * 1024 + l16, lb + L_K + w * 1024, 8192);
    dma_strided<2, true, 0x2000>(ws + WS_V, w * 1024 + l16, lb + L_V + w * 1024, 8192);
    if (tid < 272) ldsw<u32x4>(L_ONES + tid * 16, (u32x4){0x3F803F80u, 0x3F803F80u, 0x3F803F80u, 0x3F803F80u});
    if (tid == 511) ldsw<u32x4>(L_CNT, (u32x4){0u, 0u, 0u, 0u});
    WAITV(0);
    BAR();
    STAMP(6);
  }
  u32 opk[4];
  bf16x8 a_o[8], a_1[8], a_2[8], a3[32];
  u32x2 xres[2];
  {
    u32 ka = L_K + l16;
    const bool isv = r32 < 16;
    u32 va = isv ? L_V + h2 * 256 + r32 * 16 : L_ONES + ((r32 >= 20 && r32 < 28) ? 64 : 0);
    const u32 vinc = isv ? 4096u : 0u;
    f32x16 o = {};
    f32x16 negm;
    {
      const bf16x8 kf = ldsr<bf16x8>(ka);
      f32x16 z = {};
      const f32x16 s = mfma32(kf, qf, z);
      float m = fmaxf(fmaxf(s[0], s[1]), fmaxf(s[2], s[3]));
#pragma unroll
      for (int i = 4; i < 16; i += 2) m = fmaxf(m, fmaxf(s[i], s[i + 1]));
      const auto rr = __builtin_amdgcn_permlane32_swap(__float_as_uint(m), __float_as_uint(m), false, false);
      m = fmaxf(__uint_as_float(rr[0]), __uint_as_float(rr[1]));
#pragma unroll
      for (int i = 0; i < 16; ++i) negm[i] = -m;
    }
    f32x16 s = mfma32(ldsr<bf16x8>(ka), qf, negm);
    auto tiles4 = [&]() {
#pragma unroll
      for (int u = 0; u < 4; ++u) {
        const bf16x8 v0 = ldsr<bf16x8>(va + u * 1024), v1 = ldsr<bf16x8>(va + u * 1024 + 512);
        const bf16x8 kn = ldsr<bf16x8>(ka + (u + 1) * 1024);
        const f32x16 sn = mfma32(kn, qf, negm);
        u32 pk[8];
#pragma unroll
        for (int i = 0; i < 8; ++i) pk[i] = cvtpk(__builtin_amdgcn_exp2f(s[2 * i]), __builtin_amdgcn_exp2f(s[2 * i + 1]));
        o = mfma32(v0, asbf((u32x4){pk[0], pk[1], pk[2], pk[3]}), o);
        o = mfma32(v1, asbf((u32x4){pk[4], pk[5], pk[6], pk[7]}), o);
        s = sn;
      }
      va += vinc; ka += 4096;
    };
    if (w < 4) __builtin_amdgcn_s_setprio(2); else __builtin_amdgcn_s_setprio(1);
    auto kv_rest = [&](u32 img, u32 limg, int c0, auto n) {
      dma_strided<n.value, true, 4096>(ws + img + c0 * 8192, opaque<u32>((w & 3) * 1024 + l16), lb + limg + c0 * 8192 + (w & 3) * 1024, 4096);
    };
    using N2 = std::integral_constant<int, 2>; using N4 = std::integral_constant<int, 4>;
    auto lds_count = [&](u32 off) {
      u32 n;
      asm volatile("ds_add_rtn_u32 %0, %1, %2\n\ts_waitcnt lgkmcnt(0)" : "=v"(n) : "v"(lb + off), "v"(lane == 0 ? 1u : 0u) : "memory");
      return (u32)__builtin_amdgcn_readfirstlane(n);
    };
    const u32x4 s4 = {SENT, SENT, SENT, SENT};
    u32x4 fa, fb;
    auto fetch = [&](int p, u32 l) {
      const u32 fo = (2 * j * 32 + 4 * w) * 1024;
      if (p < 24) {
        const int t = p & 7;
        const bf16x8 v = asbf(ld16(wsr, l + (t & 3) * 1024, (p < 8 ? WS_WO : p < 16 ? WS_W1 : WS_W2) + fo + (t >> 2) * 32768));
        if (p < 8) a_o[t] = v; else if (p < 16) a_1[t] = v; else a_2[t] = v;
      } else if (p < 26) {
        xres[p - 24] = *(const GAS u32x2*)((const char*)x + (u32)((blk_of(p - 24) * 32 + (l >> 5)) * 1024 + (64 * j + 8 * w) * 2 + ((l >> 4) & 1) * 8));
      } else if (p < 26 + A3_N0) {
        a3[p - 26] = asbf(ld16(wsr, w < 7 ? ((7 * j + w) * 32) * 1024 + l : OOB, WS_W3 + (p - 26) * 1024));
      } else if (p < 33 + A3_N0) {
        dma1(ws + WS_W3 + (p - 26 - A3_N0) * 1024, (((u32)(b >> 6) & 3u) * 8 + w) * 7168 + j * 229376 + l, lb + L_DUM1);
      } else if (W4_WARM == 1 && p < 40 + A3_N0) {
        dma1(ws + WS_W4 + (p - 33 - A3_N0) * 1024, (((u32)(b >> 6) & 3u) * 8 + w) * 7168 + j * 229376 + l, lb + L_DUM1);
      }
    };
    static_for<0, 16>([&](auto IT) {
      constexpr int it = IT.value;
      ASTAMP(it, 0);
      if (w < 4) {
        if (it == 0) {
          const u32 l = opaque<u32>(lane);
          const u32 sb = L_STG + w * 5120;
#pragma unroll
          for (int i = 0; i < 5; ++i) ldsw<u32x4>(sb + i * 1024 + l * 16, rts[i]);
          const u32 tb = sb + ((l & 15) >> 2) * 64 + ((l >> 4) & 1) * 32 + (l & 3) * 8;
#pragma unroll
          for (int i = 0; i < 5; ++i) {
            const bool perm = JOB(i).perm;
            const u32 t0a = tb + i * 1024 + (l >> 5) * (perm ? 256 : 512);
            st16(wsr, l * 16, WS_WO + (b * 20 + w * 5 + i) * 1024, asu(cat(ldstr(t0a), ldstr(t0a + (perm ? 512 : 256)))));
          }
        } else if (it == 1) {
          const u32 l = opaque<u32>(lane);
#pragma unroll
          for (int half = 0; half < 2; ++half) {
            const u32 po = blk_of(half) * 32768 + (4 * j + w) * 1024;
            st16(wsr, l * 16, WS_OX + po, s4); st16(wsr, l * 16, WS_HX + po, s4); st16(wsr, l * 16, WS_X1 + po, s4);
            const int R = blk_of(half) * 32 + (int)(l & 31) + dlt;
            const u32 mk = (u32)R < 2048u ? SENT : 0u;
            st16(wsr, ((R & 2047) >> 5) * 32768 + (l >> 5) * 512 + (R & 31) * 16, WS_TS + (4 * j + w) * 1024, (u32x4){mk, mk, mk, mk});
          }
        } else if (it == 2) {
          const u32 uo = WS_UX + (blk_of(w >> 1) * 112 + 14 * j + 7 * (w & 1)) * 1024;
          const u32 l = opaque<u32>(l16);
#pragma unroll
          for (int i = 0; i < 7; ++i) st16(wsr, l + (i & 3) * 1024, uo + (i >> 2) * 4096, s4);
        } else if (it == 5) {
          WAITV(0);
          if (lds_count(L_CNT + 4) == 3 && lane < 8) __hip_atomic_store((GAS u64*)(ws + WS_FW + lane * 2048 + b * 8), tag, __ATOMIC_RELAXED, __HIP_MEMORY_SCOPE_AGENT);
        }
      } else {
        if (it == 0) {
          kv_rest(WS_V, L_V, 2, N4{}); kv_rest(WS_K, L_K, 3, N4{}); kv_rest(WS_V, L_V, 4, N4{}); kv_rest(WS_K, L_K, 5, N4{});
          kv_rest(WS_V, L_V, 6, N4{}); kv_rest(WS_K, L_K, 7, N2{});
        } else if (it == 1) {
          WAITV(0);
          lds_count(L_CNT);
        }
      }
      if (it == 4) {
        while (__builtin_amdgcn_readfirstlane(*(volatile LAS u32*)((LAS char*)smem + L_CNT)) != 4) {
          if (timed_out(t0)) break;
          __builtin_amdgcn_s_sleep(1);
        }
        FENCE();
      }
      if (it == 7) {
        const u32 l = opaque<u32>(l16);
        fa = ld16(wsr, l * 2, WS_FW + (b & 7) * 2048); fb = ld16(wsr, l * 2 + 16, WS_FW + (b & 7) * 2048);
      }
      if (it == 8) {
        if (!flags_ok(fa, fb, taglo, taghi)) wait_flags(wsr, WS_FW + (b & 7) * 2048, taglo, taghi, lane, t0);
      }
      if (it >= 8) {
        const u32 l = opaque<u32>(l16);
#pragma unroll
        for (int p = FPI * (it - 8); p < FPI * (it - 7); ++p) fetch(p, l);
      }
      if (it == 12 && w < 4) __builtin_amdgcn_s_setprio(0);
      FENCE();
      ASTAMP(it, 1);
      tiles4();
      FENCE();
      if (it == 9) STAMP(7);
    });
    STAMP(8);
    __builtin_amdgcn_s_setprio(0);
    ka = L_K + l16;
    float l = o[8];
    if (!__all(l <= 1.8446744e19f)) {
      float m = -INFINITY;
      o = (f32x16){};
      u32 vb = isv ? L_V + h2 * 256 + r32 * 16 : L_ONES;
      for (int t = 0; t < 64; ++t) {
        f32x16 z = {};
        const f32x16 sx = mfma32(ldsr<bf16x8>(ka + t * 1024), qf, z);
        float mt = sx[0];
#pragma unroll
        for (int i = 1; i < 16; ++i) mt = fmaxf(mt, sx[i]);
        const auto rr = __builtin_amdgcn_permlane32_swap(__float_as_uint(mt), __float_as_uint(mt), false, false);
        mt = fmaxf(fmaxf(__uint_as_float(rr[0]), __uint_as_float(rr[1])), m);
        const float f = __builtin_amdgcn_exp2f(m - mt);
        m = mt;
        u32 pk[8];
#pragma unroll
        for (int i = 0; i < 8; ++i) pk[i] = cvtpk(__builtin_amdgcn_exp2f(sx[2 * i] - m), __builtin_amdgcn_exp2f(sx[2 * i + 1] - m));
#pragma unroll
        for (int i = 0; i < 16; ++i) o[i] *= f;
        o = mfma32(ldsr<bf16x8>(vb), asbf((u32x4){pk[0], pk[1], pk[2], pk[3]}), o);
        o = mfma32(ldsr<bf16x8>(vb + 512), asbf((u32x4){pk[4], pk[5], pk[6], pk[7]}), o);
        vb += isv ? 1024u : 0u;
      }
      MFMA_GAP32(o);
      l = o[8];
    }
    const float rl = __builtin_amdgcn_rcpf(l);
#pragma unroll
    for (int i = 0; i < 4; ++i) opk[i] = canon(cvtpk(o[2 * i] * rl, o[2 * i + 1] * rl));
  }
  st16(wsr, ox_off(), 0, (u32x4){opk[0], opk[1], opk[2], opk[3]});
  STAMP(9);
  bf16x8 sel;
  auto make_sel = [&]() {
    const u32 l = opaque<u32>(lane), e = ((l >> 2) & 3) != (l >> 4) ? 0u : (l & 1) ? 0x3F800000u : 0x00003F80u;
    const u32 d0 = (l & 2) ? 0u : e, d1 = (l & 2) ? e : 0u;
    sel = asbf((u32x4){d0, d1, d0, d1});
  };
  make_sel();
  const u32 gvo = opaque<u32>(w * 4096 + l16) & 0x73F0u;
  auto pw_of = [&](u32 l) { return L_P + ((l >> 3) & 3) * 256 + (w >> 1) * 1024 + (l & 7) * 32 + (l >> 5) * 16 + (((w ^ (l >> 3)) & 1) * 8); };
  const u32 pw = pw_of(lane);
  const u32 pr = L_P + gvo;
  auto g_load = [&](bf16x8 (&f)[4], u32 src) {
#pragma unroll
    for (int t = 0; t < 4; ++t) f[t] = asbf(ld16(wsr, gvo | (t * 1024), src));
  };
  auto g_wait = [&](bf16x8 (&f)[4], u32 src) {
    if (__builtin_expect(unwritten(f), 0)) {
      do {
        if (timed_out(t0)) break;
        regather4(srd, gvo, src, f[0], f[1], f[2], f[3]);
      } while (unwritten(f));
    }
    asm volatile("" : "+v"(f[0]), "+v"(f[1]), "+v"(f[2]), "+v"(f[3]));
  };
  auto row_stats = [&](int half, const bf16x8 (&f)[4]) {
    float s0 = 0.f, s1 = 0.f, q0 = 0.f, q1 = 0.f;
#pragma unroll
    for (int t = 0; t < 4; ++t) {
      const u32x4 d = asu(f[t]);
      s0 = dot2(d[0], 0x3F803F80u, s0); q0 = dot2(d[0], d[0], q0); s1 = dot2(d[1], 0x3F803F80u, s1); q1 = dot2(d[1], d[1], q1);
      s0 = dot2(d[2], 0x3F803F80u, s0); q0 = dot2(d[2], d[2], q0); s1 = dot2(d[3], 0x3F803F80u, s1); q1 = dot2(d[3], d[3], q1);
    }
    const f32x2 sq = {swap32_add(s0 + s1), swap32_add(q0 + q1)};
    if (lane < 32) ldsw<f32x2>(L_STAT + half * STH + lane * STP + w * 8, sq);
  };
  auto partials = [&](int half, f32x16& acc0, f32x16& acc1, u32 pw) {
#pragma unroll
    for (int a = 0; a < 4; ++a) {
      ldsw<u32x2>(pw + half * 32768 + a * 4096, (u32x2){cvtpk(acc0[4 * a], acc0[4 * a + 1]), cvtpk(acc0[4 * a + 2], acc0[4 * a + 3])});
      ldsw<u32x2>(pw + half * 32768 + (4 + a) * 4096, (u32x2){cvtpk(acc1[4 * a], acc1[4 * a + 1]), cvtpk(acc1[4 * a + 2], acc1[4 * a + 3])});
    }
  };
  const u32 stw = opaque<u32>(L_STAT + r32 * STP + w * 8);
  auto slice_gemm = [&](int half, const bf16x8 (&a)[8], const bf16x8 (&f)[4], bool stats, auto&& mid) {
    f32x16 acc0 = {}, acc1 = {};
    auto stat_block = [&]() {
      float s0 = 0.f, s1 = 0.f, q0 = 0.f, q1 = 0.f;
#pragma unroll
      for (int t = 0; t < 4; ++t) {
        const u32x4 d = asu(f[t]);
        s0 = dot2(d[0], 0x3F803F80u, s0); q0 = dot2(d[0], d[0], q0); s1 = dot2(d[1], 0x3F803F80u, s1); q1 = dot2(d[1], d[1], q1);
        s0 = dot2(d[2], 0x3F803F80u, s0); q0 = dot2(d[2], d[2], q0); s1 = dot2(d[3], 0x3F803F80u, s1); q1 = dot2(d[3], d[3], q1);
      }
      ldsw<f32x2>(stw + half * STH, (f32x2){swap32_add(s0 + s1), swap32_add(q0 + q1)});
      __builtin_amdgcn_sched_barrier(0);
    };
    if (stats && STAT_POS == 0) stat_block();
#pragma unroll
    for (int t = 0; t < 4; ++t) {
      acc0 = mfma32(a[t], f[t], acc0);
      acc1 = mfma32(a[4 + t], f[t], acc1);
    }
    __builtin_amdgcn_sched_barrier(0);
    if (stats && STAT_POS == 1) stat_block();
    mid();
    __builtin_amdgcn_sched_barrier(0);
    partials(half, acc0, acc1, pw);
  };
  auto reduce_at = [&](int half, u32 pr_) -> f32x4 {
    f32x4 acc = {0, 0, 0, 0};
#pragma unroll
    for (int q = 0; q < 4; ++q) acc = mfma16(sel, ldsr<bf16x8>(pr_ + half * 32768 + q * 1024), acc);
    return acc;
  };
  auto reduce = [&](int half) { return reduce_at(half, pr); };
  auto src_of = [&](u32 base, int half) { return base + blk_of(half) * 32768; };
  const u32 hvo = opaque<u32>(PUT16 && (lane & 1) ? OOB : (u32)lane * 8);
  auto put_at = [&](u32 vo, u32 so, u32x2 v) {
    if (PUT16) {
      const u32 n0 = __builtin_amdgcn_mov_dpp(v[0], 0xB1, 0xF, 0xF, true), n1 = __builtin_amdgcn_mov_dpp(v[1], 0xB1, 0xF, 0xF, true);
      st16(wsr, vo, so, (u32x4){v[0], v[1], n0, n1});
    } else st8(wsr, vo, so, v);
  };
  auto pack4 = [&](f32x4 v) { return (u32x2){canon(cvtpk(v[0], v[1])), canon(cvtpk(v[2], v[3]))}; };
  const u32 a3off = w < 7 ? ((7 * j + w) * 32) * 1024 + l16 : OOB;
  auto a3_load = [&](int t0_, int t1_) {
#pragma unroll
    for (int t = 0; t < 32; ++t) if (t >= t0_ && t < t1_) a3[t] = asbf(ld16(wsr, a3off, WS_W3 + t * 1024));
  };

  auto a3_slot = [&](int k) {
    constexpr int n = 32 - A3_N0;
    a3_load(A3_N0 + k * n / 6, A3_N0 + (k + 1) * n / 6);
  };
  f32x4 cs3p = {0.f, 0.f, 0.f, 0.f};
  auto cs3_acc = [&](int t0_, int t1_) {
#pragma unroll
    for (int t = 0; t < 32; ++t) if (t >= t0_ && t < t1_) cs3p = mfma16(sel, a3[t], cs3p);
  };
  f32x4 hres[2], xp[2], cs1;
  bf16x8 fA[4], fB[4];
  const u32 str = opaque<u32>(L_STAT + (lane >> 1) * STP + (lane & 1) * 32);
  auto back = [&](int half, bool stats, auto&& epi, auto&& hook) {
    bf16x8 p[4];
    f32x4 p0 = {0, 0, 0, 0}, p1 = {0, 0, 0, 0};
#pragma unroll
    for (int q = 0; q < 4; ++q) p[q] = ldsr<bf16x8>(pr + half * 32768 + q * 1024);
    if (stats) { p0 = ldsr<f32x4>(str + half * STH); p1 = ldsr<f32x4>(str + half * STH + 16); }
    hook();
    __builtin_amdgcn_sched_barrier(0);
    f32x4 acc = {0, 0, 0, 0};
#pragma unroll
    for (int q = 0; q < 4; ++q) acc = mfma16(sel, p[q], acc);
    RowStat st = {0.f, 0.f, 0.f};
    if (stats) { const f32x4 p = p0 + p1; st = finish_stat(xor1_add(p[0] + p[2]), xor1_add(p[1] + p[3])); }
    return epi(half, acc, st);
  };
  auto phase = [&](const bf16x8 (&a)[8], u32 src, bool stats, int gb, auto&& first, auto&& epi, auto&& put, auto&& pre, auto&& slotA, auto&& slotB, int sbase) {
    PSTAMP(sbase, 0);
    g_wait(fA, src_of(src, 0));
    STAMP(sbase);
    PSTAMP(sbase, 1);
    slice_gemm(0, a, fA, stats, [&]() {
      if (gb == 0) g_load(fB, src_of(src, 1));
      if (SLOT_POS == 0) slotA();
      first();
    });
    PSTAMP(sbase, 4);
    BAR();
    PSTAMP(sbase, 5);
    const u32x2 vA = back(0, stats, epi, [&]() { if (gb == 1) g_load(fB, src_of(src, 1)); });
    PSTAMP(sbase, 6);
    if (gb == 2) g_load(fB, src_of(src, 1));
    __builtin_amdgcn_sched_barrier(0);
    put(0, vA);
    if (SLOT_POS == 1) slotA();
    __builtin_amdgcn_sched_barrier(0);
    PSTAMP(sbase, 7);
    g_wait(fB, src_of(src, 1));
    STAMP(sbase + 1);
    PSTAMP(sbase, 8);
    slice_gemm(1, a, fB, stats, [&]() { if (SLOT_POS == 0) slotB(); });
    PSTAMP(sbase, 11);
    BAR();
    PSTAMP(sbase, 12);
    const u32x2 vB = back(1, stats, epi, [&]() {
      pre();
    });
    PSTAMP(sbase, 13);
    PSTAMP(sbase, 14);
    __builtin_amdgcn_sched_barrier(0);
    put(1, vB);
    if (SLOT_POS == 1) slotB();
    __builtin_amdgcn_sched_barrier(0);
    PSTAMP(sbase, 15);
  };
  auto prio_late = [&](int p) {
#define PRIO_ASM(P) asm volatile("s_cmp_lt_u32 %0, 4\n\ts_cbranch_scc1 .Lprio%=\n\ts_setprio " #P "\n.Lprio%=:" :: "s"(w) : "scc")
    if (p == 0) PRIO_ASM(0); else if (p == 1) PRIO_ASM(1); else PRIO_ASM(2);
#undef PRIO_ASM
  };
  const u32 a4off = (2 * j * 112 + 14 * w) * 1024 + l16;
  {
    float c[2] = {0.f, 0.f};
#pragma unroll
    for (int t = 0; t < 8; ++t) {
      const u32x4 d = asu(a_1[t]);
#pragma unroll
      for (int e = 0; e < 4; ++e) c[t >> 2] = dot2(d[e], 0x3F803F80u, c[t >> 2]);
    }
    const float c0 = swap32_add(c[0]), c1 = swap32_add(c[1]);
    ldsw<float>(L_CS1P + w * 256 + lane * 4, lane < 32 ? c0 : c1);
  }
  cs3_acc(0, A3_N0);
  __builtin_amdgcn_sched_barrier(0);
  if (O_SLEEP) __builtin_amdgcn_s_sleep(O_SLEEP);
  g_load(fA, src_of(WS_OX, 0));
  g_load(fB, src_of(WS_OX, 1));
  __builtin_amdgcn_sched_barrier(0);
  phase(a_o, WS_OX, false, -1,
        [&]() { BAR(); },
        [&](int half, f32x4 acc, RowStat) {
          if (half == 1) {
            cs1 = (f32x4){0.f, 0.f, 0.f, 0.f};
#pragma unroll
            for (int v = 0; v < 8; ++v) cs1 += ldsr<f32x4>(L_CS1P + v * 256 + w * 32 + (lane & 1) * 16);
            cs1 = opaque(cs1);
          }
          hres[half] = acc + (f32x4){bflo(xres[half][0]), bfhi(xres[half][0]), bflo(xres[half][1]), bfhi(xres[half][1])};
          return pack4(hres[half]);
        },
        [&](int half, u32x2 v) { put_at(hvo, hx_so(WS_HX, half), v); },
        [&]() { g_load(fA, src_of(WS_HX, 0)); },
        [&]() {
          if (W4_WARM == 2) {
#pragma unroll
            for (int i = 0; i < 7; ++i) dma1(ws + WS_W4 + i * 1024, (((u32)(b >> 6) & 3u) * 8 + w) * 7168 + j * 229376 + l16, lb + L_DUM1);
          }
          a3_slot(0);
        },
        [&]() { a3_slot(1); }, 10);
  cs3_acc(A3_N0, A3_N0 + (32 - A3_N0) / 6);
  __builtin_amdgcn_sched_barrier(0);
  STAMP(12);
  const u32 tso[2] = {opaque<u32>(ts_vo(0)), opaque<u32>(ts_vo(1))};
  phase(a_1, WS_HX, true, GB_POS, [&]() { },
        [&](int half, f32x4 acc, RowStat st) {
          const float r2 = __builtin_amdgcn_rsqf(st.var * st.rstd * st.rstd + LN_EPS), rr = st.rstd * r2;
          const f32x4 pre = (acc - st.mu * cs1) * rr;
          const f32x2 g0 = gelu2((f32x2){pre[0], pre[1]}), g1 = gelu2((f32x2){pre[2], pre[3]});
          xp[half] = (hres[half] - st.mu) * st.rstd;
          return (u32x2){canon(cvtpk(g0[0], g0[1])), canon(cvtpk(g1[0], g1[1]))};
        },
        [&](int half, u32x2 v) { put_at(tso[half], hx_so(WS_TS, half) - 32768, v); },
        [&]() { g_load(fA, src_of(WS_TS, 0)); },
        [&]() { a3_slot(2); },
        [&]() { a3_slot(3); dma4_sc1(ws + WS_W4 + 10 * 1024, a4off, lb + L_A4 + w * 8192); }, 13);
  cs3_acc(A3_N0 + (32 - A3_N0) / 6, A3_N0 + 3 * (32 - A3_N0) / 6);
  __builtin_amdgcn_sched_barrier(0);
  STAMP(15);
  phase(a_2, WS_TS, false, GB_POS, [&]() { },
        [&](int half, f32x4 acc, RowStat) {
          const f32x4 x1 = xp[half] + acc;
          ldsw<f32x4>(L_RES + half * 8192 + w * 1024 + l16, hres[half] + x1);
          return pack4(x1);
        },
        [&](int half, u32x2 v) { put_at(hvo, hx_so(WS_X1, half), v); },
        [&]() { dma4_sc1(ws + src_of(WS_X1, 0), gvo, lb + L_P + w * 4096); },
        [&]() { a3_slot(4); dma4_sc1(ws + WS_W4 + 114688 + 10 * 1024, a4off, lb + L_A4 + w * 8192 + 4096); },
        [&]() { a3_slot(5); }, 16);
  cs3_acc(A3_N0 + 3 * (32 - A3_N0) / 6, 32);
  {
    const float c = swap32_add((cs3p[0] + cs3p[1]) + (cs3p[2] + cs3p[3]));
    ldsw<float>(L_CS + w * 128 + r32 * 4, c);
  }
  __builtin_amdgcn_sched_barrier(0);
  STAMP(18);
  bf16x8 a4[10][2];
  auto a4_load = [&](int n) { a4[n >> 1][n & 1] = asbf(ld16(wsr, a4off, WS_W4 + (n & 1) * 114688 + (n >> 1) * 1024)); };
  constexpr int ord[14] = {0, 2, 4, 6, 1, 3, 5, 7, 8, 10, 12, 9, 11, 13};
  u32x4 uf[28];
  auto u_load1 = [&](int k) { uf[k] = ld16(wsr, l16, WS_UX + (blk_of(k / 14) * 112 + w * 14 + ord[k % 14]) * 1024); };
  {
    auto x1_finish = [&](int half) {
#pragma unroll
      for (int t = 0; t < 4; ++t) fA[t] = ldsr<bf16x8>(pr + half * 32768 + t * 1024);
      if (__builtin_expect(unwritten(fA), 0)) {
        do {
          if (timed_out(t0)) break;
          __builtin_amdgcn_s_sleep(1);
          dma4_sc1(ws + src_of(WS_X1, half), gvo, lb + L_P + half * 32768 + w * 4096);
          WAITV(0);
#pragma unroll
          for (int t = 0; t < 4; ++t) fA[t] = ldsr<bf16x8>(pr + half * 32768 + t * 1024);
        } while (unwritten(fA));
      }
      row_stats(half, fA);
    };
    struct UOut { float nmu, k1, k2, ir; f32x4 cs[2]; f32x4 p[4]; u32 up[8]; u32 cb, vo; };
    auto u_stat0 = [&](UOut& u, int half) {
      const u32 l = opaque<u32>(lane), sr = opaque<u32>(L_STAT + half * STH + (l & 31) * STP);
#pragma unroll
      for (int i = 0; i < 4; ++i) u.p[i] = ldsr<f32x4>(sr + 16 * i);
    };
    auto u_stat1 = [&](UOut& u) {
      const f32x4 p = (u.p[0] + u.p[1]) + (u.p[2] + u.p[3]);
      const RowStat st = finish_stat(p[0] + p[2], p[1] + p[3]);
      const float c1 = -2.0f * 0.7978845608028654f * 1.4426950408889634f, c2 = c1 * 0.044715f;
      u.nmu = opaque(-st.mu); u.k1 = opaque(c1 * st.rstd); u.k2 = opaque(c2 * st.rstd * st.rstd * st.rstd); u.ir = opaque(__builtin_amdgcn_sqrtf(st.var + LN_EPS));
    };
    auto u_cs = [&](UOut& u, int a) {
      if (a == 0) {
        const u32 l = opaque<u32>(lane);
        u.cb = opaque<u32>(L_CS + w * 128 + (l >> 5) * 16);
        u.vo = opaque<u32>(w < 7 ? l * 16 : OOB);
      }
      u.cs[a & 1] = ldsr<f32x4>(u.cb + a * 32);
    };
    auto u_store = [&](UOut& u, int half, int p) {
      if (p == 3 || p == 7) {
        const u32 so = WS_UX + (blk_of(half) * 112 + j * 14 + 2 * w) * 1024;
        if (p == 3) st16(wsr, u.vo, so, (u32x4){u.up[0], u.up[1], u.up[2], u.up[3]});
        else st16(wsr, u.vo, so + 1024, (u32x4){u.up[4], u.up[5], u.up[6], u.up[7]});
      }
    };
    auto u_quad = [&](UOut& u, int half, const f32x16& acc, int p) {
      float t[4], e[4];
#pragma unroll
      for (int i = 0; i < 4; ++i) { const int v = 2 * p + i; t[i] = opaque(__builtin_fmaf(u.nmu, u.cs[(v >> 2) & 1][v & 3], acc[v])); }
#pragma unroll
      for (int i = 0; i < 4; ++i) e[i] = __builtin_amdgcn_exp2f(t[i] * __builtin_fmaf(t[i] * t[i], u.k2, u.k1));
#pragma unroll
      for (int i = 0; i < 4; ++i) t[i] = opaque(t[i] * __builtin_amdgcn_rcpf(__builtin_fmaf(e[i], u.ir, u.ir)));
      u.up[p] = canon(cvtpk(t[0], t[1])); u.up[p + 1] = canon(cvtpk(t[2], t[3]));
      u_store(u, half, p + 1);
    };
    auto u_pair = [&](UOut& u, int half, const f32x16& acc, int p) {
      const int a = p >> 1, e = 2 * (p & 1);
      const f32x2 t = (f32x2){u.cs[a & 1][e], u.cs[a & 1][e + 1]} * u.nmu + (f32x2){acc[2 * p], acc[2 * p + 1]};
      const f32x2 ar = t * ((t * t) * u.k2 + u.k1);
      const f32x2 d = (f32x2){__builtin_amdgcn_exp2f(ar[0]), __builtin_amdgcn_exp2f(ar[1])} * u.ir + u.ir;
      const f32x2 g = t * (f32x2){__builtin_amdgcn_rcpf(d[0]), __builtin_amdgcn_rcpf(d[1])};
      u.up[p] = canon(cvtpk(g[0], g[1]));
    };
    WAITV(1);
    x1_finish(0);
    BAR();
    STAMP(19);
    dma4_sc1(ws + src_of(WS_X1, 1), gvo, lb + L_P + 32768 + w * 4096);
    const u32 xb = L_P + (w < 7 ? l16 : 0u);
    f32x16 accA = {}, accB = {};
    bf16x8 bq[4];
    UOut uA, uB;
    if (M_PRIO) __builtin_amdgcn_s_setprio(1);
    {
#pragma unroll
      for (int t = 0; t < 4; ++t) bq[t] = ldsr<bf16x8>(xb + t * 1024);
#pragma unroll
      for (int t = 0; t < 32; ++t) {
        accA = mfma32(a3[t], bq[t & 3], accA);
        if (t + 4 < 32) bq[t & 3] = ldsr<bf16x8>(xb + (t + 4) * 1024);
        if (t == 0) u_stat0(uA, 0);
        if (t == 3) u_stat1(uA);
        if (t == 29) u_cs(uA, 0);
        if (t >= 4 && (t & 3) == 0 && t / 4 - 1 < A4_N0) a4_load(t / 4 - 1);
        __builtin_amdgcn_sched_barrier(0);
      }
    }
    if (M_PRIO) __builtin_amdgcn_s_setprio(0);
    STAMP(20);
    auto gelu_half = [&](UOut& u, int half, const f32x16& acc) {
#pragma unroll
      for (int p = 0; p < 8; p += 2) {
        if (p + 2 < 8) u_cs(u, (p + 2) >> 1);
        constexpr int nr = (8 - UE_P) / 2;
        if (half == 1) {
#pragma unroll
          for (int n = 20 - A4_LATE + p / 2 * A4_LATE / 4; n < 20 - A4_LATE + (p / 2 + 1) * A4_LATE / 4; ++n) a4_load(n);
        }
        if (half == 1 && p >= UE_P) {
#pragma unroll
          for (int i = (p - UE_P) / 2 * U_INIT / (nr ? nr : 1); i < ((p - UE_P) / 2 + 1) * U_INIT / (nr ? nr : 1); ++i) u_load1(i);
        }
        if (GB_PK) { u_pair(u, half, acc, p); u_pair(u, half, acc, p + 1); u_store(u, half, p + 1); }
        else u_quad(u, half, acc, p);
        __builtin_amdgcn_sched_barrier(0);
      }
      if (half == 1 && UE_P >= 8) {
#pragma unroll
        for (int i = 0; i < U_INIT; ++i) u_load1(i);
      }
    };
    if (PQ == 0 || w < 4) gelu_half(uA, 0, accA);
    asm volatile("s_waitcnt vmcnt(%0)" :: "n"(A4_N0 + (PQ ? 0 : 2)) : "memory");
    x1_finish(1);
    BAR();
    STAMP(21);
    if (!M_PRIO) prio_late(Q_PRIO);
    if (PQ && w >= 4) gelu_half(uA, 0, accA);
    if (M_PRIO) __builtin_amdgcn_s_setprio(1);
    {
#pragma unroll
      for (int t = 0; t < 4; ++t) bq[t] = ldsr<bf16x8>(xb + 32768 + t * 1024);
#pragma unroll
      for (int t = 0; t < 32; ++t) {
        if (W3B_TOG && (t % (W3B_TOG ? W3B_TOG : 1)) == 0) prio_late(((t / (W3B_TOG ? W3B_TOG : 1)) & 1) ? 0 : 1);
        accB = mfma32(a3[t], bq[t & 3], accB);
        if (t + 4 < 32) bq[t & 3] = ldsr<bf16x8>(xb + 32768 + (t + 4) * 1024);
        if (t == 0) u_stat0(uB, 1);
        if (t == 3) u_stat1(uB);
        if (t == 29) u_cs(uB, 0);
        if (A4_SPREAD == 0) { const int n = A4_N0 + t - 1; if (t >= 1 && n < 20 - A4_LATE) a4_load(n); }
        else if (A4_SPREAD == 1) { const int n = A4_N0 + (t - 1) / 2; if ((t & 1) && n < 20 - A4_LATE) a4_load(n); }
        else { const int n = A4_N0 + (t - 1) / 4; if ((t & 3) == 1 && n < 20 - A4_LATE) a4_load(n); }
        __builtin_amdgcn_sched_barrier(0);
      }
    }
    if (M_PRIO) __builtin_amdgcn_s_setprio(0); else prio_late(GB_PRIO);
    if (!MID_BAR && lane == 0) asm volatile("ds_add_u32 %0, %1" :: "v"(lb + L_CNT + 8), "v"(1u) : "memory");
    STAMP(22);
    gelu_half(uB, 1, accB);
    STAMP(23);
  }
  {
    if (M_PRIO) { __builtin_amdgcn_s_setprio(1); if (W4_PRIO) prio_late(2); } else prio_late(W4_PRIO);
#pragma unroll
    for (int half = 0; half < 2; ++half) {
      const u32 sa = L_A4 + w * 8192 + opaque<u32>(l16);
      f32x16 acc0 = {}, acc1 = {};
      bf16x8 sq[4][2];
      auto step = [&](int s_, u32x4 u) {
        if (s_ + 2 < 14 && ord[s_ + 2] >= 10) { const int k = ord[s_ + 2] - 10; sq[k][0] = ldsr<bf16x8>(sa + k * 1024); sq[k][1] = ldsr<bf16x8>(sa + 4096 + k * 1024); }
        const int t = ord[s_];
        acc0 = mfma32(t < 10 ? a4[t < 10 ? t : 0][0] : sq[t < 10 ? 0 : t - 10][0], asbf(u), acc0);
        acc1 = mfma32(t < 10 ? a4[t < 10 ? t : 0][1] : sq[t < 10 ? 0 : t - 10][1], asbf(u), acc1);
      };
      u32 m = 0;
#pragma unroll
      for (int s_ = 0; s_ < 14; ++s_) {
        const int k = 14 * half + s_;
        step(s_, uf[k]);
        m = opaque(umax(umax(umax(m, uf[k][0]), uf[k][1]), umax(uf[k][2], uf[k][3])));
        if (k + U_WIN < 28 && k + U_WIN >= U_INIT) u_load1(k + U_WIN);
        __builtin_amdgcn_sched_barrier(0);
      }
      if (__builtin_expect(__any(m == SENT), 0)) {
        u32x4 ug[14];
        bool bad;
        do {
          __builtin_amdgcn_s_sleep(1);
          asm volatile("s_nop 7" ::: "memory");
          const u32 so = WS_UX + (blk_of(half) * 112 + w * 14) * 1024, l = opaque<u32>(l16);
#pragma unroll
          for (int i = 0; i < 14; ++i) regather1(srd, l, so + ord[i] * 1024, ug[i]);
          asm volatile("s_waitcnt vmcnt(0)" : "+v"(ug[0]), "+v"(ug[1]), "+v"(ug[2]), "+v"(ug[3]), "+v"(ug[4]), "+v"(ug[5]), "+v"(ug[6]), "+v"(ug[7]), "+v"(ug[8]), "+v"(ug[9]),
                       "+v"(ug[10]), "+v"(ug[11]), "+v"(ug[12]), "+v"(ug[13]) :: "memory");
          u32 mm = 0;
#pragma unroll
          for (int i = 0; i < 14; ++i) mm = umax(mm, max4(ug[i]));
          bad = __any(mm == SENT);
        } while (bad && !timed_out(t0));
        acc0 = (f32x16){}; acc1 = (f32x16){};
#pragma unroll
        for (int s_ = 0; s_ < 14; ++s_) step(s_, ug[s_]);
      }
      STAMP(24 + half);
      if (!MID_BAR && half == 1) {
        while (__builtin_amdgcn_readfirstlane(*(volatile LAS u32*)((LAS char*)smem + L_CNT + 8)) != 8) {
          if (timed_out(t0)) break;
          __builtin_amdgcn_s_sleep(1);
        }
        FENCE();
      }
      partials(half, acc0, acc1, pw_of(opaque<u32>(lane)));
      if (MID_BAR ? half == 0 || !OUT_EARLY : half == 1) make_sel();
      if (half == 1) STAMP(26);
      if (half == 1 || (MID_BAR && OUT_EARLY)) BAR();
      auto out_half = [&](int hf) {
        const u32 l = opaque<u32>(lane);
        const f32x4 v = reduce_at(hf, L_P + w * 4096 + l * 16) + ldsr<f32x4>(L_RES + hf * 8192 + w * 1024 + l * 16);
        *(GAS u32x2*)((char*)out + (u32)((blk_of(hf) * 32 + (l >> 1)) * 1024 + (64 * j + 8 * w + 4 * (l & 1)) * 2)) = (u32x2){cvtpk(v[0], v[1]), cvtpk(v[2], v[3])};
      };
      if (MID_BAR && OUT_EARLY) out_half(half);
      else if (half == 1) { out_half(0); out_half(1); }
      else if (MID_BAR) BAR();
      FENCE();
    }
  }
  STAMP(27);
}

extern "C" void kernel_launch(void* const* d_in, const int* in_sizes, int n_in, void* d_out, int out_size, void* d_ws, size_t ws_size, hipStream_t stream) {
  (void)in_sizes; (void)n_in; (void)out_size; (void)ws_size;
  fused_block_kernel<<<256, 512, 0, stream>>>((const u16*)d_in[0], (const u16*)d_in[1], (const u16*)d_in[2], (const u16*)d_in[3], (const u16*)d_in[4],
                                              (const u16*)d_in[6], (const u16*)d_in[7], (const u16*)d_in[8], (const u16*)d_in[9], (u16*)d_out, (char*)d_ws);
}
```

```cpp
#include <hip/hip_runtime.h>
#include <stdint.h>
#include <type_traits>

typedef unsigned short u16;
typedef unsigned int u32;
typedef unsigned long long u64;
typedef short bf16x8 __attribute__((ext_vector_type(8)));
typedef short s16x4 __attribute__((ext_vector_type(4)));
typedef float f32x2 __attribute__((ext_vector_type(2)));
typedef float f32x4 __attribute__((ext_vector_type(4)));
typedef float f32x16 __attribute__((ext_vector_type(16)));
typedef u32 u32x2 __attribute__((ext_vector_type(2)));
typedef u32 u32x4 __attribute__((ext_vector_type(4)));
typedef __bf16 bf16x2_t __attribute__((ext_vector_type(2)));
#define LAS __attribute__((address_space(3)))
#define GAS __attribute__((address_space(1)))
#define DI __device__ __forceinline__

constexpr u32 WS_EPOCH = 0x0;
constexpr u32 WS_FKV = 0x1000;
constexpr u32 WS_FW = 0x5000;
constexpr u32 WS_FM = 0x9000;
constexpr u32 WS_K = 0x10000;
constexpr u32 WS_V = 0x20000;
constexpr u32 WS_DBG = 0x1E00000;
constexpr u32 WS_WO = 0x100000, WS_W1 = 0x180000, WS_W2 = 0x200000, WS_W3 = 0x280000, WS_W4 = 0x440000;
constexpr u32 WS_OX = 0x600000, WS_HX = 0x800000, WS_TS = 0xA00000, WS_X1 = 0xC00000, WS_UX = 0xE00000;
constexpr u32 WS_STAMP = 0x2000000;
constexpr u32 WS_END = 0x2100000;
constexpr u32 OOB = 0x80000000u;

constexpr u32 XP = 1040;
constexpr u32 L_XIMG = 0;
constexpr u32 L_WQ = 66560;
constexpr u32 L_WKV = 132096;
constexpr u32 S_PM = 148480, S_PC = 156672, S_PS = 157184, S_PQ = 157696, S_FIN = 158208, S_QS = 158336, S_QC = 159360;
constexpr u32 L_DUM1 = 160384;
constexpr u32 L_CNT = 161408;
constexpr u32 L_K = 0, L_V = 65536, L_ONES = 151552;
constexpr u32 L_STG = 131072;
constexpr u32 L_CS1P = 155904;
constexpr u32 L_P = 0;
constexpr u32 L_STAT = 65536;
constexpr u32 STP = 80, STH = 2560;
constexpr u32 L_CS = 71680;
constexpr u32 L_RES = 73728;
constexpr u32 L_A4 = 90112;
constexpr u32 L_STAMP = 161792;
constexpr u32 LDS_BYTES = 163840;

__shared__ __attribute__((aligned(16))) char smem[LDS_BYTES];

constexpr u32 SENT = 0xFFFFFFFFu;
constexpr float LN_EPS = 1e-5f;
constexpr float QSCALE = 0.25f * 1.4426950408889634f;
#define CODE_KB 45

DI f32x16 mfma32(bf16x8 a, bf16x8 b, f32x16 c) { return __builtin_amdgcn_mfma_f32_32x32x16_bf16(a, b, c, 0, 0, 0); }
DI f32x4 mfma16(bf16x8 a, bf16x8 b, f32x4 c) { return __builtin_amdgcn_mfma_f32_16x16x32_bf16(a, b, c, 0, 0, 0); }
DI u32 cvtpk(float lo, float hi) { f32x2 v = {lo, hi}; bf16x2_t b = __builtin_convertvector(v, bf16x2_t); return __builtin_bit_cast(u32, b); }
DI u32 canon(u32 d) { return d < 0xFFFFFFFEu ? d : 0xFFFFFFFEu; }
DI float bflo(u32 d) { return __uint_as_float(d << 16); }
DI float bfhi(u32 d) { return __uint_as_float(d & 0xFFFF0000u); }
DI float dot2(u32 a, u32 b, float c) { return __builtin_amdgcn_fdot2_f32_bf16(__builtin_bit_cast(bf16x2_t, a), __builtin_bit_cast(bf16x2_t, b), c, false); }
DI float gelu(float x) {
  const float c1 = -2.0f * 0.7978845608028654f * 1.4426950408889634f, c2 = c1 * 0.044715f;
  const float e = __builtin_amdgcn_exp2f(x * (c1 + c2 * x * x));
  return x * __builtin_amdgcn_rcpf(1.0f + e);
}
DI f32x2 gelu2(f32x2 x) {
  const float c1 = -2.0f * 0.7978845608028654f * 1.4426950408889634f, c2 = c1 * 0.044715f;
  const f32x2 a = x * ((x * x) * c2 + c1);
  const f32x2 d = (f32x2){__builtin_amdgcn_exp2f(a[0]), __builtin_amdgcn_exp2f(a[1])} + 1.0f;
  return x * (f32x2){__builtin_amdgcn_rcpf(d[0]), __builtin_amdgcn_rcpf(d[1])};
}
DI float swap32_add(float v) {
  const auto r = __builtin_amdgcn_permlane32_swap(__float_as_uint(v), __float_as_uint(v), false, false);
  return __uint_as_float(r[0]) + __uint_as_float(r[1]);
}
DI float xor1_add(float v) { return v + __int_as_float(__builtin_amdgcn_mov_dpp(__float_as_int(v), 0xB1, 0xF, 0xF, true)); }

#define BAR() asm volatile("s_waitcnt lgkmcnt(0)\n\ts_barrier" ::: "memory")
#define WAITV(n) asm volatile("s_waitcnt vmcnt(" #n ")" ::: "memory")
#define FENCE() asm volatile("" ::: "memory")
template <class T> DI T opaque(T v) { asm volatile("" : "+v"(v)); return v; }

template <class T> DI T ldsr(u32 off) { return *(const LAS T*)((LAS char*)smem + off); }
template <class T> DI void ldsw(u32 off, T v) { *(LAS T*)((LAS char*)smem + off) = v; }
DI s16x4 ldstr(u32 off) {
  typedef short v4i16_t __attribute__((ext_vector_type(4)));
  return __builtin_bit_cast(s16x4, __builtin_amdgcn_ds_read_tr16_b64_v4i16((LAS v4i16_t*)((LAS char*)smem + off)));
}
DI bf16x8 cat(s16x4 a, s16x4 b) { return __builtin_shufflevector(a, b, 0, 1, 2, 3, 4, 5, 6, 7); }
DI u32 lds_base() { return (u32)(uintptr_t)(LAS char*)smem; }

#define DMA_HEAD "s_mov_b32 %[k], m0\n\ts_mov_b32 m0, %[lds]\n\ts_nop 0\n\t"
#define DMA_TAIL "s_mov_b32 m0, %[k]"
DI void dma1(const void* sbase, u32 voff, u32 ldsdst) {
  u32 keep;
  asm volatile(DMA_HEAD "global_load_lds_dwordx4 %[v], %[s]\n\t" DMA_TAIL : [k] "=&s"(keep) : [v] "v"(voff), [s] "s"(sbase), [lds] "s"(ldsdst) : "memory");
}
DI void dma2(const void* sbase, u32 voff, u32 ldsdst) {
  u32 keep;
  asm volatile(DMA_HEAD "global_load_lds_dwordx4 %[v], %[s]\n\tglobal_load_lds_dwordx4 %[v], %[s] offset:1024\n\t" DMA_TAIL
               : [k] "=&s"(keep) : [v] "v"(voff), [s] "s"(sbase), [lds] "s"(ldsdst) : "memory");
}
DI void dma4_rows(const void* sbase, u32 v0, u32 v1, u32 v2, u32 v3, u32 ldsdst) {
  u32 keep;
  asm volatile(DMA_HEAD "global_load_lds_dwordx4 %[a], %[s]\n\tglobal_load_lds_dwordx4 %[b], %[s] offset:1040\n\t"
               "global_load_lds_dwordx4 %[c], %[s] offset:2080\n\tglobal_load_lds_dwordx4 %[d], %[s] offset:3120\n\t" DMA_TAIL
               : [k] "=&s"(keep) : [a] "v"(v0), [b] "v"(v1), [c] "v"(v2), [d] "v"(v3), [s] "s"(sbase), [lds] "s"(ldsdst) : "memory");
}
DI void dma4_sc1(const void* sbase, u32 voff, u32 ldsdst) {
  u32 keep;
  asm volatile(DMA_HEAD "global_load_lds_dwordx4 %[v], %[s] sc1\n\tglobal_load_lds_dwordx4 %[v], %[s] offset:1024 sc1\n\t"
               "global_load_lds_dwordx4 %[v], %[s] offset:2048 sc1\n\tglobal_load_lds_dwordx4 %[v], %[s] offset:3072 sc1\n\t" DMA_TAIL
               : [k] "=&s"(keep) : [v] "v"(voff), [s] "s"(sbase), [lds] "s"(ldsdst) : "memory");
}
DI void dma_pair_late(int w, const void* sbase, u32 voff, u32 ldsdst) {
  u32 keep;
  asm volatile("s_cmp_lt_u32 %[w], 4\n\ts_cbranch_scc1 .Lkv%=\n\t" DMA_HEAD "global_load_lds_dwordx4 %[a], %[s] sc1\n\ts_add_u32 m0, m0, 0x10000\n\ts_nop 0\n\t"
               "global_load_lds_dwordx4 %[b], %[s] sc1\n\t" DMA_TAIL "\n.Lkv%=:"
               : [k] "=&s"(keep) : [w] "s"(w), [a] "v"(voff), [b] "v"(voff + 0x10000u), [s] "s"(sbase), [lds] "s"(ldsdst) : "memory", "scc");
}
template <int N> DI void count_late(int w, u32 addr) {
  u64 keep;
  asm volatile("s_cmp_lt_u32 %[w], 4\n\ts_cbranch_scc1 .Lcl%=\n\ts_waitcnt vmcnt(%[n])\n\ts_mov_b64 %[k], exec\n\ts_mov_b64 exec, 1\n\tds_add_u32 %[a], %[o]\n\ts_mov_b64 exec, %[k]\n.Lcl%=:"
               : [k] "=&s"(keep) : [w] "s"(w), [n] "n"(N), [a] "v"(addr), [o] "v"(1u) : "memory", "scc");
}
DI void st16_early(int w, u32x4 srd, u32 voff, u32 soff, u32x4 v) {
  asm volatile("s_cmp_ge_u32 %[w], 4\n\ts_cbranch_scc1 .Lse%=\n\tbuffer_store_dwordx4 %[d], %[v], %[r], %[s] offen sc1\n\ts_nop 1\n.Lse%=:"
               :: [w] "s"(w), [d] "v"(v), [v] "v"(voff), [r] "s"(srd), [s] "s"(soff) : "memory", "scc");
}
#define DMA_STEP(SC) "global_load_lds_dwordx4 %[v], %[s]" SC "\n\ts_add_u32 m0, m0, %[ls]\n\tv_add_u32 %[v], %[gs], %[v]\n\t"
template <int N, bool SC1, u32 GS>
DI void dma_strided(const void* sbase, u32 voff, u32 ldsdst, u32 lstep) {
  u32 keep;
  static_assert(N == 2 || N == 3 || N == 4 || N == 8, "");
#define DMA_RUN(BODY) asm volatile(DMA_HEAD BODY DMA_TAIL : [k] "=&s"(keep), [v] "+v"(voff) : [s] "s"(sbase), [lds] "s"(ldsdst), [ls] "s"(lstep), [gs] "n"(GS) : "memory", "scc")
  if constexpr (SC1) {
    if constexpr (N == 2) DMA_RUN(DMA_STEP(" sc1") DMA_STEP(" sc1"));
    if constexpr (N == 3) DMA_RUN(DMA_STEP(" sc1") DMA_STEP(" sc1") DMA_STEP(" sc1"));
    if constexpr (N == 4) DMA_RUN(DMA_STEP(" sc1") DMA_STEP(" sc1") DMA_STEP(" sc1") DMA_STEP(" sc1"));
  } else {
    if constexpr (N == 8) DMA_RUN(DMA_STEP("") DMA_STEP("") DMA_STEP("") DMA_STEP("") DMA_STEP("") DMA_STEP("") DMA_STEP("") DMA_STEP(""));
  }
#undef DMA_RUN
}

typedef __amdgpu_buffer_rsrc_t rsrc_t;
DI rsrc_t mkrsrc(const void* p, u32 bytes) { return __builtin_amdgcn_make_buffer_rsrc((void*)p, 0, bytes, 0x00020000); }
DI u32x4 ld16(rsrc_t r, u32 voff, u32 soff) { return __builtin_amdgcn_raw_buffer_load_b128(r, voff, soff, 16); }
DI void st16(rsrc_t r, u32 voff, u32 soff, u32x4 v) {
  __builtin_amdgcn_raw_buffer_store_b128(v, r, voff, soff, 16);
  asm volatile("s_nop 1" :: "v"(v) : "memory");
}
DI void st8(rsrc_t r, u32 voff, u32 soff, u32x2 v) { __builtin_amdgcn_raw_buffer_store_b64(v, r, voff, soff, 16); }
DI void st4(rsrc_t r, u32 voff, u32 soff, u32 v) { __builtin_amdgcn_raw_buffer_store_b32(v, r, voff, soff, 16); }
DI bf16x8 asbf(u32x4 v) { return __builtin_bit_cast(bf16x8, v); }
DI u32x4 asu(bf16x8 v) { return __builtin_bit_cast(u32x4, v); }
DI void regather4(u32x4 srd, u32 voff, u32 soff, bf16x8& f0, bf16x8& f1, bf16x8& f2, bf16x8& f3) {
  asm volatile("s_sleep 1\n\tbuffer_load_dwordx4 %0, %4, %5, %6 offen sc1\n\tbuffer_load_dwordx4 %1, %4, %5, %6 offen offset:1024 sc1\n\t"
               "buffer_load_dwordx4 %2, %4, %5, %6 offen offset:2048 sc1\n\tbuffer_load_dwordx4 %3, %4, %5, %6 offen offset:3072 sc1\n\ts_waitcnt vmcnt(0)"
               : "+v"(f0), "+v"(f1), "+v"(f2), "+v"(f3) : "v"(voff), "s"(srd), "s"(soff) : "memory");
}
DI void regather1(u32x4 srd, u32 voff, u32 soff, u32x4& f0) {
  asm volatile("buffer_load_dwordx4 %0, %1, %2, %3 offen sc1" : "=&v"(f0) : "v"(voff), "s"(srd), "s"(soff) : "memory");
}
DI u32 umax(u32 a, u32 b) { return a > b ? a : b; }
DI u32 max4(u32x4 v) { return umax(umax(v[0], v[1]), umax(v[2], v[3])); }

constexpr u64 SPIN_TICKS = 400000ull;
DI bool timed_out(u64 t0) { return __builtin_amdgcn_s_memrealtime() - t0 > SPIN_TICKS; }

#define STAMP(i) do { } while (0)
#define ASTAMP(it, pos) do { } while (0)
#define PSTAMP(sb, i) do { } while (0)
#define MFMA_GAP16(v) asm volatile("s_nop 7" : "+v"(v))
#define MFMA_GAP32(v) asm volatile("s_nop 7\n\ts_nop 3" : "+v"(v))

DI bool flags_ok(u32x4 a, u32x4 b, u32 lo, u32 hi) {
  return __all(a[0] == lo && a[1] == hi && a[2] == lo && a[3] == hi && b[0] == lo && b[1] == hi && b[2] == lo && b[3] == hi);
}
DI void wait_flags(rsrc_t wsr, u32 base, u32 taglo, u32 taghi, int lane, u64 t0) {
  for (;;) {
    const u32x4 a = ld16(wsr, lane * 32, base), b = ld16(wsr, lane * 32 + 16, base);
    if (flags_ok(a, b, taglo, taghi)) break;
    if (timed_out(t0)) break;
    __builtin_amdgcn_s_sleep(1);
    FENCE();
  }
}

DI bf16x8 ones_frag() { return asbf((u32x4){0x3F803F80u, 0x3F803F80u, 0x3F803F80u, 0x3F803F80u}); }
DI float diag_pick(f32x4 g, int lane) { const int r = lane & 3; float a = (r & 1) ? g[1] : g[0], b = (r & 1) ? g[3] : g[2]; return (r & 2) ? b : a; }

struct RowStat { float mu, rstd, var; };
DI RowStat finish_stat(float s, float q) {
  RowStat r; r.mu = s * (1.0f / 512.0f);
  r.var = fmaxf(q * (1.0f / 512.0f) - r.mu * r.mu, 0.0f);
  r.rstd = __builtin_amdgcn_rsqf(r.var + LN_EPS);
  return r;
}
DI bool unwritten(const bf16x8 (&f)[4]) {
  return __any(umax(umax(max4(asu(f[0])), max4(asu(f[1]))), umax(max4(asu(f[2])), max4(asu(f[3])))) == SENT);
}

template <int I, int N, class F> DI void static_for(F&& f) {
  if constexpr (I < N) { f(std::integral_constant<int, I>{}); static_for<I + 1, N>(f); }
}
struct Job { const char* src; u32 prow; u32 f; bool perm; };
template <int I> DI Job retile_job(int b, int w, const u16* wo, const u16* w1, const u16* w2, const u16* w3, const u16* w4) {
  auto small = [&](const u16* m, u32 g, u32 f0, bool perm) { return Job{(const char*)m + (u32)(16 * (g & 31) * 1024 + (g >> 5) * 64), 1024u, f0 + g, perm}; };
  auto big3 = [&](u32 g) { return Job{(const char*)w3 + (u32)(16 * (g & 31) * 3584 + (g >> 5) * 64), 3584u, 1536 + g, false}; };
  auto big4 = [&](u32 mm) { return Job{(const char*)w4 + (u32)(16 * ((b & 15) * 7 + mm) * 1024 + (b >> 4) * 64), 1024u, 3328 + 7 * b + mm, true}; };
  if constexpr (I == 0) return small(w < 2 ? wo : w1, 2 * b + (w & 1), w < 2 ? 0 : 512, w < 2);
  else if constexpr (I == 1) { const Job s = small(w2, 2 * b + (w & 1), 1024, false), t = big3(7 * b + (w & 1)); return w < 2 ? s : t; }
  else if constexpr (I == 2) return big3(7 * b + 2 + w);
  else if constexpr (I == 3) { const Job s = big3(7 * b + 6), t = big4(w - 1); return w == 0 ? s : t; }
  else return big4(3 + w);
}

#define ATT_PIPE 2
#define PRIO_IT 12
#define KV_SPREAD 2
#define CNT_U 2
#define NOMARK 0
#define MARK_IT 1
#define FLAG_IT 5
#define MARK_P1 0
#define MARK_ST 2
#define FM_IT 10
#define FM_CHK 14
#define KVW 4
#define P1_BAR 0
#define FLAG_POS 0
#define PRE_POS 0
#define PUT16 0
#define GB_POS 1
#define O_SLEEP 16
#define SLOT_POS 0
#define W4_WARM 0
#define FPI 6
#define U_IN 0
#define W4_PRIO Q_PRIO
#define A4_SPREAD 1
#define W3B_TOG 0
#define GB_PRIO Q_PRIO
#define UE_P 0
#define U_WIN 5
#define OUT_EARLY 1
#define MID_BAR 0
#define PQ 1
#define Q_PRIO 1
#ifndef M_PRIO
#define M_PRIO 1
#endif
#define U_INIT U_WIN
#define A4_LATE 0
#define STAT_POS 0
#define GB_PK 0
#define A3_N0 14
static_assert(33 + A3_N0 + (W4_WARM == 1 ? 7 : 0) <= 8 * FPI, "fetch schedule");
#define A4_N0 6

constexpr int marks_before(int it) { const int n = it <= MARK_IT ? 0 : (it - MARK_IT) * (4 / MARK_ST); return n < 15 ? n : 15; }
static_assert(MARK_P1 != 2 || (marks_before(FM_IT - 1) == 15 && FM_CHK > FM_IT && FM_CHK > 8 && FLAG_IT < 8), "mark schedule");

__global__ __launch_bounds__(512, 2) void fused_block_kernel(const u16* __restrict__ x, const u16* __restrict__ wq, const u16* __restrict__ wk,
                                                             const u16* __restrict__ wv, const u16* __restrict__ wo, const u16* __restrict__ w1,
                                                             const u16* __restrict__ w2, const u16* __restrict__ w3, const u16* __restrict__ w4,
                                                             u16* __restrict__ out, char* ws) {
  const int tid = threadIdx.x, lane = tid & 63, w = __builtin_amdgcn_readfirstlane(tid >> 6);
  const int b = blockIdx.x;
  const int cl = (b & 7) * 4 + (b >> 6), j = (b >> 3) & 7;
  const u32 lb = lds_base();
  const u32 l16 = lane * 16;
  {
    u64 pc;
    asm volatile("s_getpc_b64 %0" : "=s"(pc));
    dma1((const void*)(pc & ~63ull), (u32)((((b >> 3) * 8 + w) % CODE_KB) * 1024) + l16, lb + L_DUM1);
  }
  asm volatile("" :: "s"(x), "s"(wq), "s"(wk), "s"(wv), "s"(wo), "s"(w1), "s"(w2), "s"(w3), "s"(w4), "s"(out), "s"(ws));
  const u64 t0 = __builtin_amdgcn_s_memrealtime();
  const int i16 = lane & 15, g4 = lane >> 4, r32 = lane & 31, h2 = lane >> 5;
  STAMP(0);

  auto blk_of = [&](int half) { return half * 32 + cl; };
  const int dlt = (j >> 1) == 0 ? 1 : (j >> 1) == 1 ? -1 : (j >> 1) == 2 ? 2 : -2;
  const int kvrt = j >> 1, kvmat = j & 1;
  auto grow = [&](int rho) { return (rho < 32 ? 0 : 1024 - 32) + 32 * cl + rho; };
  const int R0 = grow(16 * kvrt);
  u32x4 xk0, xk1;
  u64 tag;
  {
    const u32 xo = (u32)(i16 * 1024 + g4 * 32);
    asm volatile("global_load_dwordx4 %0, %2, %3\n\tglobal_load_dwordx4 %1, %2, %3 offset:16"
                 : "=&v"(xk0), "=&v"(xk1) : "v"(xo), "s"((const char*)x + R0 * 1024 + w * 128) : "memory");
    dma2((const char*)(kvmat ? wv : wk) + w * 2048, l16, lb + L_WKV + w * 2048);
    asm volatile("s_load_dwordx2 %0, %1, 0x0" : "=s"(tag) : "s"(ws + WS_EPOCH) : "memory");
    if (P1_BAR) asm volatile("s_barrier" ::: "memory");
    const char* xr = (const char*)x + grow(8 * w) * 1024 - 48;
    dma4_rows(xr, l16 + 48, l16 + 32, l16 + 16, l16, lb + L_XIMG + 8 * w * XP);
    dma4_rows(xr + 4096, l16 + 48, l16 + 32, l16 + 16, l16, lb + L_XIMG + (8 * w + 4) * XP);
    dma_strided<8, false, 0x2000>((const char*)wq + w * 65536 + j * 128, (u32)((lane >> 3) * 1024 + (((lane & 7) ^ (4 * ((lane >> 4) & 1))) * 16)), lb + L_WQ + w * 8192, 1024);
    asm volatile("s_waitcnt vmcnt(16) lgkmcnt(0)" : "+v"(xk0), "+v"(xk1), "+s"(tag) :: "memory");
  }
  tag += 1ull;
  const u32 taglo = (u32)tag, taghi = (u32)(tag >> 32);
  const rsrc_t wsr = mkrsrc(ws, WS_END);
  const u32x4 srd = {(u32)(uintptr_t)ws, (u32)((uintptr_t)ws >> 32) & 0xFFFFu, WS_END, 0x00020000u};
  const bf16x8 one = ones_frag();
  STAMP(1);

  {
    f32x4 am = {0, 0, 0, 0}, ac = {0, 0, 0, 0}, as = {0, 0, 0, 0}, aq = {0, 0, 0, 0};
    const u32 wa = L_WKV + w * 2048 + (16 * g4 + (i16 >> 2)) * 32 + (i16 & 3) * 8;
    const bf16x8 wf0 = cat(ldstr(wa), ldstr(wa + 128)), wf1 = cat(ldstr(wa + 256), ldstr(wa + 384));
    const bf16x8 xf0 = asbf(xk0), xf1 = asbf(xk1);
    as = mfma16(one, xf0, as); aq = mfma16(xf0, xf0, aq); as = mfma16(one, xf1, as); aq = mfma16(xf1, xf1, aq);
    if (kvmat) {
      am = mfma16(xf0, wf0, am); ac = mfma16(one, wf0, ac); am = mfma16(xf1, wf1, am); ac = mfma16(one, wf1, ac);
      MFMA_GAP16(am); MFMA_GAP16(ac);
      if (lane < 16) ldsw<float>(S_PC + (w * 16 + lane) * 4, ac[0]);
    } else {
      am = mfma16(wf0, xf0, am); ac = mfma16(wf0, one, ac); am = mfma16(wf1, xf1, am); ac = mfma16(wf1, one, ac);
      MFMA_GAP16(am); MFMA_GAP16(ac);
      if (i16 == 0) ldsw<f32x4>(S_PC + (w * 16 + 4 * g4) * 4, ac);
    }
    ldsw<f32x4>(S_PM + w * 1024 + l16, am);
    if (lane < 16) ldsw<float>(S_PS + (lane * 8 + w) * 4, as[0]);
    if ((i16 >> 2) == g4) ldsw<float>(S_PQ + (i16 * 8 + w) * 4, diag_pick(aq, lane));
    BAR();
  }
  if (w == KVW) {
    {
      const f32x4 s0 = ldsr<f32x4>(S_PS + i16 * 32), s1 = ldsr<f32x4>(S_PS + i16 * 32 + 16);
      const f32x4 q0 = ldsr<f32x4>(S_PQ + i16 * 32), q1 = ldsr<f32x4>(S_PQ + i16 * 32 + 16);
      const f32x4 s = s0 + s1, q = q0 + q1;
      const RowStat st = finish_stat((s[0] + s[1]) + (s[2] + s[3]), (q[0] + q[1]) + (q[2] + q[3]));
      if (lane < 16) { ldsw<float>(S_FIN + lane * 4, st.mu); ldsw<float>(S_FIN + 64 + lane * 4, st.rstd); }
    }
    f32x4 m = {0, 0, 0, 0};
#pragma unroll
    for (int ww = 0; ww < 8; ++ww) m += ldsr<f32x4>(S_PM + ww * 1024 + l16);
    f32x4 v;
    u32 off;
    if (kvmat) {
      float c1 = 0.f;
#pragma unroll
      for (int ww = 0; ww < 8; ++ww) c1 += ldsr<float>(S_PC + (ww * 16 + i16) * 4);
      asm volatile("s_waitcnt lgkmcnt(0)" ::: "memory");
      const f32x4 mu = ldsr<f32x4>(S_FIN + g4 * 16), rs = ldsr<f32x4>(S_FIN + 64 + g4 * 16);
      v = rs * (m - mu * c1);
      off = WS_V + (R0 >> 5) * 1024 + ((R0 >> 4) & 1) * 512 + (g4 & 1) * 256 + i16 * 16 + (g4 >> 1) * 8;
    } else {
      f32x4 c4 = {0, 0, 0, 0};
#pragma unroll
      for (int ww = 0; ww < 8; ++ww) c4 += ldsr<f32x4>(S_PC + (ww * 16 + 4 * g4) * 4);
      asm volatile("s_waitcnt lgkmcnt(0)" ::: "memory");
      const float mu = ldsr<float>(S_FIN + i16 * 4), rs = ldsr<float>(S_FIN + 64 + i16 * 4);
      v = rs * (m - mu * c4);
      off = WS_K + (R0 >> 5) * 1024 + (g4 & 1) * 512 + ((R0 & 31) + i16) * 16 + (g4 >> 1) * 8;
    }
    st8(wsr, off, 0, (u32x2){cvtpk(v[0], v[1]), cvtpk(v[2], v[3])});
    WAITV(1);
  } else if (MARK_P1 == 1 && w >= 4) {
      const rsrc_t mr = mkrsrc(ws, WS_END);
      const u32x4 s4 = {SENT, SENT, SENT, SENT};
      const int wm = w & 3;
#pragma unroll
      for (int half = 0; half < 2; ++half) {
        const u32 po = blk_of(half) * 32768 + (4 * j + wm) * 1024;
        __builtin_amdgcn_raw_buffer_store_b128(s4, mr, l16, WS_OX + po, 16);
        __builtin_amdgcn_raw_buffer_store_b128(s4, mr, l16, WS_HX + po, 16);
        __builtin_amdgcn_raw_buffer_store_b128(s4, mr, l16, WS_X1 + po, 16);
        const int R = blk_of(half) * 32 + (lane & 31) + dlt;
        const u32 mk = (u32)R < 2048u ? SENT : 0u;
        st16(mr, ((R & 2047) >> 5) * 32768 + (lane >> 5) * 512 + (R & 31) * 16, WS_TS + (4 * j + wm) * 1024, (u32x4){mk, mk, mk, mk});
      }
      const u32 uo = WS_UX + (blk_of(wm >> 1) * 112 + 14 * j + 7 * (wm & 1)) * 1024;
#pragma unroll
      for (int i = 0; i < 7; ++i) st16(mr, l16 + (i & 3) * 1024, uo + (i >> 2) * 4096, s4);
    WAITV(15);
  } else {
    WAITV(0);
  }
#define JOB(i) retile_job<i>(b, w, wo, w1, w2, w3, w4)
  u32x4 rts[5];
  if (w < 4) {
    static_for<0, 5>([&](auto I) {
      const Job jb = JOB(I.value);
      rts[I.value] = *(const GAS u32x4*)(jb.src + (u32)((lane >> 2) * jb.prow + (lane & 3) * 16));
    });
  }
  FENCE();
  STAMP(2);
  const int q_hp = w & 1, q_rt = (w >> 1) & 1, q_kh = w >> 2;
  const int head = 2 * q_hp + q_kh;
  bf16x8 qf;
  {
    BAR();
    STAMP(3);
    {
      f32x4 as = {0, 0, 0, 0}, aq = {0, 0, 0, 0};
      const int rt = w & 3, kh = w >> 2;
      const u32 sa = L_XIMG + (16 * rt + i16) * XP + kh * 512 + g4 * 16;
#pragma unroll
      for (int t = 0; t < 8; ++t) {
        const bf16x8 xf = ldsr<bf16x8>(sa + t * 64);
        as = mfma16(one, xf, as); aq = mfma16(xf, xf, aq);
      }
      MFMA_GAP16(as); MFMA_GAP16(aq);
      if (lane < 16) ldsw<float>(S_QS + ((16 * rt + lane) * 2 + kh) * 4, as[0]);
      if ((i16 >> 2) == g4) ldsw<float>(S_QS + 512 + ((16 * rt + i16) * 2 + kh) * 4, diag_pick(aq, lane));
    }
    auto kv_flag = [&]() {
      if (w == KVW) {
        if (KVW < 4) WAITV(5); else WAITV(0);
        if (lane < 8) __hip_atomic_store((GAS u64*)(ws + WS_FKV + lane * 2048 + b * 8), tag, __ATOMIC_RELAXED, __HIP_MEMORY_SCOPE_AGENT);
      }
    };
    if (FLAG_POS == 0) kv_flag();
    if (tid < 272) ldsw<u32x4>(L_ONES + tid * 16, (u32x4){0x3F803F80u, 0x3F803F80u, 0x3F803F80u, 0x3F803F80u});
    if (tid == 511) ldsw<u32x4>(L_CNT, (u32x4){0u, 0u, 0u, 0u});
    f32x16 acc = {}, accc = {};
    const int q = i16 >> 2, p = i16 & 3, cb = g4 & 1;
    const u32 wa = L_WQ + (256 * q_kh + 8 * h2 + q) * 128 + (((4 * q_hp + 2 * cb + (p >> 1)) ^ (4 * ((q >> 1) & 1))) * 16) + (p & 1) * 8;
    const u32 xa = L_XIMG + (32 * q_rt + r32) * XP + q_kh * 512 + h2 * 16;
#pragma unroll
    for (int t = 0; t < 16; ++t) {
      const bf16x8 af = cat(ldstr(wa + t * 2048), ldstr(wa + t * 2048 + 512));
      acc = mfma32(af, ldsr<bf16x8>(xa + t * 32), acc);
      if (FLAG_POS == 1 && t == 3) kv_flag();
    }
    u32x4 fka, fkb;
    if (w == 1) { fka = ld16(wsr, lane * 32, WS_FKV + (b & 7) * 2048); fkb = ld16(wsr, lane * 32 + 16, WS_FKV + (b & 7) * 2048); }
    const u32 wb = wa + q_rt * 16384;
#pragma unroll
    for (int t = 0; t < 8; ++t) accc = mfma32(cat(ldstr(wb + t * 2048), ldstr(wb + t * 2048 + 512)), one, accc);
    if (FLAG_POS == 2) kv_flag();
    MFMA_GAP32(acc); MFMA_GAP32(accc);
    f32x4 k0, k1, s0, s1;
    if (q_kh) {
      k0 = (f32x4){acc[8], acc[9], acc[10], acc[11]}; k1 = (f32x4){acc[12], acc[13], acc[14], acc[15]};
      s0 = (f32x4){acc[0], acc[1], acc[2], acc[3]}; s1 = (f32x4){acc[4], acc[5], acc[6], acc[7]};
    } else {
      k0 = (f32x4){acc[0], acc[1], acc[2], acc[3]}; k1 = (f32x4){acc[4], acc[5], acc[6], acc[7]};
      s0 = (f32x4){acc[8], acc[9], acc[10], acc[11]}; s1 = (f32x4){acc[12], acc[13], acc[14], acc[15]};
    }
    ldsw<f32x4>(L_WKV + w * 2048 + l16, s0); ldsw<f32x4>(L_WKV + w * 2048 + 1024 + l16, s1);
    if (r32 == 0) {
#pragma unroll
      for (int a = 0; a < 4; ++a) ldsw<f32x4>(S_QC + (((q_hp * 2 + q_rt) * 2 + q_kh) * 32 + 8 * a + 4 * h2) * 4, (f32x4){accc[4 * a], accc[4 * a + 1], accc[4 * a + 2], accc[4 * a + 3]});
    }
    if (w == 1) {
      if (!flags_ok(fka, fkb, taglo, taghi)) wait_flags(wsr, WS_FKV + (b & 7) * 2048, taglo, taghi, lane, t0);
      if (b == 0 && lane == 0) __hip_atomic_store((GAS u64*)(ws + WS_EPOCH), tag, __ATOMIC_RELAXED, __HIP_MEMORY_SCOPE_AGENT);
    }
    BAR();
    STAMP(5);
    dma_strided<KV_SPREAD ? 2 : 3, true, 0x2000>(ws + WS_K, w * 1024 + l16, lb + L_K + w * 1024, 8192);
    dma_strided<2, true, 0x2000>(ws + WS_V, w * 1024 + l16, lb + L_V + w * 1024, 8192);
    k0 += ldsr<f32x4>(L_WKV + (w ^ 4) * 2048 + l16); k1 += ldsr<f32x4>(L_WKV + (w ^ 4) * 2048 + 1024 + l16);
    f32x4 c0 = {0, 0, 0, 0}, c1 = {0, 0, 0, 0};
#pragma unroll
    for (int s = 0; s < 4; ++s) {
      c0 += ldsr<f32x4>(S_QC + ((q_hp * 4 + s) * 32 + 16 * q_kh + 4 * h2) * 4);
      c1 += ldsr<f32x4>(S_QC + ((q_hp * 4 + s) * 32 + 16 * q_kh + 8 + 4 * h2) * 4);
    }
    const f32x2 sp = ldsr<f32x2>(S_QS + (32 * q_rt + r32) * 8), qp = ldsr<f32x2>(S_QS + 512 + (32 * q_rt + r32) * 8);
    const RowStat st = finish_stat(sp[0] + sp[1], qp[0] + qp[1]);
    const float sc = st.rstd * QSCALE;
    k0 = (k0 - st.mu * c0) * sc; k1 = (k1 - st.mu * c1) * sc;
    qf = opaque(asbf((u32x4){cvtpk(k0[0], k0[1]), cvtpk(k0[2], k0[3]), cvtpk(k1[0], k1[1]), cvtpk(k1[2], k1[3])}));
  }
  STAMP(4);
  const u32 pc_in = (u32)((4 * j + (w >> 1)) * 1024 + (w & 1) * 512);
  auto hx_so = [&](u32 base, int half) { return base + blk_of(half) * 32768 + pc_in; };
  auto ts_vo = [&](int half) {
    const u32 l = opaque<u32>(lane);
    const int rr = (int)(l >> 1) + dlt;
    const u32 R = (u32)(blk_of(half) * 32 + rr);
    const u32 o = (u32)((rr >> 5) + 1) * 32768 + (u32)(rr & 31) * 16 + (l & 1) * 8;
    return R < 2048u && !(PUT16 && (l & 1)) ? o : OOB;
  };
  auto ox_off = [&]() { return WS_OX + blk_of(q_rt) * 32768 + (4 * j + head) * 1024 + l16; };
  {
    WAITV(0);
    asm volatile("" : "+v"(rts[0]), "+v"(rts[1]), "+v"(rts[2]), "+v"(rts[3]), "+v"(rts[4]));
    BAR();
    STAMP(6);
  }
  u32 opk[4];
  bf16x8 a_o[8], a_1[8], a_2[8], a3[32];
  u32x2 xres[2];
  {
    u32 ka = L_K + l16;
    const bool isv = r32 < 16;
    u32 va = isv ? L_V + h2 * 256 + r32 * 16 : L_ONES + ((r32 >= 20 && r32 < 28) ? 64 : 0);
    const u32 vinc = isv ? 4096u : 0u;
    f32x16 o = {};
    f32x16 negm;
    {
      const bf16x8 kf = ldsr<bf16x8>(ka);
      f32x16 z = {};
      const f32x16 s = mfma32(kf, qf, z);
      float m = fmaxf(fmaxf(s[0], s[1]), fmaxf(s[2], s[3]));
#pragma unroll
      for (int i = 4; i < 16; i += 2) m = fmaxf(m, fmaxf(s[i], s[i + 1]));
      const auto rr = __builtin_amdgcn_permlane32_swap(__float_as_uint(m), __float_as_uint(m), false, false);
      m = fmaxf(__uint_as_float(rr[0]), __uint_as_float(rr[1]));
#pragma unroll
      for (int i = 0; i < 16; ++i) negm[i] = -m;
    }
    f32x16 s = mfma32(ldsr<bf16x8>(ka), qf, negm);
    bf16x8 kf = ldsr<bf16x8>(ka + 1024), v0 = ldsr<bf16x8>(va), v1 = ldsr<bf16x8>(va + 512);
#define SB() __builtin_amdgcn_sched_barrier(0)
    auto tiles4 = [&](auto&& hook) {
#pragma unroll
      for (int u = 0; u < 4; ++u) {
        float e[16];
        u32 pk[8];
        f32x16 sn;
        auto ex = [&](int a, int b_) {
#pragma unroll
          for (int i = a; i < b_; ++i) e[i] = opaque(__builtin_amdgcn_exp2f(s[i]));
        };
        auto cv = [&](int a, int b_) {
#pragma unroll
          for (int i = a; i < b_; ++i) pk[i] = opaque(cvtpk(e[2 * i], e[2 * i + 1]));
        };
        auto ms = [&]() { sn = mfma32(kf, qf, negm); asm volatile("" : "+v"(sn)); kf = ldsr<bf16x8>(ka + (u + 2) * 1024); };
        if (u == 3) va += vinc;
        const u32 vn = va + ((u + 1) & 3) * 1024;
        auto m0 = [&]() { o = mfma32(v0, asbf((u32x4){pk[0], pk[1], pk[2], pk[3]}), o); asm volatile("" : "+v"(o)); v0 = ldsr<bf16x8>(vn); };
        auto m1 = [&]() { o = mfma32(v1, asbf((u32x4){pk[4], pk[5], pk[6], pk[7]}), o); asm volatile("" : "+v"(o)); v1 = ldsr<bf16x8>(vn + 512); };
        if (ATT_PIPE == 1) {
          ex(0, 4); SB(); ms(); hook(u); SB(); ex(4, 8); cv(0, 4); SB(); m0(); SB(); ex(8, 16); cv(4, 8); SB(); m1(); SB();
        } else if (ATT_PIPE == 2) {
          ex(0, 4); SB(); ms(); hook(u); SB(); ex(4, 8); cv(0, 4); ex(8, 10); SB(); m0(); SB(); ex(10, 16); cv(4, 8); SB(); m1(); SB();
        } else if (ATT_PIPE == 3) {
          ms(); hook(u); SB(); ex(0, 8); cv(0, 4); SB(); m0(); SB(); ex(8, 16); cv(4, 8); SB(); m1(); SB();
        } else if (ATT_PIPE == 4) {
          ex(0, 6); cv(0, 2); SB(); ms(); hook(u); SB(); ex(6, 8); cv(2, 4); ex(8, 12); SB(); m0(); SB(); ex(12, 16); cv(4, 8); SB(); m1(); SB();
        } else {
          ex(0, 4); cv(0, 2); SB(); ms(); hook(u); SB(); ex(4, 8); cv(2, 4); SB(); m0(); SB(); ex(8, 12); cv(4, 6); ex(12, 16); cv(6, 8); SB(); m1(); SB();
        }
        s = sn;
      }
      ka += 4096;
    };
    if (w < 4) __builtin_amdgcn_s_setprio(2); else __builtin_amdgcn_s_setprio(1);
    auto kv_rest = [&](u32 img, u32 limg, int c0, auto n) {
      dma_strided<n.value, true, 4096>(ws + img + c0 * 8192, opaque<u32>((w & 3) * 1024 + l16), lb + limg + c0 * 8192 + (w & 3) * 1024, 4096);
    };
    using N2 = std::integral_constant<int, 2>; using N4 = std::integral_constant<int, 4>;
    auto lds_count = [&](u32 off) {
      u32 n;
      asm volatile("ds_add_rtn_u32 %0, %1, %2\n\ts_waitcnt lgkmcnt(0)" : "=v"(n) : "v"(lb + off), "v"(lane == 0 ? 1u : 0u) : "memory");
      return (u32)__builtin_amdgcn_readfirstlane(n);
    };
    const u32x4 s4 = {SENT, SENT, SENT, SENT};
    const u32 kvo = opaque<u32>((w & 3) * 1024 + l16);
    static_assert(WS_V - WS_K == 0x10000 && L_V - L_K == 0x10000, "K and V images");
    u32x4 fa, fb;
    auto fetch = [&](int p, u32 l) {
      const u32 fo = (2 * j * 32 + 4 * w) * 1024;
      if (p < 24) {
        const int t = p & 7;
        const bf16x8 v = asbf(ld16(wsr, l + (t & 3) * 1024, (p < 8 ? WS_WO : p < 16 ? WS_W1 : WS_W2) + fo + (t >> 2) * 32768));
        if (p < 8) a_o[t] = v; else if (p < 16) a_1[t] = v; else a_2[t] = v;
      } else if (p < 26) {
        xres[p - 24] = *(const GAS u32x2*)((const char*)x + (u32)((blk_of(p - 24) * 32 + (l >> 5)) * 1024 + (64 * j + 8 * w) * 2 + ((l >> 4) & 1) * 8));
      } else if (p < 26 + A3_N0) {
        a3[p - 26] = asbf(ld16(wsr, w < 7 ? ((7 * j + w) * 32) * 1024 + l : OOB, WS_W3 + (p - 26) * 1024));
      } else if (p < 33 + A3_N0) {
        dma1(ws + WS_W3 + (p - 26 - A3_N0) * 1024, (((u32)(b >> 6) & 3u) * 8 + w) * 7168 + j * 229376 + l, lb + L_DUM1);
      } else if (W4_WARM == 1 && p < 40 + A3_N0) {
        dma1(ws + WS_W4 + (p - 33 - A3_N0) * 1024, (((u32)(b >> 6) & 3u) * 8 + w) * 7168 + j * 229376 + l, lb + L_DUM1);
      }
    };
    static_for<0, 16>([&](auto IT) {
      constexpr int it = IT.value;
      ASTAMP(it, 0);
      if (w < 4) {
        if (it == 0) {
          const u32 l = opaque<u32>(lane);
          const u32 sb = L_STG + w * 5120;
#pragma unroll
          for (int i = 0; i < 5; ++i) ldsw<u32x4>(sb + i * 1024 + l * 16, rts[i]);
          const u32 tb = sb + ((l & 15) >> 2) * 64 + ((l >> 4) & 1) * 32 + (l & 3) * 8;
          static_for<0, 5>([&](auto I) {
            const Job jb = JOB(I.value);
            const u32 t0a = tb + I.value * 1024 + (l >> 5) * (jb.perm ? 256 : 512);
            st16(wsr, l * 16, WS_WO + jb.f * 1024, asu(cat(ldstr(t0a), ldstr(t0a + (jb.perm ? 512 : 256)))));
          });
        } else if (it == MARK_IT && !NOMARK && MARK_P1 == 0) {
          const u32 l = opaque<u32>(lane);
#pragma unroll
          for (int half = 0; half < 2; ++half) {
            const u32 po = blk_of(half) * 32768 + (4 * j + w) * 1024;
            st16(wsr, l * 16, WS_OX + po, s4); st16(wsr, l * 16, WS_HX + po, s4); st16(wsr, l * 16, WS_X1 + po, s4);
            const int R = blk_of(half) * 32 + (int)(l & 31) + dlt;
            const u32 mk = (u32)R < 2048u ? SENT : 0u;
            st16(wsr, ((R & 2047) >> 5) * 32768 + (l >> 5) * 512 + (R & 31) * 16, WS_TS + (4 * j + w) * 1024, (u32x4){mk, mk, mk, mk});
          }
        } else if (it == MARK_IT + 1 && !NOMARK && MARK_P1 == 0) {
          const u32 uo = WS_UX + (blk_of(w >> 1) * 112 + 14 * j + 7 * (w & 1)) * 1024;
          const u32 l = opaque<u32>(l16);
#pragma unroll
          for (int i = 0; i < 7; ++i) st16(wsr, l + (i & 3) * 1024, uo + (i >> 2) * 4096, s4);
        } else if (it == FLAG_IT) {
          asm volatile("s_waitcnt vmcnt(%0)" :: "n"(MARK_P1 == 2 ? marks_before(FLAG_IT) : 0) : "memory");
          if (lds_count(L_CNT + 4) == 3 && lane < 8) __hip_atomic_store((GAS u64*)(ws + WS_FW + lane * 2048 + b * 8), tag, __ATOMIC_RELAXED, __HIP_MEMORY_SCOPE_AGENT);
        } else if (MARK_P1 == 2 && it == FM_IT) {
          asm volatile("s_waitcnt vmcnt(%0)" :: "n"(FM_IT - 1 >= 8 ? FPI : 0) : "memory");
          if (lds_count(L_CNT + 12) == 3 && lane < 8) __hip_atomic_store((GAS u64*)(ws + WS_FM + lane * 2048 + b * 8), tag, __ATOMIC_RELAXED, __HIP_MEMORY_SCOPE_AGENT);
        }
      } else {
        if (KV_SPREAD) {
          if (KV_SPREAD == 1 && it >= 1 && it <= 3) {
            WAITV(0);
            if (lane == 0) asm volatile("ds_add_u32 %0, %1" :: "v"(lb + L_CNT), "v"(1u) : "memory");
          }
        } else if (it == 0) {
          kv_rest(WS_V, L_V, 2, N4{}); kv_rest(WS_K, L_K, 3, N4{}); kv_rest(WS_V, L_V, 4, N4{}); kv_rest(WS_K, L_K, 5, N4{});
          kv_rest(WS_V, L_V, 6, N4{}); kv_rest(WS_K, L_K, 7, N2{});
        } else if (it == 1) {
          WAITV(0);
          lds_count(L_CNT);
        }
      }
      if (KV_SPREAD ? (it & 3) == 3 && it < 12 : it == (ATT_PIPE ? 3 : 4)) {
        while ((u32)__builtin_amdgcn_readfirstlane(*(volatile LAS u32*)((LAS char*)smem + L_CNT)) < (KV_SPREAD ? (u32)(it + 1) : 4u)) {
          if (timed_out(t0)) break;
          __builtin_amdgcn_s_sleep(1);
        }
        FENCE();
      }
      if (it == 7) {
        const u32 l = opaque<u32>(l16);
        fa = ld16(wsr, l * 2, WS_FW + (b & 7) * 2048); fb = ld16(wsr, l * 2 + 16, WS_FW + (b & 7) * 2048);
      }
      if (it == 8) {
        if (!flags_ok(fa, fb, taglo, taghi)) wait_flags(wsr, WS_FW + (b & 7) * 2048, taglo, taghi, lane, t0);
      }
      if (MARK_P1 == 2 && it == FM_CHK - 1) {
        const u32 l = opaque<u32>(l16);
        fa = ld16(wsr, l * 2, WS_FM + (b & 7) * 2048); fb = ld16(wsr, l * 2 + 16, WS_FM + (b & 7) * 2048);
      }
      if (MARK_P1 == 2 && it == FM_CHK) {
        if (!flags_ok(fa, fb, taglo, taghi)) wait_flags(wsr, WS_FM + (b & 7) * 2048, taglo, taghi, lane, t0);
      }
      if (it >= 8) {
        const u32 l = opaque<u32>(l16);
#pragma unroll
        for (int p = FPI * (it - 8); p < FPI * (it - 7); ++p) fetch(p, l);
      }
      if (it == PRIO_IT) asm volatile("s_cmp_ge_u32 %0, 4\n\ts_cbranch_scc1 .Lpe%=\n\ts_setprio 0\n.Lpe%=:" :: "s"(w) : "scc");
      FENCE();
      ASTAMP(it, 1);
      tiles4([&](int u) {
        if (KV_SPREAD == 2 && it >= 1 && it <= 3 && u == CNT_U) count_late<(it < 3 ? 2 * CNT_U : 0)>(w, lb + L_CNT);
        if (MARK_P1 == 2 && it >= MARK_IT && (u % MARK_ST) == 0) {
          const int n = marks_before(it) + u / MARK_ST;
          if (n < 8) {
            const int half = n >> 2, kind = n & 3;
            const u32 l = opaque<u32>(lane);
            if (kind < 3) st16_early(w, srd, l * 16, (kind == 0 ? WS_OX : kind == 1 ? WS_HX : WS_X1) + blk_of(half) * 32768 + (4 * j + w) * 1024, s4);
            else {
              const int R = blk_of(half) * 32 + (int)(l & 31) + dlt;
              const u32 mk = (u32)R < 2048u ? SENT : 0u;
              st16_early(w, srd, ((R & 2047) >> 5) * 32768 + (l >> 5) * 512 + (R & 31) * 16, WS_TS + (4 * j + w) * 1024, (u32x4){mk, mk, mk, mk});
            }
          } else if (n < 15) {
            const int i = n - 8;
            st16_early(w, srd, opaque<u32>(l16), WS_UX + (blk_of(w >> 1) * 112 + 14 * j + 7 * (w & 1) + i) * 1024, s4);
          }
        }
        if (KV_SPREAD && it < 3) {
          const int T = 16 + 16 * it + 4 * u;
          dma_pair_late(w, ws + WS_K + T * 1024, kvo, lb + L_K + T * 1024 + (w & 3) * 1024);
        }
      });
      FENCE();
      if (it == 9) STAMP(7);
    });
    STAMP(8);
    __builtin_amdgcn_s_setprio(0);
    ka = L_K + l16;
    float l = o[8];
    if (!__all(l <= 1.8446744e19f)) {
      float m = -INFINITY;
      o = (f32x16){};
      u32 vb = isv ? L_V + h2 * 256 + r32 * 16 : L_ONES;
      for (int t = 0; t < 64; ++t) {
        f32x16 z = {};
        const f32x16 sx = mfma32(ldsr<bf16x8>(ka + t * 1024), qf, z);
        float mt = sx[0];
#pragma unroll
        for (int i = 1; i < 16; ++i) mt = fmaxf(mt, sx[i]);
        const auto rr = __builtin_amdgcn_permlane32_swap(__float_as_uint(mt), __float_as_uint(mt), false, false);
        mt = fmaxf(fmaxf(__uint_as_float(rr[0]), __uint_as_float(rr[1])), m);
        const float f = __builtin_amdgcn_exp2f(m - mt);
        m = mt;
        u32 pk[8];
#pragma unroll
        for (int i = 0; i < 8; ++i) pk[i] = cvtpk(__builtin_amdgcn_exp2f(sx[2 * i] - m), __builtin_amdgcn_exp2f(sx[2 * i + 1] - m));
#pragma unroll
        for (int i = 0; i < 16; ++i) o[i] *= f;
        o = mfma32(ldsr<bf16x8>(vb), asbf((u32x4){pk[0], pk[1], pk[2], pk[3]}), o);
        o = mfma32(ldsr<bf16x8>(vb + 512), asbf((u32x4){pk[4], pk[5], pk[6], pk[7]}), o);
        vb += isv ? 1024u : 0u;
      }
      MFMA_GAP32(o);
      l = o[8];
    }
    const float rl = __builtin_amdgcn_rcpf(l);
#pragma unroll
    for (int i = 0; i < 4; ++i) opk[i] = canon(cvtpk(o[2 * i] * rl, o[2 * i + 1] * rl));
  }
  st16(wsr, ox_off(), 0, (u32x4){opk[0], opk[1], opk[2], opk[3]});
  STAMP(9);
  bf16x8 sel;
  auto make_sel = [&]() {
    const u32 l = opaque<u32>(lane), e = ((l >> 2) & 3) != (l >> 4) ? 0u : (l & 1) ? 0x3F800000u : 0x00003F80u;
    const u32 d0 = (l & 2) ? 0u : e, d1 = (l & 2) ? e : 0u;
    sel = asbf((u32x4){d0, d1, d0, d1});
  };
  make_sel();
  const u32 gvo = opaque<u32>(w * 4096 + l16) & 0x73F0u;
  auto pw_of = [&](u32 l) { return L_P + ((l >> 3) & 3) * 256 + (w >> 1) * 1024 + (l & 7) * 32 + (l >> 5) * 16 + (((w ^ (l >> 3)) & 1) * 8); };
  const u32 pw = pw_of(lane);
  const u32 pr = L_P + gvo;
  auto g_load = [&](bf16x8 (&f)[4], u32 src) {
#pragma unroll
    for (int t = 0; t < 4; ++t) f[t] = asbf(ld16(wsr, gvo | (t * 1024), src));
  };
  auto g_wait = [&](bf16x8 (&f)[4], u32 src) {
    if (__builtin_expect(unwritten(f), 0)) {
      do {
        if (timed_out(t0)) break;
        regather4(srd, gvo, src, f[0], f[1], f[2], f[3]);
      } while (unwritten(f));
    }
    asm volatile("" : "+v"(f[0]), "+v"(f[1]), "+v"(f[2]), "+v"(f[3]));
  };
  auto row_stats = [&](int half, const bf16x8 (&f)[4]) {
    float s0 = 0.f, s1 = 0.f, q0 = 0.f, q1 = 0.f;
#pragma unroll
    for (int t = 0; t < 4; ++t) {
      const u32x4 d = asu(f[t]);
      s0 = dot2(d[0], 0x3F803F80u, s0); q0 = dot2(d[0], d[0], q0); s1 = dot2(d[1], 0x3F803F80u, s1); q1 = dot2(d[1], d[1], q1);
      s0 = dot2(d[2], 0x3F803F80u, s0); q0 = dot2(d[2], d[2], q0); s1 = dot2(d[3], 0x3F803F80u, s1); q1 = dot2(d[3], d[3], q1);
    }
    const f32x2 sq = {swap32_add(s0 + s1), swap32_add(q0 + q1)};
    if (lane < 32) ldsw<f32x2>(L_STAT + half * STH + lane * STP + w * 8, sq);
  };
  auto partials = [&](int half, f32x16& acc0, f32x16& acc1, u32 pw) {
#pragma unroll
    for (int a = 0; a < 4; ++a) {
      ldsw<u32x2>(pw + half * 32768 + a * 4096, (u32x2){cvtpk(acc0[4 * a], acc0[4 * a + 1]), cvtpk(acc0[4 * a + 2], acc0[4 * a + 3])});
      ldsw<u32x2>(pw + half * 32768 + (4 + a) * 4096, (u32x2){cvtpk(acc1[4 * a], acc1[4 * a + 1]), cvtpk(acc1[4 * a + 2], acc1[4 * a + 3])});
    }
  };
  const u32 stw = opaque<u32>(L_STAT + r32 * STP + w * 8);
  auto slice_gemm = [&](int half, const bf16x8 (&a)[8], const bf16x8 (&f)[4], bool stats, auto&& mid) {
    f32x16 acc0 = {}, acc1 = {};
    auto stat_block = [&]() {
      float s0 = 0.f, s1 = 0.f, q0 = 0.f, q1 = 0.f;
#pragma unroll
      for (int t = 0; t < 4; ++t) {
        const u32x4 d = asu(f[t]);
        s0 = dot2(d[0], 0x3F803F80u, s0); q0 = dot2(d[0], d[0], q0); s1 = dot2(d[1], 0x3F803F80u, s1); q1 = dot2(d[1], d[1], q1);
        s0 = dot2(d[2], 0x3F803F80u, s0); q0 = dot2(d[2], d[2], q0); s1 = dot2(d[3], 0x3F803F80u, s1); q1 = dot2(d[3], d[3], q1);
      }
      ldsw<f32x2>(stw + half * STH, (f32x2){swap32_add(s0 + s1), swap32_add(q0 + q1)});
      __builtin_amdgcn_sched_barrier(0);
    };
    if (stats && STAT_POS == 0) stat_block();
#pragma unroll
    for (int t = 0; t < 4; ++t) {
      acc0 = mfma32(a[t], f[t], acc0);
      acc1 = mfma32(a[4 + t], f[t], acc1);
    }
    __builtin_amdgcn_sched_barrier(0);
    if (stats && STAT_POS == 1) stat_block();
    mid();
    __builtin_amdgcn_sched_barrier(0);
    partials(half, acc0, acc1, pw);
  };
  auto reduce_at = [&](int half, u32 pr_) -> f32x4 {
    f32x4 acc = {0, 0, 0, 0};
#pragma unroll
    for (int q = 0; q < 4; ++q) acc = mfma16(sel, ldsr<bf16x8>(pr_ + half * 32768 + q * 1024), acc);
    return acc;
  };
  auto reduce = [&](int half) { return reduce_at(half, pr); };
  auto src_of = [&](u32 base, int half) { return base + blk_of(half) * 32768; };
  const u32 hvo = opaque<u32>(PUT16 && (lane & 1) ? OOB : (u32)lane * 8);
  auto put_at = [&](u32 vo, u32 so, u32x2 v) {
    if (PUT16) {
      const u32 n0 = __builtin_amdgcn_mov_dpp(v[0], 0xB1, 0xF, 0xF, true), n1 = __builtin_amdgcn_mov_dpp(v[1], 0xB1, 0xF, 0xF, true);
      st16(wsr, vo, so, (u32x4){v[0], v[1], n0, n1});
    } else st8(wsr, vo, so, v);
  };
  auto pack4 = [&](f32x4 v) { return (u32x2){canon(cvtpk(v[0], v[1])), canon(cvtpk(v[2], v[3]))}; };
  const u32 a3off = w < 7 ? ((7 * j + w) * 32) * 1024 + l16 : OOB;
  auto a3_load = [&](int t0_, int t1_) {
#pragma unroll
    for (int t = 0; t < 32; ++t) if (t >= t0_ && t < t1_) a3[t] = asbf(ld16(wsr, a3off, WS_W3 + t * 1024));
  };

  auto a3_slot = [&](int k) {
    constexpr int n = 32 - A3_N0;
    a3_load(A3_N0 + k * n / 6, A3_N0 + (k + 1) * n / 6);
  };
  f32x4 cs3p = {0.f, 0.f, 0.f, 0.f};
  auto cs3_acc = [&](int t0_, int t1_) {
#pragma unroll
    for (int t = 0; t < 32; ++t) if (t >= t0_ && t < t1_) cs3p = mfma16(sel, a3[t], cs3p);
  };
  f32x4 hres[2], xp[2], cs1;
  bf16x8 fA[4], fB[4];
  const u32 str = opaque<u32>(L_STAT + (lane >> 1) * STP + (lane & 1) * 32);
  auto back = [&](int half, bool stats, auto&& epi, auto&& hook) {
    bf16x8 p[4];
    f32x4 p0 = {0, 0, 0, 0}, p1 = {0, 0, 0, 0};
#pragma unroll
    for (int q = 0; q < 4; ++q) p[q] = ldsr<bf16x8>(pr + half * 32768 + q * 1024);
    if (stats) { p0 = ldsr<f32x4>(str + half * STH); p1 = ldsr<f32x4>(str + half * STH + 16); }
    hook();
    __builtin_amdgcn_sched_barrier(0);
    f32x4 acc = {0, 0, 0, 0};
#pragma unroll
    for (int q = 0; q < 4; ++q) acc = mfma16(sel, p[q], acc);
    RowStat st = {0.f, 0.f, 0.f};
    if (stats) { const f32x4 p = p0 + p1; st = finish_stat(xor1_add(p[0] + p[2]), xor1_add(p[1] + p[3])); }
    return epi(half, acc, st);
  };
  auto phase = [&](const bf16x8 (&a)[8], u32 src, bool stats, int gb, auto&& first, auto&& epi, auto&& put, auto&& pre, auto&& slotA, auto&& slotB, int sbase) {
    PSTAMP(sbase, 0);
    g_wait(fA, src_of(src, 0));
    STAMP(sbase);
    PSTAMP(sbase, 1);
    slice_gemm(0, a, fA, stats, [&]() {
      if (gb == 0) g_load(fB, src_of(src, 1));
      if (SLOT_POS == 0) slotA();
      first();
    });
    PSTAMP(sbase, 4);
    BAR();
    PSTAMP(sbase, 5);
    const u32x2 vA = back(0, stats, epi, [&]() { if (gb == 1) g_load(fB, src_of(src, 1)); });
    PSTAMP(sbase, 6);
    if (gb == 2) g_load(fB, src_of(src, 1));
    __builtin_amdgcn_sched_barrier(0);
    put(0, vA);
    if (SLOT_POS == 1) slotA();
    __builtin_amdgcn_sched_barrier(0);
    PSTAMP(sbase, 7);
    g_wait(fB, src_of(src, 1));
    STAMP(sbase + 1);
    PSTAMP(sbase, 8);
    slice_gemm(1, a, fB, stats, [&]() { if (SLOT_POS == 0) slotB(); });
    PSTAMP(sbase, 11);
    BAR();
    PSTAMP(sbase, 12);
    const u32x2 vB = back(1, stats, epi, [&]() {
      pre();
    });
    PSTAMP(sbase, 13);
    PSTAMP(sbase, 14);
    __builtin_amdgcn_sched_barrier(0);
    put(1, vB);
    if (SLOT_POS == 1) slotB();
    __builtin_amdgcn_sched_barrier(0);
    PSTAMP(sbase, 15);
  };
  auto prio_late = [&](int p) {
#define PRIO_ASM(P) asm volatile("s_cmp_lt_u32 %0, 4\n\ts_cbranch_scc1 .Lprio%=\n\ts_setprio " #P "\n.Lprio%=:" :: "s"(w) : "scc")
    if (p == 0) PRIO_ASM(0); else if (p == 1) PRIO_ASM(1); else PRIO_ASM(2);
#undef PRIO_ASM
  };
  const u32 a4off = (2 * j * 112 + 14 * w) * 1024 + l16;
  {
    float c[2] = {0.f, 0.f};
#pragma unroll
    for (int t = 0; t < 8; ++t) {
      const u32x4 d = asu(a_1[t]);
#pragma unroll
      for (int e = 0; e < 4; ++e) c[t >> 2] = dot2(d[e], 0x3F803F80u, c[t >> 2]);
    }
    const float c0 = swap32_add(c[0]), c1 = swap32_add(c[1]);
    ldsw<float>(L_CS1P + w * 256 + lane * 4, lane < 32 ? c0 : c1);
  }
  cs3_acc(0, A3_N0);
  __builtin_amdgcn_sched_barrier(0);
  if (O_SLEEP) __builtin_amdgcn_s_sleep(O_SLEEP);
  g_load(fA, src_of(WS_OX, 0));
  g_load(fB, src_of(WS_OX, 1));
  __builtin_amdgcn_sched_barrier(0);
  phase(a_o, WS_OX, false, -1,
        [&]() { BAR(); },
        [&](int half, f32x4 acc, RowStat) {
          if (half == 1) {
            cs1 = (f32x4){0.f, 0.f, 0.f, 0.f};
#pragma unroll
            for (int v = 0; v < 8; ++v) cs1 += ldsr<f32x4>(L_CS1P + v * 256 + w * 32 + (lane & 1) * 16);
            cs1 = opaque(cs1);
          }
          hres[half] = acc + (f32x4){bflo(xres[half][0]), bfhi(xres[half][0]), bflo(xres[half][1]), bfhi(xres[half][1])};
          return pack4(hres[half]);
        },
        [&](int half, u32x2 v) { put_at(hvo, hx_so(WS_HX, half), v); },
        [&]() { g_load(fA, src_of(WS_HX, 0)); },
        [&]() {
          if (W4_WARM == 2) {
#pragma unroll
            for (int i = 0; i < 7; ++i) dma1(ws + WS_W4 + i * 1024, (((u32)(b >> 6) & 3u) * 8 + w) * 7168 + j * 229376 + l16, lb + L_DUM1);
          }
          a3_slot(0);
        },
        [&]() { a3_slot(1); }, 10);
  cs3_acc(A3_N0, A3_N0 + (32 - A3_N0) / 6);
  __builtin_amdgcn_sched_barrier(0);
  STAMP(12);
  const u32 tso[2] = {opaque<u32>(ts_vo(0)), opaque<u32>(ts_vo(1))};
  phase(a_1, WS_HX, true, GB_POS, [&]() { },
        [&](int half, f32x4 acc, RowStat st) {
          const float r2 = __builtin_amdgcn_rsqf(st.var * st.rstd * st.rstd + LN_EPS), rr = st.rstd * r2;
          const f32x4 pre = (acc - st.mu * cs1) * rr;
          const f32x2 g0 = gelu2((f32x2){pre[0], pre[1]}), g1 = gelu2((f32x2){pre[2], pre[3]});
          xp[half] = (hres[half] - st.mu) * st.rstd;
          return (u32x2){canon(cvtpk(g0[0], g0[1])), canon(cvtpk(g1[0], g1[1]))};
        },
        [&](int half, u32x2 v) { put_at(tso[half], hx_so(WS_TS, half) - 32768, v); },
        [&]() { g_load(fA, src_of(WS_TS, 0)); },
        [&]() { a3_slot(2); },
        [&]() { a3_slot(3); dma4_sc1(ws + WS_W4 + 10 * 1024, a4off, lb + L_A4 + w * 8192); }, 13);
  cs3_acc(A3_N0 + (32 - A3_N0) / 6, A3_N0 + 3 * (32 - A3_N0) / 6);
  __builtin_amdgcn_sched_barrier(0);
  STAMP(15);
  phase(a_2, WS_TS, false, GB_POS, [&]() { },
        [&](int half, f32x4 acc, RowStat) {
          const f32x4 x1 = xp[half] + acc;
          ldsw<f32x4>(L_RES + half * 8192 + w * 1024 + l16, hres[half] + x1);
          return pack4(x1);
        },
        [&](int half, u32x2 v) { put_at(hvo, hx_so(WS_X1, half), v); },
        [&]() { dma4_sc1(ws + src_of(WS_X1, 0), gvo, lb + L_P + w * 4096); },
        [&]() { a3_slot(4); dma4_sc1(ws + WS_W4 + 114688 + 10 * 1024, a4off, lb + L_A4 + w * 8192 + 4096); },
        [&]() { a3_slot(5); }, 16);
  cs3_acc(A3_N0 + 3 * (32 - A3_N0) / 6, 32);
  {
    const float c = swap32_add((cs3p[0] + cs3p[1]) + (cs3p[2] + cs3p[3]));
    ldsw<float>(L_CS + w * 128 + r32 * 4, c);
  }
  __builtin_amdgcn_sched_barrier(0);
  STAMP(18);
  bf16x8 a4[10][2];
  auto a4_load = [&](int n) { a4[n >> 1][n & 1] = asbf(ld16(wsr, a4off, WS_W4 + (n & 1) * 114688 + (n >> 1) * 1024)); };
  constexpr int ord[14] = {0, 2, 4, 6, 1, 3, 5, 7, 8, 10, 12, 9, 11, 13};
  u32x4 uf[28];
  auto u_load1 = [&](int k) { uf[k] = ld16(wsr, l16, WS_UX + (blk_of(k / 14) * 112 + w * 14 + ord[k % 14]) * 1024); };
  {
    auto x1_finish = [&](int half) {
#pragma unroll
      for (int t = 0; t < 4; ++t) fA[t] = ldsr<bf16x8>(pr + half * 32768 + t * 1024);
      if (__builtin_expect(unwritten(fA), 0)) {
        do {
          if (timed_out(t0)) break;
          __builtin_amdgcn_s_sleep(1);
          dma4_sc1(ws + src_of(WS_X1, half), gvo, lb + L_P + half * 32768 + w * 4096);
          WAITV(0);
#pragma unroll
          for (int t = 0; t < 4; ++t) fA[t] = ldsr<bf16x8>(pr + half * 32768 + t * 1024);
        } while (unwritten(fA));
      }
      row_stats(half, fA);
    };
    struct UOut { float nmu, k1, k2, ir; f32x4 cs[2]; f32x4 p[4]; u32 up[8]; u32 cb, vo; };
    auto u_stat0 = [&](UOut& u, int half) {
      const u32 l = opaque<u32>(lane), sr = opaque<u32>(L_STAT + half * STH + (l & 31) * STP);
#pragma unroll
      for (int i = 0; i < 4; ++i) u.p[i] = ldsr<f32x4>(sr + 16 * i);
    };
    auto u_stat1 = [&](UOut& u) {
      const f32x4 p = (u.p[0] + u.p[1]) + (u.p[2] + u.p[3]);
      const RowStat st = finish_stat(p[0] + p[2], p[1] + p[3]);
      const float c1 = -2.0f * 0.7978845608028654f * 1.4426950408889634f, c2 = c1 * 0.044715f;
      u.nmu = opaque(-st.mu); u.k1 = opaque(c1 * st.rstd); u.k2 = opaque(c2 * st.rstd * st.rstd * st.rstd); u.ir = opaque(__builtin_amdgcn_sqrtf(st.var + LN_EPS));
    };
    auto u_cs = [&](UOut& u, int a) {
      if (a == 0) {
        const u32 l = opaque<u32>(lane);
        u.cb = opaque<u32>(L_CS + w * 128 + (l >> 5) * 16);
        u.vo = opaque<u32>(w < 7 ? l * 16 : OOB);
      }
      u.cs[a & 1] = ldsr<f32x4>(u.cb + a * 32);
    };
    auto u_store = [&](UOut& u, int half, int p) {
      if (p == 3 || p == 7) {
        const u32 so = WS_UX + (blk_of(half) * 112 + j * 14 + 2 * w) * 1024;
        if (p == 3) st16(wsr, u.vo, so, (u32x4){u.up[0], u.up[1], u.up[2], u.up[3]});
        else st16(wsr, u.vo, so + 1024, (u32x4){u.up[4], u.up[5], u.up[6], u.up[7]});
      }
    };
    auto u_quad = [&](UOut& u, int half, const f32x16& acc, int p) {
      float t[4], e[4];
#pragma unroll
      for (int i = 0; i < 4; ++i) { const int v = 2 * p + i; t[i] = opaque(__builtin_fmaf(u.nmu, u.cs[(v >> 2) & 1][v & 3], acc[v])); }
#pragma unroll
      for (int i = 0; i < 4; ++i) e[i] = __builtin_amdgcn_exp2f(t[i] * __builtin_fmaf(t[i] * t[i], u.k2, u.k1));
#pragma unroll
      for (int i = 0; i < 4; ++i) t[i] = opaque(t[i] * __builtin_amdgcn_rcpf(__builtin_fmaf(e[i], u.ir, u.ir)));
      u.up[p] = canon(cvtpk(t[0], t[1])); u.up[p + 1] = canon(cvtpk(t[2], t[3]));
      u_store(u, half, p + 1);
    };
    auto u_pair = [&](UOut& u, int half, const f32x16& acc, int p) {
      const int a = p >> 1, e = 2 * (p & 1);
      const f32x2 t = (f32x2){u.cs[a & 1][e], u.cs[a & 1][e + 1]} * u.nmu + (f32x2){acc[2 * p], acc[2 * p + 1]};
      const f32x2 ar = t * ((t * t) * u.k2 + u.k1);
      const f32x2 d = (f32x2){__builtin_amdgcn_exp2f(ar[0]), __builtin_amdgcn_exp2f(ar[1])} * u.ir + u.ir;
      const f32x2 g = t * (f32x2){__builtin_amdgcn_rcpf(d[0]), __builtin_amdgcn_rcpf(d[1])};
      u.up[p] = canon(cvtpk(g[0], g[1]));
    };
    WAITV(1);
    x1_finish(0);
    BAR();
    STAMP(19);
    dma4_sc1(ws + src_of(WS_X1, 1), gvo, lb + L_P + 32768 + w * 4096);
    const u32 xb = L_P + (w < 7 ? l16 : 0u);
    f32x16 accA = {}, accB = {};
    bf16x8 bq[4];
    UOut uA, uB;
    if (M_PRIO) __builtin_amdgcn_s_setprio(1);
    {
#pragma unroll
      for (int t = 0; t < 4; ++t) bq[t] = ldsr<bf16x8>(xb + t * 1024);
#pragma unroll
      for (int t = 0; t < 32; ++t) {
        accA = mfma32(a3[t], bq[t & 3], accA);
        if (t + 4 < 32) bq[t & 3] = ldsr<bf16x8>(xb + (t + 4) * 1024);
        if (t == 0) u_stat0(uA, 0);
        if (t == 3) u_stat1(uA);
        if (t == 29) u_cs(uA, 0);
        if (t >= 4 && (t & 3) == 0 && t / 4 - 1 < A4_N0) a4_load(t / 4 - 1);
        __builtin_amdgcn_sched_barrier(0);
      }
    }
    if (M_PRIO) __builtin_amdgcn_s_setprio(0);
    STAMP(20);
    auto gelu_half = [&](UOut& u, int half, const f32x16& acc) {
#pragma unroll
      for (int p = 0; p < 8; p += 2) {
        if (p + 2 < 8) u_cs(u, (p + 2) >> 1);
        constexpr int nr = (8 - UE_P) / 2;
        if (half == 1) {
#pragma unroll
          for (int n = 20 - A4_LATE + p / 2 * A4_LATE / 4; n < 20 - A4_LATE + (p / 2 + 1) * A4_LATE / 4; ++n) a4_load(n);
        }
        if (half == 1 && p >= UE_P) {
#pragma unroll
          for (int i = (p - UE_P) / 2 * U_INIT / (nr ? nr : 1); i < ((p - UE_P) / 2 + 1) * U_INIT / (nr ? nr : 1); ++i) u_load1(i);
        }
        if (GB_PK) { u_pair(u, half, acc, p); u_pair(u, half, acc, p + 1); u_store(u, half, p + 1); }
        else u_quad(u, half, acc, p);
        __builtin_amdgcn_sched_barrier(0);
      }
      if (half == 1 && UE_P >= 8) {
#pragma unroll
        for (int i = 0; i < U_INIT; ++i) u_load1(i);
      }
    };
    if (PQ == 0 || w < 4) gelu_half(uA, 0, accA);
    asm volatile("s_waitcnt vmcnt(%0)" :: "n"(A4_N0 + (PQ ? 0 : 2)) : "memory");
    x1_finish(1);
    BAR();
    STAMP(21);
    if (!M_PRIO) prio_late(Q_PRIO);
    if (PQ && w >= 4) gelu_half(uA, 0, accA);
    if (M_PRIO) __builtin_amdgcn_s_setprio(1);
    {
#pragma unroll
      for (int t = 0; t < 4; ++t) bq[t] = ldsr<bf16x8>(xb + 32768 + t * 1024);
#pragma unroll
      for (int t = 0; t < 32; ++t) {
        if (W3B_TOG && (t % (W3B_TOG ? W3B_TOG : 1)) == 0) prio_late(((t / (W3B_TOG ? W3B_TOG : 1)) & 1) ? 0 : 1);
        accB = mfma32(a3[t], bq[t & 3], accB);
        if (t + 4 < 32) bq[t & 3] = ldsr<bf16x8>(xb + 32768 + (t + 4) * 1024);
        if (t == 0) u_stat0(uB, 1);
        if (t == 3) u_stat1(uB);
        if (t == 29) u_cs(uB, 0);
        if (A4_SPREAD == 0) { const int n = A4_N0 + t - 1; if (t >= 1 && n < 20 - A4_LATE) a4_load(n); }
        else if (A4_SPREAD == 1) { const int n = A4_N0 + (t - 1) / 2; if ((t & 1) && n < 20 - A4_LATE) a4_load(n); }
        else { const int n = A4_N0 + (t - 1) / 4; if ((t & 3) == 1 && n < 20 - A4_LATE) a4_load(n); }
        __builtin_amdgcn_sched_barrier(0);
      }
    }
    if (M_PRIO) __builtin_amdgcn_s_setprio(0); else prio_late(GB_PRIO);
    if (!MID_BAR && lane == 0) asm volatile("ds_add_u32 %0, %1" :: "v"(lb + L_CNT + 8), "v"(1u) : "memory");
    STAMP(22);
    gelu_half(uB, 1, accB);
    STAMP(23);
  }
  {
    if (M_PRIO) { __builtin_amdgcn_s_setprio(1); if (W4_PRIO) prio_late(2); } else prio_late(W4_PRIO);
#pragma unroll
    for (int half = 0; half < 2; ++half) {
      const u32 sa = L_A4 + w * 8192 + opaque<u32>(l16);
      f32x16 acc0 = {}, acc1 = {};
      bf16x8 sq[4][2];
      auto step = [&](int s_, u32x4 u) {
        if (s_ + 2 < 14 && ord[s_ + 2] >= 10) { const int k = ord[s_ + 2] - 10; sq[k][0] = ldsr<bf16x8>(sa + k * 1024); sq[k][1] = ldsr<bf16x8>(sa + 4096 + k * 1024); }
        const int t = ord[s_];
        acc0 = mfma32(t < 10 ? a4[t < 10 ? t : 0][0] : sq[t < 10 ? 0 : t - 10][0], asbf(u), acc0);
        acc1 = mfma32(t < 10 ? a4[t < 10 ? t : 0][1] : sq[t < 10 ? 0 : t - 10][1], asbf(u), acc1);
      };
      u32 m = 0;
#pragma unroll
      for (int s_ = 0; s_ < 14; ++s_) {
        const int k = 14 * half + s_;
        step(s_, uf[k]);
        m = opaque(umax(umax(umax(m, uf[k][0]), uf[k][1]), umax(uf[k][2], uf[k][3])));
        if (k + U_WIN < 28 && k + U_WIN >= U_INIT) u_load1(k + U_WIN);
        __builtin_amdgcn_sched_barrier(0);
      }
      if (__builtin_expect(__any(m == SENT), 0)) {
        u32x4 ug[14];
        bool bad;
        do {
          __builtin_amdgcn_s_sleep(1);
          asm volatile("s_nop 7" ::: "memory");
          const u32 so = WS_UX + (blk_of(half) * 112 + w * 14) * 1024, l = opaque<u32>(l16);
#pragma unroll
          for (int i = 0; i < 14; ++i) regather1(srd, l, so + ord[i] * 1024, ug[i]);
          asm volatile("s_waitcnt vmcnt(0)" : "+v"(ug[0]), "+v"(ug[1]), "+v"(ug[2]), "+v"(ug[3]), "+v"(ug[4]), "+v"(ug[5]), "+v"(ug[6]), "+v"(ug[7]), "+v"(ug[8]), "+v"(ug[9]),
                       "+v"(ug[10]), "+v"(ug[11]), "+v"(ug[12]), "+v"(ug[13]) :: "memory");
          u32 mm = 0;
#pragma unroll
          for (int i = 0; i < 14; ++i) mm = umax(mm, max4(ug[i]));
          bad = __any(mm == SENT);
        } while (bad && !timed_out(t0));
        acc0 = (f32x16){}; acc1 = (f32x16){};
#pragma unroll
        for (int s_ = 0; s_ < 14; ++s_) step(s_, ug[s_]);
      }
      STAMP(24 + half);
      if (!MID_BAR && half == 1) {
        while (__builtin_amdgcn_readfirstlane(*(volatile LAS u32*)((LAS char*)smem + L_CNT + 8)) != 8) {
          if (timed_out(t0)) break;
          __builtin_amdgcn_s_sleep(1);
        }
        FENCE();
      }
      partials(half, acc0, acc1, pw_of(opaque<u32>(lane)));
      if (MID_BAR ? half == 0 || !OUT_EARLY : half == 1) make_sel();
      if (half == 1) STAMP(26);
      if (half == 1 || (MID_BAR && OUT_EARLY)) BAR();
      auto out_half = [&](int hf) {
        const u32 l = opaque<u32>(lane);
        const f32x4 v = reduce_at(hf, L_P + w * 4096 + l * 16) + ldsr<f32x4>(L_RES + hf * 8192 + w * 1024 + l * 16);
        *(GAS u32x2*)((char*)out + (u32)((blk_of(hf) * 32 + (l >> 1)) * 1024 + (64 * j + 8 * w + 4 * (l & 1)) * 2)) = (u32x2){cvtpk(v[0], v[1]), cvtpk(v[2], v[3])};
      };
      if (MID_BAR && OUT_EARLY) out_half(half);
      else if (half == 1) { out_half(0); out_half(1); }
      else if (MID_BAR) BAR();
      FENCE();
    }
  }
  STAMP(27);
}

extern "C" void kernel_launch(void* const* d_in, const int* in_sizes, int n_in, void* d_out, int out_size, void* d_ws, size_t ws_size, hipStream_t stream) {
  (void)in_sizes; (void)n_in; (void)out_size; (void)ws_size;
  fused_block_kernel<<<256, 512, 0, stream>>>((const u16*)d_in[0], (const u16*)d_in[1], (const u16*)d_in[2], (const u16*)d_in[3], (const u16*)d_in[4],
                                              (const u16*)d_in[6], (const u16*)d_in[7], (const u16*)d_in[8], (const u16*)d_in[9], (u16*)d_out, (char*)d_ws);
}
```

```cpp
#include <hip/hip_runtime.h>
#include <stdint.h>
#include <type_traits>

typedef unsigned short u16;
typedef unsigned int u32;
typedef unsigned long long u64;
typedef short bf16x8 __attribute__((ext_vector_type(8)));
typedef short s16x4 __attribute__((ext_vector_type(4)));
typedef float f32x2 __attribute__((ext_vector_type(2)));
typedef float f32x4 __attribute__((ext_vector_type(4)));
typedef float f32x16 __attribute__((ext_vector_type(16)));
typedef u32 u32x2 __attribute__((ext_vector_type(2)));
typedef u32 u32x4 __attribute__((ext_vector_type(4)));
typedef __bf16 bf16x2_t __attribute__((ext_vector_type(2)));
#define LAS __attribute__((address_space(3)))
#define GAS __attribute__((address_space(1)))
#define DI __device__ __forceinline__

constexpr u32 WS_EPOCH = 0x0;
constexpr u32 WS_FKV = 0x1000;
constexpr u32 WS_FW = 0x5000;
constexpr u32 WS_FM = 0x9000;
constexpr u32 WS_K = 0x10000;
constexpr u32 WS_V = 0x20000;
constexpr u32 WS_DBG = 0x1E00000;
constexpr u32 WS_WO = 0x100000, WS_W1 = 0x180000, WS_W2 = 0x200000, WS_W3 = 0x280000, WS_W4 = 0x440000;
constexpr u32 WS_OX = 0x600000, WS_HX = 0x800000, WS_TS = 0xA00000, WS_X1 = 0xC00000, WS_UX = 0xE00000;
constexpr u32 WS_STAMP = 0x2000000;
constexpr u32 WS_END = 0x2100000;
constexpr u32 OOB = 0x80000000u;

constexpr u32 XP = 1040;
constexpr u32 L_XIMG = 0;
constexpr u32 L_WQ = 66560;
constexpr u32 L_WKV = 132096;
constexpr u32 S_PM = 148480, S_PC = 156672, S_PS = 157184, S_PQ = 157696, S_FIN = 158208, S_QS = 158336, S_QC = 159360;
constexpr u32 L_DUM1 = 160384;
constexpr u32 L_CNT = 161408;
constexpr u32 L_K = 0, L_V = 65536, L_ONES = 151552;
constexpr u32 L_STG = 131072;
constexpr u32 L_CS1P = 155904;
constexpr u32 L_P = 0;
constexpr u32 L_STAT = 65536;
constexpr u32 STP = 80, STH = 2560;
constexpr u32 L_CS = 71680;
constexpr u32 L_RES = 73728;
constexpr u32 L_A4 = 90112;
constexpr u32 L_STAMP = 161792;
constexpr u32 LDS_BYTES = 163840;

__shared__ __attribute__((aligned(16))) char smem[LDS_BYTES];

constexpr u32 SENT = 0xFFFFFFFFu;
constexpr float LN_EPS = 1e-5f;
constexpr float QSCALE = 0.25f * 1.4426950408889634f;
#define CODE_KB 45

DI f32x16 mfma32(bf16x8 a, bf16x8 b, f32x16 c) { return __builtin_amdgcn_mfma_f32_32x32x16_bf16(a, b, c, 0, 0, 0); }
DI f32x4 mfma16(bf16x8 a, bf16x8 b, f32x4 c) { return __builtin_amdgcn_mfma_f32_16x16x32_bf16(a, b, c, 0, 0, 0); }
DI u32 cvtpk(float lo, float hi) { f32x2 v = {lo, hi}; bf16x2_t b = __builtin_convertvector(v, bf16x2_t); return __builtin_bit_cast(u32, b); }
DI u32 canon(u32 d) { return d < 0xFFFFFFFEu ? d : 0xFFFFFFFEu; }
DI float bflo(u32 d) { return __uint_as_float(d << 16); }
DI float bfhi(u32 d) { return __uint_as_float(d & 0xFFFF0000u); }
DI float dot2(u32 a, u32 b, float c) { return __builtin_amdgcn_fdot2_f32_bf16(__builtin_bit_cast(bf16x2_t, a), __builtin_bit_cast(bf16x2_t, b), c, false); }
DI float gelu(float x) {
  const float c1 = -2.0f * 0.7978845608028654f * 1.4426950408889634f, c2 = c1 * 0.044715f;
  const float e = __builtin_amdgcn_exp2f(x * (c1 + c2 * x * x));
  return x * __builtin_amdgcn_rcpf(1.0f + e);
}
DI f32x2 gelu2(f32x2 x) {
  const float c1 = -2.0f * 0.7978845608028654f * 1.4426950408889634f, c2 = c1 * 0.044715f;
  const f32x2 a = x * ((x * x) * c2 + c1);
  const f32x2 d = (f32x2){__builtin_amdgcn_exp2f(a[0]), __builtin_amdgcn_exp2f(a[1])} + 1.0f;
  return x * (f32x2){__builtin_amdgcn_rcpf(d[0]), __builtin_amdgcn_rcpf(d[1])};
}
DI float swap32_add(float v) {
  const auto r = __builtin_amdgcn_permlane32_swap(__float_as_uint(v), __float_as_uint(v), false, false);
  return __uint_as_float(r[0]) + __uint_as_float(r[1]);
}
DI float xor1_add(float v) { return v + __int_as_float(__builtin_amdgcn_mov_dpp(__float_as_int(v), 0xB1, 0xF, 0xF, true)); }

#define BAR() asm volatile("s_waitcnt lgkmcnt(0)\n\ts_barrier" ::: "memory")
#define WAITV(n) asm volatile("s_waitcnt vmcnt(" #n ")" ::: "memory")
#define FENCE() asm volatile("" ::: "memory")
template <class T> DI T opaque(T v) { asm volatile("" : "+v"(v)); return v; }

template <class T> DI T ldsr(u32 off) { return *(const LAS T*)((LAS char*)smem + off); }
template <class T> DI void ldsw(u32 off, T v) { *(LAS T*)((LAS char*)smem + off) = v; }
DI s16x4 ldstr(u32 off) {
  typedef short v4i16_t __attribute__((ext_vector_type(4)));
  return __builtin_bit_cast(s16x4, __builtin_amdgcn_ds_read_tr16_b64_v4i16((LAS v4i16_t*)((LAS char*)smem + off)));
}
DI bf16x8 cat(s16x4 a, s16x4 b) { return __builtin_shufflevector(a, b, 0, 1, 2, 3, 4, 5, 6, 7); }
DI u32 lds_base() { return (u32)(uintptr_t)(LAS char*)smem; }

#define DMA_HEAD "s_mov_b32 %[k], m0\n\ts_mov_b32 m0, %[lds]\n\ts_nop 0\n\t"
#define DMA_TAIL "s_mov_b32 m0, %[k]"
DI void dma1(const void* sbase, u32 voff, u32 ldsdst) {
  u32 keep;
  asm volatile(DMA_HEAD "global_load_lds_dwordx4 %[v], %[s]\n\t" DMA_TAIL : [k] "=&s"(keep) : [v] "v"(voff), [s] "s"(sbase), [lds] "s"(ldsdst) : "memory");
}
DI void dma2(const void* sbase, u32 voff, u32 ldsdst) {
  u32 keep;
  asm volatile(DMA_HEAD "global_load_lds_dwordx4 %[v], %[s]\n\tglobal_load_lds_dwordx4 %[v], %[s] offset:1024\n\t" DMA_TAIL
               : [k] "=&s"(keep) : [v] "v"(voff), [s] "s"(sbase), [lds] "s"(ldsdst) : "memory");
}
DI void dma4_rows(const void* sbase, u32 v0, u32 v1, u32 v2, u32 v3, u32 ldsdst) {
  u32 keep;
  asm volatile(DMA_HEAD "global_load_lds_dwordx4 %[a], %[s]\n\tglobal_load_lds_dwordx4 %[b], %[s] offset:1040\n\t"
               "global_load_lds_dwordx4 %[c], %[s] offset:2080\n\tglobal_load_lds_dwordx4 %[d], %[s] offset:3120\n\t" DMA_TAIL
               : [k] "=&s"(keep) : [a] "v"(v0), [b] "v"(v1), [c] "v"(v2), [d] "v"(v3), [s] "s"(sbase), [lds] "s"(ldsdst) : "memory");
}
DI void dma4_sc1(const void* sbase, u32 voff, u32 ldsdst) {
  u32 keep;
  asm volatile(DMA_HEAD "global_load_lds_dwordx4 %[v], %[s] sc1\n\tglobal_load_lds_dwordx4 %[v], %[s] offset:1024 sc1\n\t"
               "global_load_lds_dwordx4 %[v], %[s] offset:2048 sc1\n\tglobal_load_lds_dwordx4 %[v], %[s] offset:3072 sc1\n\t" DMA_TAIL
               : [k] "=&s"(keep) : [v] "v"(voff), [s] "s"(sbase), [lds] "s"(ldsdst) : "memory");
}
DI void dma_pair_late(int w, const void* sbase, u32 voff, u32 ldsdst) {
  u32 keep;
  asm volatile("s_cmp_lt_u32 %[w], 4\n\ts_cbranch_scc1 .Lkv%=\n\t" DMA_HEAD "global_load_lds_dwordx4 %[a], %[s] sc1\n\ts_add_u32 m0, m0, 0x10000\n\ts_nop 0\n\t"
               "global_load_lds_dwordx4 %[b], %[s] sc1\n\t" DMA_TAIL "\n.Lkv%=:"
               : [k] "=&s"(keep) : [w] "s"(w), [a] "v"(voff), [b] "v"(voff + 0x10000u), [s] "s"(sbase), [lds] "s"(ldsdst) : "memory", "scc");
}
template <int N> DI void count_late(int w, u32 addr) {
  u64 keep;
  asm volatile("s_cmp_lt_u32 %[w], 4\n\ts_cbranch_scc1 .Lcl%=\n\ts_waitcnt vmcnt(%[n])\n\ts_mov_b64 %[k], exec\n\ts_mov_b64 exec, 1\n\tds_add_u32 %[a], %[o]\n\ts_mov_b64 exec, %[k]\n.Lcl%=:"
               : [k] "=&s"(keep) : [w] "s"(w), [n] "n"(N), [a] "v"(addr), [o] "v"(1u) : "memory", "scc");
}
DI void st16_early(int w, u32x4 srd, u32 voff, u32 soff, u32x4 v) {
  asm volatile("s_cmp_ge_u32 %[w], 4\n\ts_cbranch_scc1 .Lse%=\n\tbuffer_store_dwordx4 %[d], %[v], %[r], %[s] offen sc1\n\ts_nop 1\n.Lse%=:"
               :: [w] "s"(w), [d] "v"(v), [v] "v"(voff), [r] "s"(srd), [s] "s"(soff) : "memory", "scc");
}
#define DMA_STEP(SC) "global_load_lds_dwordx4 %[v], %[s]" SC "\n\ts_add_u32 m0, m0, %[ls]\n\tv_add_u32 %[v], %[gs], %[v]\n\t"
template <int N, bool SC1, u32 GS>
DI void dma_strided(const void* sbase, u32 voff, u32 ldsdst, u32 lstep) {
  u32 keep;
  static_assert(N == 2 || N == 3 || N == 4 || N == 8, "");
#define DMA_RUN(BODY) asm volatile(DMA_HEAD BODY DMA_TAIL : [k] "=&s"(keep), [v] "+v"(voff) : [s] "s"(sbase), [lds] "s"(ldsdst), [ls] "s"(lstep), [gs] "n"(GS) : "memory", "scc")
  if constexpr (SC1) {
    if constexpr (N == 2) DMA_RUN(DMA_STEP(" sc1") DMA_STEP(" sc1"));
    if constexpr (N == 3) DMA_RUN(DMA_STEP(" sc1") DMA_STEP(" sc1") DMA_STEP(" sc1"));
    if constexpr (N == 4) DMA_RUN(DMA_STEP(" sc1") DMA_STEP(" sc1") DMA_STEP(" sc1") DMA_STEP(" sc1"));
  } else {
    if constexpr (N == 8) DMA_RUN(DMA_STEP("") DMA_STEP("") DMA_STEP("") DMA_STEP("") DMA_STEP("") DMA_STEP("") DMA_STEP("") DMA_STEP(""));
  }
#undef DMA_RUN
}

typedef __amdgpu_buffer_rsrc_t rsrc_t;
DI rsrc_t mkrsrc(const void* p, u32 bytes) { return __builtin_amdgcn_make_buffer_rsrc((void*)p, 0, bytes, 0x00020000); }
DI u32x4 ld16(rsrc_t r, u32 voff, u32 soff) { return __builtin_amdgcn_raw_buffer_load_b128(r, voff, soff, 16); }
DI void st16(rsrc_t r, u32 voff, u32 soff, u32x4 v) {
  __builtin_amdgcn_raw_buffer_store_b128(v, r, voff, soff, 16);
  asm volatile("s_nop 1" :: "v"(v) : "memory");
}
DI void st8(rsrc_t r, u32 voff, u32 soff, u32x2 v) { __builtin_amdgcn_raw_buffer_store_b64(v, r, voff, soff, 16); }
DI void st4(rsrc_t r, u32 voff, u32 soff, u32 v) { __builtin_amdgcn_raw_buffer_store_b32(v, r, voff, soff, 16); }
DI bf16x8 asbf(u32x4 v) { return __builtin_bit_cast(bf16x8, v); }
DI u32x4 asu(bf16x8 v) { return __builtin_bit_cast(u32x4, v); }
DI void regather4(u32x4 srd, u32 voff, u32 soff, bf16x8& f0, bf16x8& f1, bf16x8& f2, bf16x8& f3) {
  asm volatile("s_sleep 1\n\tbuffer_load_dwordx4 %0, %4, %5, %6 offen sc1\n\tbuffer_load_dwordx4 %1, %4, %5, %6 offen offset:1024 sc1\n\t"
               "buffer_load_dwordx4 %2, %4, %5, %6 offen offset:2048 sc1\n\tbuffer_load_dwordx4 %3, %4, %5, %6 offen offset:3072 sc1\n\ts_waitcnt vmcnt(0)"
               : "+v"(f0), "+v"(f1), "+v"(f2), "+v"(f3) : "v"(voff), "s"(srd), "s"(soff) : "memory");
}
DI void regather1(u32x4 srd, u32 voff, u32 soff, u32x4& f0) {
  asm volatile("buffer_load_dwordx4 %0, %1, %2, %3 offen sc1" : "=&v"(f0) : "v"(voff), "s"(srd), "s"(soff) : "memory");
}
DI u32 umax(u32 a, u32 b) { return a > b ? a : b; }
DI u32 max4(u32x4 v) { return umax(umax(v[0], v[1]), umax(v[2], v[3])); }

constexpr u64 SPIN_TICKS = 400000ull;
DI bool timed_out(u64 t0) { return __builtin_amdgcn_s_memrealtime() - t0 > SPIN_TICKS; }

#define STAMP(i) do { } while (0)
#define ASTAMP(it, pos) do { } while (0)
#define PSTAMP(sb, i) do { } while (0)
#define MFMA_GAP16(v) asm volatile("s_nop 7" : "+v"(v))
#define MFMA_GAP32(v) asm volatile("s_nop 7\n\ts_nop 3" : "+v"(v))

DI bool flags_ok(u32x4 a, u32x4 b, u32 lo, u32 hi) {
  return __all(a[0] == lo && a[1] == hi && a[2] == lo && a[3] == hi && b[0] == lo && b[1] == hi && b[2] == lo && b[3] == hi);
}
DI void wait_flags(rsrc_t wsr, u32 base, u32 taglo, u32 taghi, int lane, u64 t0) {
  for (;;) {
    const u32x4 a = ld16(wsr, lane * 32, base), b = ld16(wsr, lane * 32 + 16, base);
    if (flags_ok(a, b, taglo, taghi)) break;
    if (timed_out(t0)) break;
    __builtin_amdgcn_s_sleep(1);
    FENCE();
  }
}

DI bf16x8 ones_frag() { return asbf((u32x4){0x3F803F80u, 0x3F803F80u, 0x3F803F80u, 0x3F803F80u}); }
DI float diag_pick(f32x4 g, int lane) { const int r = lane & 3; float a = (r & 1) ? g[1] : g[0], b = (r & 1) ? g[3] : g[2]; return (r & 2) ? b : a; }

struct RowStat { float mu, rstd, var; };
DI RowStat finish_stat(float s, float q) {
  RowStat r; r.mu = s * (1.0f / 512.0f);
  r.var = fmaxf(q * (1.0f / 512.0f) - r.mu * r.mu, 0.0f);
  r.rstd = __builtin_amdgcn_rsqf(r.var + LN_EPS);
  return r;
}
DI bool unwritten(const bf16x8 (&f)[4]) {
  return __any(umax(umax(max4(asu(f[0])), max4(asu(f[1]))), umax(max4(asu(f[2])), max4(asu(f[3])))) == SENT);
}

template <int I, int N, class F> DI void static_for(F&& f) {
  if constexpr (I < N) { f(std::integral_constant<int, I>{}); static_for<I + 1, N>(f); }
}
struct Job { const char* src; u32 prow; u32 f; bool perm; };
template <int I> DI Job retile_job(int b, int w, const u16* wo, const u16* w1, const u16* w2, const u16* w3, const u16* w4) {
  auto small = [&](const u16* m, u32 g, u32 f0, bool perm) { return Job{(const char*)m + (u32)(16 * (g & 31) * 1024 + (g >> 5) * 64), 1024u, f0 + g, perm}; };
  auto big3 = [&](u32 g) { return Job{(const char*)w3 + (u32)(16 * (g & 31) * 3584 + (g >> 5) * 64), 3584u, 1536 + g, false}; };
  auto big4 = [&](u32 mm) { return Job{(const char*)w4 + (u32)(16 * ((b & 15) * 7 + mm) * 1024 + (b >> 4) * 64), 1024u, 3328 + 7 * b + mm, true}; };
  if constexpr (I == 0) return small(w < 2 ? wo : w1, 2 * b + (w & 1), w < 2 ? 0 : 512, w < 2);
  else if constexpr (I == 1) { const Job s = small(w2, 2 * b + (w & 1), 1024, false), t = big3(7 * b + (w & 1)); return w < 2 ? s : t; }
  else if constexpr (I == 2) return big3(7 * b + 2 + w);
  else if constexpr (I == 3) { const Job s = big3(7 * b + 6), t = big4(w - 1); return w == 0 ? s : t; }
  else return big4(3 + w);
}

#define ATT_PIPE 2
#define PRIO_IT 12
#define KV_SPREAD 2
#define CNT_U 2
#define NOMARK 0
#define MARK_IT 1
#define FLAG_IT 5
#define MARK_P1 0
#define MARK_ST 2
#define FM_IT 10
#define FM_CHK 14
#define KVW 4
#define P1_BAR 0
#define FLAG_POS 0
#define DLY -1
#define DELAY(p) do { if (DLY == (p)) { const u64 d0_ = __builtin_amdgcn_s_memrealtime(); while (__builtin_amdgcn_s_memrealtime() - d0_ < 200) __builtin_amdgcn_s_sleep(1); } } while (0)
#define MEASP 0
#define MEAS(p) do { if (MEASP == (p)) tmeas = __builtin_amdgcn_s_memrealtime(); } while (0)
#define PRE_POS 0
#define PUT16 0
#define GB_POS 1
#define O_SLEEP 16
#define SLOT_POS 0
#define W4_WARM 0
#define FPI 6
#define U_IN 0
#define W4_PRIO Q_PRIO
#define A4_SPREAD 1
#define W3B_TOG 0
#define GB_PRIO Q_PRIO
#define UE_P 0
#define U_WIN 5
#define OUT_EARLY 1
#define MID_BAR 0
#define PQ 1
#define Q_PRIO 1
#ifndef M_PRIO
#define M_PRIO 1
#endif
#define U_INIT U_WIN
#define A4_LATE 0
#define STAT_POS 0
#define GB_PK 0
#define A3_N0 14
static_assert(33 + A3_N0 + (W4_WARM == 1 ? 7 : 0) <= 8 * FPI, "fetch schedule");
#define A4_N0 6

constexpr int marks_before(int it) { const int n = it <= MARK_IT ? 0 : (it - MARK_IT) * (4 / MARK_ST); return n < 15 ? n : 15; }
static_assert(MARK_P1 != 2 || (marks_before(FM_IT - 1) == 15 && FM_CHK > FM_IT && FM_CHK > 8 && FLAG_IT < 8), "mark schedule");

__global__ __launch_bounds__(512, 2) void fused_block_kernel(const u16* __restrict__ x, const u16* __restrict__ wq, const u16* __restrict__ wk,
                                                             const u16* __restrict__ wv, const u16* __restrict__ wo, const u16* __restrict__ w1,
                                                             const u16* __restrict__ w2, const u16* __restrict__ w3, const u16* __restrict__ w4,
                                                             u16* __restrict__ out, char* ws) {
  const u64 tA = MEASP == 0 ? __builtin_amdgcn_s_memrealtime() : 0;
  if (MEASP == 0) asm volatile("" :: "s"(tA));
  const int tid = threadIdx.x, lane = tid & 63, w = __builtin_amdgcn_readfirstlane(tid >> 6);
  const int b = blockIdx.x;
  const int cl = (b & 7) * 4 + (b >> 6), j = (b >> 3) & 7;
  const u32 lb = lds_base();
  const u32 l16 = lane * 16;
  {
    u64 pc;
    asm volatile("s_getpc_b64 %0" : "=s"(pc));
    dma1((const void*)(pc & ~63ull), (u32)((((b >> 3) * 8 + w) % CODE_KB) * 1024) + l16, lb + L_DUM1);
  }
  asm volatile("" :: "s"(x), "s"(wq), "s"(wk), "s"(wv), "s"(wo), "s"(w1), "s"(w2), "s"(w3), "s"(w4), "s"(out), "s"(ws));
  const u64 t0 = __builtin_amdgcn_s_memrealtime();
  u64 tmeas = t0;
  const u64 c0 = MEASP == 99 ? __builtin_amdgcn_s_memtime() : 0;
  const int i16 = lane & 15, g4 = lane >> 4, r32 = lane & 31, h2 = lane >> 5;
  STAMP(0);

  auto blk_of = [&](int half) { return half * 32 + cl; };
  const int dlt = (j >> 1) == 0 ? 1 : (j >> 1) == 1 ? -1 : (j >> 1) == 2 ? 2 : -2;
  const int kvrt = j >> 1, kvmat = j & 1;
  auto grow = [&](int rho) { return (rho < 32 ? 0 : 1024 - 32) + 32 * cl + rho; };
  const int R0 = grow(16 * kvrt);
  u32x4 xk0, xk1;
  u64 tag;
  {
    const u32 xo = (u32)(i16 * 1024 + g4 * 32);
    asm volatile("global_load_dwordx4 %0, %2, %3\n\tglobal_load_dwordx4 %1, %2, %3 offset:16"
                 : "=&v"(xk0), "=&v"(xk1) : "v"(xo), "s"((const char*)x + R0 * 1024 + w * 128) : "memory");
    dma2((const char*)(kvmat ? wv : wk) + w * 2048, l16, lb + L_WKV + w * 2048);
    asm volatile("s_load_dwordx2 %0, %1, 0x0" : "=s"(tag) : "s"(ws + WS_EPOCH) : "memory");
    if (P1_BAR) asm volatile("s_barrier" ::: "memory");
    const char* xr = (const char*)x + grow(8 * w) * 1024 - 48;
    dma4_rows(xr, l16 + 48, l16 + 32, l16 + 16, l16, lb + L_XIMG + 8 * w * XP);
    dma4_rows(xr + 4096, l16 + 48, l16 + 32, l16 + 16, l16, lb + L_XIMG + (8 * w + 4) * XP);
    dma_strided<8, false, 0x2000>((const char*)wq + w * 65536 + j * 128, (u32)((lane >> 3) * 1024 + (((lane & 7) ^ (4 * ((lane >> 4) & 1))) * 16)), lb + L_WQ + w * 8192, 1024);
    asm volatile("s_waitcnt vmcnt(16) lgkmcnt(0)" : "+v"(xk0), "+v"(xk1), "+s"(tag) :: "memory");
  }
  tag += 1ull;
  const u32 taglo = (u32)tag, taghi = (u32)(tag >> 32);
  const rsrc_t wsr = mkrsrc(ws, WS_END);
  const u32x4 srd = {(u32)(uintptr_t)ws, (u32)((uintptr_t)ws >> 32) & 0xFFFFu, WS_END, 0x00020000u};
  const bf16x8 one = ones_frag();
  STAMP(1);
  MEAS(1);
  DELAY(6);

  {
    f32x4 am = {0, 0, 0, 0}, ac = {0, 0, 0, 0}, as = {0, 0, 0, 0}, aq = {0, 0, 0, 0};
    const u32 wa = L_WKV + w * 2048 + (16 * g4 + (i16 >> 2)) * 32 + (i16 & 3) * 8;
    const bf16x8 wf0 = cat(ldstr(wa), ldstr(wa + 128)), wf1 = cat(ldstr(wa + 256), ldstr(wa + 384));
    const bf16x8 xf0 = asbf(xk0), xf1 = asbf(xk1);
    as = mfma16(one, xf0, as); aq = mfma16(xf0, xf0, aq); as = mfma16(one, xf1, as); aq = mfma16(xf1, xf1, aq);
    if (kvmat) {
      am = mfma16(xf0, wf0, am); ac = mfma16(one, wf0, ac); am = mfma16(xf1, wf1, am); ac = mfma16(one, wf1, ac);
      MFMA_GAP16(am); MFMA_GAP16(ac);
      if (lane < 16) ldsw<float>(S_PC + (w * 16 + lane) * 4, ac[0]);
    } else {
      am = mfma16(wf0, xf0, am); ac = mfma16(wf0, one, ac); am = mfma16(wf1, xf1, am); ac = mfma16(wf1, one, ac);
      MFMA_GAP16(am); MFMA_GAP16(ac);
      if (i16 == 0) ldsw<f32x4>(S_PC + (w * 16 + 4 * g4) * 4, ac);
    }
    ldsw<f32x4>(S_PM + w * 1024 + l16, am);
    if (lane < 16) ldsw<float>(S_PS + (lane * 8 + w) * 4, as[0]);
    if ((i16 >> 2) == g4) ldsw<float>(S_PQ + (i16 * 8 + w) * 4, diag_pick(aq, lane));
    BAR();
  }
  if (w == KVW) {
    {
      const f32x4 s0 = ldsr<f32x4>(S_PS + i16 * 32), s1 = ldsr<f32x4>(S_PS + i16 * 32 + 16);
      const f32x4 q0 = ldsr<f32x4>(S_PQ + i16 * 32), q1 = ldsr<f32x4>(S_PQ + i16 * 32 + 16);
      const f32x4 s = s0 + s1, q = q0 + q1;
      const RowStat st = finish_stat((s[0] + s[1]) + (s[2] + s[3]), (q[0] + q[1]) + (q[2] + q[3]));
      if (lane < 16) { ldsw<float>(S_FIN + lane * 4, st.mu); ldsw<float>(S_FIN + 64 + lane * 4, st.rstd); }
    }
    f32x4 m = {0, 0, 0, 0};
#pragma unroll
    for (int ww = 0; ww < 8; ++ww) m += ldsr<f32x4>(S_PM + ww * 1024 + l16);
    f32x4 v;
    u32 off;
    if (kvmat) {
      float c1 = 0.f;
#pragma unroll
      for (int ww = 0; ww < 8; ++ww) c1 += ldsr<float>(S_PC + (ww * 16 + i16) * 4);
      asm volatile("s_waitcnt lgkmcnt(0)" ::: "memory");
      const f32x4 mu = ldsr<f32x4>(S_FIN + g4 * 16), rs = ldsr<f32x4>(S_FIN + 64 + g4 * 16);
      v = rs * (m - mu * c1);
      off = WS_V + (R0 >> 5) * 1024 + ((R0 >> 4) & 1) * 512 + (g4 & 1) * 256 + i16 * 16 + (g4 >> 1) * 8;
    } else {
      f32x4 c4 = {0, 0, 0, 0};
#pragma unroll
      for (int ww = 0; ww < 8; ++ww) c4 += ldsr<f32x4>(S_PC + (ww * 16 + 4 * g4) * 4);
      asm volatile("s_waitcnt lgkmcnt(0)" ::: "memory");
      const float mu = ldsr<float>(S_FIN + i16 * 4), rs = ldsr<float>(S_FIN + 64 + i16 * 4);
      v = rs * (m - mu * c4);
      off = WS_K + (R0 >> 5) * 1024 + (g4 & 1) * 512 + ((R0 & 31) + i16) * 16 + (g4 >> 1) * 8;
    }
    st8(wsr, off, 0, (u32x2){cvtpk(v[0], v[1]), cvtpk(v[2], v[3])});
    WAITV(1);
  } else if (MARK_P1 == 1 && w >= 4) {
      const rsrc_t mr = mkrsrc(ws, WS_END);
      const u32x4 s4 = {SENT, SENT, SENT, SENT};
      const int wm = w & 3;
#pragma unroll
      for (int half = 0; half < 2; ++half) {
        const u32 po = blk_of(half) * 32768 + (4 * j + wm) * 1024;
        __builtin_amdgcn_raw_buffer_store_b128(s4, mr, l16, WS_OX + po, 16);
        __builtin_amdgcn_raw_buffer_store_b128(s4, mr, l16, WS_HX + po, 16);
        __builtin_amdgcn_raw_buffer_store_b128(s4, mr, l16, WS_X1 + po, 16);
        const int R = blk_of(half) * 32 + (lane & 31) + dlt;
        const u32 mk = (u32)R < 2048u ? SENT : 0u;
        st16(mr, ((R & 2047) >> 5) * 32768 + (lane >> 5) * 512 + (R & 31) * 16, WS_TS + (4 * j + wm) * 1024, (u32x4){mk, mk, mk, mk});
      }
      const u32 uo = WS_UX + (blk_of(wm >> 1) * 112 + 14 * j + 7 * (wm & 1)) * 1024;
#pragma unroll
      for (int i = 0; i < 7; ++i) st16(mr, l16 + (i & 3) * 1024, uo + (i >> 2) * 4096, s4);
    WAITV(15);
  } else {
    WAITV(0);
  }
#define JOB(i) retile_job<i>(b, w, wo, w1, w2, w3, w4)
  u32x4 rts[5];
  if (w < 4) {
    static_for<0, 5>([&](auto I) {
      const Job jb = JOB(I.value);
      rts[I.value] = *(const GAS u32x4*)(jb.src + (u32)((lane >> 2) * jb.prow + (lane & 3) * 16));
    });
  }
  FENCE();
  STAMP(2);
  const int q_hp = w & 1, q_rt = (w >> 1) & 1, q_kh = w >> 2;
  const int head = 2 * q_hp + q_kh;
  bf16x8 qf;
  {
    BAR();
    STAMP(3);
    MEAS(3);
    DELAY(1);
    {
      f32x4 as = {0, 0, 0, 0}, aq = {0, 0, 0, 0};
      const int rt = w & 3, kh = w >> 2;
      const u32 sa = L_XIMG + (16 * rt + i16) * XP + kh * 512 + g4 * 16;
#pragma unroll
      for (int t = 0; t < 8; ++t) {
        const bf16x8 xf = ldsr<bf16x8>(sa + t * 64);
        as = mfma16(one, xf, as); aq = mfma16(xf, xf, aq);
      }
      MFMA_GAP16(as); MFMA_GAP16(aq);
      if (lane < 16) ldsw<float>(S_QS + ((16 * rt + lane) * 2 + kh) * 4, as[0]);
      if ((i16 >> 2) == g4) ldsw<float>(S_QS + 512 + ((16 * rt + i16) * 2 + kh) * 4, diag_pick(aq, lane));
    }
    auto kv_flag = [&]() {
      if (w == KVW) {
        if (KVW < 4) WAITV(5); else WAITV(0);
        if (lane < 8) __hip_atomic_store((GAS u64*)(ws + WS_FKV + lane * 2048 + b * 8), tag, __ATOMIC_RELAXED, __HIP_MEMORY_SCOPE_AGENT);
      }
    };
    if (FLAG_POS == 0) kv_flag();
    if (tid < 272) ldsw<u32x4>(L_ONES + tid * 16, (u32x4){0x3F803F80u, 0x3F803F80u, 0x3F803F80u, 0x3F803F80u});
    if (tid == 511) ldsw<u32x4>(L_CNT, (u32x4){0u, 0u, 0u, 0u});
    f32x16 acc = {}, accc = {};
    const int q = i16 >> 2, p = i16 & 3, cb = g4 & 1;
    const u32 wa = L_WQ + (256 * q_kh + 8 * h2 + q) * 128 + (((4 * q_hp + 2 * cb + (p >> 1)) ^ (4 * ((q >> 1) & 1))) * 16) + (p & 1) * 8;
    const u32 xa = L_XIMG + (32 * q_rt + r32) * XP + q_kh * 512 + h2 * 16;
#pragma unroll
    for (int t = 0; t < 16; ++t) {
      const bf16x8 af = cat(ldstr(wa + t * 2048), ldstr(wa + t * 2048 + 512));
      acc = mfma32(af, ldsr<bf16x8>(xa + t * 32), acc);
      if (FLAG_POS == 1 && t == 3) kv_flag();
    }
    u32x4 fka, fkb;
    if (w == 1) { fka = ld16(wsr, lane * 32, WS_FKV + (b & 7) * 2048); fkb = ld16(wsr, lane * 32 + 16, WS_FKV + (b & 7) * 2048); }
    const u32 wb = wa + q_rt * 16384;
#pragma unroll
    for (int t = 0; t < 8; ++t) accc = mfma32(cat(ldstr(wb + t * 2048), ldstr(wb + t * 2048 + 512)), one, accc);
    if (FLAG_POS == 2) kv_flag();
    MFMA_GAP32(acc); MFMA_GAP32(accc);
    f32x4 k0, k1, s0, s1;
    if (q_kh) {
      k0 = (f32x4){acc[8], acc[9], acc[10], acc[11]}; k1 = (f32x4){acc[12], acc[13], acc[14], acc[15]};
      s0 = (f32x4){acc[0], acc[1], acc[2], acc[3]}; s1 = (f32x4){acc[4], acc[5], acc[6], acc[7]};
    } else {
      k0 = (f32x4){acc[0], acc[1], acc[2], acc[3]}; k1 = (f32x4){acc[4], acc[5], acc[6], acc[7]};
      s0 = (f32x4){acc[8], acc[9], acc[10], acc[11]}; s1 = (f32x4){acc[12], acc[13], acc[14], acc[15]};
    }
    ldsw<f32x4>(L_WKV + w * 2048 + l16, s0); ldsw<f32x4>(L_WKV + w * 2048 + 1024 + l16, s1);
    if (r32 == 0) {
#pragma unroll
      for (int a = 0; a < 4; ++a) ldsw<f32x4>(S_QC + (((q_hp * 2 + q_rt) * 2 + q_kh) * 32 + 8 * a + 4 * h2) * 4, (f32x4){accc[4 * a], accc[4 * a + 1], accc[4 * a + 2], accc[4 * a + 3]});
    }
    if (w == 1) {
      if (!flags_ok(fka, fkb, taglo, taghi)) wait_flags(wsr, WS_FKV + (b & 7) * 2048, taglo, taghi, lane, t0);
      if (b == 0 && lane == 0) __hip_atomic_store((GAS u64*)(ws + WS_EPOCH), tag, __ATOMIC_RELAXED, __HIP_MEMORY_SCOPE_AGENT);
    }
    BAR();
    STAMP(5);
    MEAS(5);
    dma_strided<KV_SPREAD ? 2 : 3, true, 0x2000>(ws + WS_K, w * 1024 + l16, lb + L_K + w * 1024, 8192);
    dma_strided<2, true, 0x2000>(ws + WS_V, w * 1024 + l16, lb + L_V + w * 1024, 8192);
    k0 += ldsr<f32x4>(L_WKV + (w ^ 4) * 2048 + l16); k1 += ldsr<f32x4>(L_WKV + (w ^ 4) * 2048 + 1024 + l16);
    f32x4 c0 = {0, 0, 0, 0}, c1 = {0, 0, 0, 0};
#pragma unroll
    for (int s = 0; s < 4; ++s) {
      c0 += ldsr<f32x4>(S_QC + ((q_hp * 4 + s) * 32 + 16 * q_kh + 4 * h2) * 4);
      c1 += ldsr<f32x4>(S_QC + ((q_hp * 4 + s) * 32 + 16 * q_kh + 8 + 4 * h2) * 4);
    }
    const f32x2 sp = ldsr<f32x2>(S_QS + (32 * q_rt + r32) * 8), qp = ldsr<f32x2>(S_QS + 512 + (32 * q_rt + r32) * 8);
    const RowStat st = finish_stat(sp[0] + sp[1], qp[0] + qp[1]);
    const float sc = st.rstd * QSCALE;
    k0 = (k0 - st.mu * c0) * sc; k1 = (k1 - st.mu * c1) * sc;
    qf = opaque(asbf((u32x4){cvtpk(k0[0], k0[1]), cvtpk(k0[2], k0[3]), cvtpk(k1[0], k1[1]), cvtpk(k1[2], k1[3])}));
  }
  STAMP(4);
  const u32 pc_in = (u32)((4 * j + (w >> 1)) * 1024 + (w & 1) * 512);
  auto hx_so = [&](u32 base, int half) { return base + blk_of(half) * 32768 + pc_in; };
  auto ts_vo = [&](int half) {
    const u32 l = opaque<u32>(lane);
    const int rr = (int)(l >> 1) + dlt;
    const u32 R = (u32)(blk_of(half) * 32 + rr);
    const u32 o = (u32)((rr >> 5) + 1) * 32768 + (u32)(rr & 31) * 16 + (l & 1) * 8;
    return R < 2048u && !(PUT16 && (l & 1)) ? o : OOB;
  };
  auto ox_off = [&]() { return WS_OX + blk_of(q_rt) * 32768 + (4 * j + head) * 1024 + l16; };
  {
    WAITV(0);
    asm volatile("" : "+v"(rts[0]), "+v"(rts[1]), "+v"(rts[2]), "+v"(rts[3]), "+v"(rts[4]));
    BAR();
    STAMP(6);
    MEAS(6);
    DELAY(2);
  }
  u32 opk[4];
  bf16x8 a_o[8], a_1[8], a_2[8], a3[32];
  u32x2 xres[2];
  {
    u32 ka = L_K + l16;
    const bool isv = r32 < 16;
    u32 va = isv ? L_V + h2 * 256 + r32 * 16 : L_ONES + ((r32 >= 20 && r32 < 28) ? 64 : 0);
    const u32 vinc = isv ? 4096u : 0u;
    f32x16 o = {};
    f32x16 negm;
    {
      const bf16x8 kf = ldsr<bf16x8>(ka);
      f32x16 z = {};
      const f32x16 s = mfma32(kf, qf, z);
      float m = fmaxf(fmaxf(s[0], s[1]), fmaxf(s[2], s[3]));
#pragma unroll
      for (int i = 4; i < 16; i += 2) m = fmaxf(m, fmaxf(s[i], s[i + 1]));
      const auto rr = __builtin_amdgcn_permlane32_swap(__float_as_uint(m), __float_as_uint(m), false, false);
      m = fmaxf(__uint_as_float(rr[0]), __uint_as_float(rr[1]));
#pragma unroll
      for (int i = 0; i < 16; ++i) negm[i] = -m;
    }
    f32x16 s = mfma32(ldsr<bf16x8>(ka), qf, negm);
    bf16x8 kf = ldsr<bf16x8>(ka + 1024), v0 = ldsr<bf16x8>(va), v1 = ldsr<bf16x8>(va + 512);
#define SB() __builtin_amdgcn_sched_barrier(0)
    auto tiles4 = [&](auto&& hook) {
#pragma unroll
      for (int u = 0; u < 4; ++u) {
        float e[16];
        u32 pk[8];
        f32x16 sn;
        auto ex = [&](int a, int b_) {
#pragma unroll
          for (int i = a; i < b_; ++i) e[i] = opaque(__builtin_amdgcn_exp2f(s[i]));
        };
        auto cv = [&](int a, int b_) {
#pragma unroll
          for (int i = a; i < b_; ++i) pk[i] = opaque(cvtpk(e[2 * i], e[2 * i + 1]));
        };
        auto ms = [&]() { sn = mfma32(kf, qf, negm); asm volatile("" : "+v"(sn)); kf = ldsr<bf16x8>(ka + (u + 2) * 1024); };
        if (u == 3) va += vinc;
        const u32 vn = va + ((u + 1) & 3) * 1024;
        auto m0 = [&]() { o = mfma32(v0, asbf((u32x4){pk[0], pk[1], pk[2], pk[3]}), o); asm volatile("" : "+v"(o)); v0 = ldsr<bf16x8>(vn); };
        auto m1 = [&]() { o = mfma32(v1, asbf((u32x4){pk[4], pk[5], pk[6], pk[7]}), o); asm volatile("" : "+v"(o)); v1 = ldsr<bf16x8>(vn + 512); };
        if (ATT_PIPE == 1) {
          ex(0, 4); SB(); ms(); hook(u); SB(); ex(4, 8); cv(0, 4); SB(); m0(); SB(); ex(8, 16); cv(4, 8); SB(); m1(); SB();
        } else if (ATT_PIPE == 2) {
          ex(0, 4); SB(); ms(); hook(u); SB(); ex(4, 8); cv(0, 4); ex(8, 10); SB(); m0(); SB(); ex(10, 16); cv(4, 8); SB(); m1(); SB();
        } else if (ATT_PIPE == 3) {
          ms(); hook(u); SB(); ex(0, 8); cv(0, 4); SB(); m0(); SB(); ex(8, 16); cv(4, 8); SB(); m1(); SB();
        } else if (ATT_PIPE == 4) {
          ex(0, 6); cv(0, 2); SB(); ms(); hook(u); SB(); ex(6, 8); cv(2, 4); ex(8, 12); SB(); m0(); SB(); ex(12, 16); cv(4, 8); SB(); m1(); SB();
        } else {
          ex(0, 4); cv(0, 2); SB(); ms(); hook(u); SB(); ex(4, 8); cv(2, 4); SB(); m0(); SB(); ex(8, 12); cv(4, 6); ex(12, 16); cv(6, 8); SB(); m1(); SB();
        }
        s = sn;
      }
      ka += 4096;
    };
    if (w < 4) __builtin_amdgcn_s_setprio(2); else __builtin_amdgcn_s_setprio(1);
    auto kv_rest = [&](u32 img, u32 limg, int c0, auto n) {
      dma_strided<n.value, true, 4096>(ws + img + c0 * 8192, opaque<u32>((w & 3) * 1024 + l16), lb + limg + c0 * 8192 + (w & 3) * 1024, 4096);
    };
    using N2 = std::integral_constant<int, 2>; using N4 = std::integral_constant<int, 4>;
    auto lds_count = [&](u32 off) {
      u32 n;
      asm volatile("ds_add_rtn_u32 %0, %1, %2\n\ts_waitcnt lgkmcnt(0)" : "=v"(n) : "v"(lb + off), "v"(lane == 0 ? 1u : 0u) : "memory");
      return (u32)__builtin_amdgcn_readfirstlane(n);
    };
    const u32x4 s4 = {SENT, SENT, SENT, SENT};
    const u32 kvo = opaque<u32>((w & 3) * 1024 + l16);
    static_assert(WS_V - WS_K == 0x10000 && L_V - L_K == 0x10000, "K and V images");
    u32x4 fa, fb;
    auto fetch = [&](int p, u32 l) {
      const u32 fo = (2 * j * 32 + 4 * w) * 1024;
      if (p < 24) {
        const int t = p & 7;
        const bf16x8 v = asbf(ld16(wsr, l + (t & 3) * 1024, (p < 8 ? WS_WO : p < 16 ? WS_W1 : WS_W2) + fo + (t >> 2) * 32768));
        if (p < 8) a_o[t] = v; else if (p < 16) a_1[t] = v; else a_2[t] = v;
      } else if (p < 26) {
        xres[p - 24] = *(const GAS u32x2*)((const char*)x + (u32)((blk_of(p - 24) * 32 + (l >> 5)) * 1024 + (64 * j + 8 * w) * 2 + ((l >> 4) & 1) * 8));
      } else if (p < 26 + A3_N0) {
        a3[p - 26] = asbf(ld16(wsr, w < 7 ? ((7 * j + w) * 32) * 1024 + l : OOB, WS_W3 + (p - 26) * 1024));
      } else if (p < 33 + A3_N0) {
        dma1(ws + WS_W3 + (p - 26 - A3_N0) * 1024, (((u32)(b >> 6) & 3u) * 8 + w) * 7168 + j * 229376 + l, lb + L_DUM1);
      } else if (W4_WARM == 1 && p < 40 + A3_N0) {
        dma1(ws + WS_W4 + (p - 33 - A3_N0) * 1024, (((u32)(b >> 6) & 3u) * 8 + w) * 7168 + j * 229376 + l, lb + L_DUM1);
      }
    };
    static_for<0, 16>([&](auto IT) {
      constexpr int it = IT.value;
      ASTAMP(it, 0);
      if (it == 6) DELAY(3);
      if (it == 10) DELAY(4);
      if (w < 4) {
        if (it == 0) {
          const u32 l = opaque<u32>(lane);
          const u32 sb = L_STG + w * 5120;
#pragma unroll
          for (int i = 0; i < 5; ++i) ldsw<u32x4>(sb + i * 1024 + l * 16, rts[i]);
          const u32 tb = sb + ((l & 15) >> 2) * 64 + ((l >> 4) & 1) * 32 + (l & 3) * 8;
          static_for<0, 5>([&](auto I) {
            const Job jb = JOB(I.value);
            const u32 t0a = tb + I.value * 1024 + (l >> 5) * (jb.perm ? 256 : 512);
            st16(wsr, l * 16, WS_WO + jb.f * 1024, asu(cat(ldstr(t0a), ldstr(t0a + (jb.perm ? 512 : 256)))));
          });
        } else if (it == MARK_IT && !NOMARK && MARK_P1 == 0) {
          const u32 l = opaque<u32>(lane);
#pragma unroll
          for (int half = 0; half < 2; ++half) {
            const u32 po = blk_of(half) * 32768 + (4 * j + w) * 1024;
            st16(wsr, l * 16, WS_OX + po, s4); st16(wsr, l * 16, WS_HX + po, s4); st16(wsr, l * 16, WS_X1 + po, s4);
            const int R = blk_of(half) * 32 + (int)(l & 31) + dlt;
            const u32 mk = (u32)R < 2048u ? SENT : 0u;
            st16(wsr, ((R & 2047) >> 5) * 32768 + (l >> 5) * 512 + (R & 31) * 16, WS_TS + (4 * j + w) * 1024, (u32x4){mk, mk, mk, mk});
          }
        } else if (it == MARK_IT + 1 && !NOMARK && MARK_P1 == 0) {
          const u32 uo = WS_UX + (blk_of(w >> 1) * 112 + 14 * j + 7 * (w & 1)) * 1024;
          const u32 l = opaque<u32>(l16);
#pragma unroll
          for (int i = 0; i < 7; ++i) st16(wsr, l + (i & 3) * 1024, uo + (i >> 2) * 4096, s4);
        } else if (it == FLAG_IT) {
          asm volatile("s_waitcnt vmcnt(%0)" :: "n"(MARK_P1 == 2 ? marks_before(FLAG_IT) : 0) : "memory");
          if (lds_count(L_CNT + 4) == 3 && lane < 8) __hip_atomic_store((GAS u64*)(ws + WS_FW + lane * 2048 + b * 8), tag, __ATOMIC_RELAXED, __HIP_MEMORY_SCOPE_AGENT);
        } else if (MARK_P1 == 2 && it == FM_IT) {
          asm volatile("s_waitcnt vmcnt(%0)" :: "n"(FM_IT - 1 >= 8 ? FPI : 0) : "memory");
          if (lds_count(L_CNT + 12) == 3 && lane < 8) __hip_atomic_store((GAS u64*)(ws + WS_FM + lane * 2048 + b * 8), tag, __ATOMIC_RELAXED, __HIP_MEMORY_SCOPE_AGENT);
        }
      } else {
        if (KV_SPREAD) {
          if (KV_SPREAD == 1 && it >= 1 && it <= 3) {
            WAITV(0);
            if (lane == 0) asm volatile("ds_add_u32 %0, %1" :: "v"(lb + L_CNT), "v"(1u) : "memory");
          }
        } else if (it == 0) {
          kv_rest(WS_V, L_V, 2, N4{}); kv_rest(WS_K, L_K, 3, N4{}); kv_rest(WS_V, L_V, 4, N4{}); kv_rest(WS_K, L_K, 5, N4{});
          kv_rest(WS_V, L_V, 6, N4{}); kv_rest(WS_K, L_K, 7, N2{});
        } else if (it == 1) {
          WAITV(0);
          lds_count(L_CNT);
        }
      }
      if (KV_SPREAD ? (it & 3) == 3 && it < 12 : it == (ATT_PIPE ? 3 : 4)) {
        while ((u32)__builtin_amdgcn_readfirstlane(*(volatile LAS u32*)((LAS char*)smem + L_CNT)) < (KV_SPREAD ? (u32)(it + 1) : 4u)) {
          if (timed_out(t0)) break;
          __builtin_amdgcn_s_sleep(1);
        }
        FENCE();
      }
      if (it == 7) {
        const u32 l = opaque<u32>(l16);
        fa = ld16(wsr, l * 2, WS_FW + (b & 7) * 2048); fb = ld16(wsr, l * 2 + 16, WS_FW + (b & 7) * 2048);
      }
      if (it == 8) {
        if (!flags_ok(fa, fb, taglo, taghi)) wait_flags(wsr, WS_FW + (b & 7) * 2048, taglo, taghi, lane, t0);
      }
      if (MARK_P1 == 2 && it == FM_CHK - 1) {
        const u32 l = opaque<u32>(l16);
        fa = ld16(wsr, l * 2, WS_FM + (b & 7) * 2048); fb = ld16(wsr, l * 2 + 16, WS_FM + (b & 7) * 2048);
      }
      if (MARK_P1 == 2 && it == FM_CHK) {
        if (!flags_ok(fa, fb, taglo, taghi)) wait_flags(wsr, WS_FM + (b & 7) * 2048, taglo, taghi, lane, t0);
      }
      if (it >= 8) {
        const u32 l = opaque<u32>(l16);
#pragma unroll
        for (int p = FPI * (it - 8); p < FPI * (it - 7); ++p) fetch(p, l);
      }
      if (it == PRIO_IT) asm volatile("s_cmp_ge_u32 %0, 4\n\ts_cbranch_scc1 .Lpe%=\n\ts_setprio 0\n.Lpe%=:" :: "s"(w) : "scc");
      FENCE();
      ASTAMP(it, 1);
      tiles4([&](int u) {
        if (KV_SPREAD == 2 && it >= 1 && it <= 3 && u == CNT_U) count_late<(it < 3 ? 2 * CNT_U : 0)>(w, lb + L_CNT);
        if (MARK_P1 == 2 && it >= MARK_IT && (u % MARK_ST) == 0) {
          const int n = marks_before(it) + u / MARK_ST;
          if (n < 8) {
            const int half = n >> 2, kind = n & 3;
            const u32 l = opaque<u32>(lane);
            if (kind < 3) st16_early(w, srd, l * 16, (kind == 0 ? WS_OX : kind == 1 ? WS_HX : WS_X1) + blk_of(half) * 32768 + (4 * j + w) * 1024, s4);
            else {
              const int R = blk_of(half) * 32 + (int)(l & 31) + dlt;
              const u32 mk = (u32)R < 2048u ? SENT : 0u;
              st16_early(w, srd, ((R & 2047) >> 5) * 32768 + (l >> 5) * 512 + (R & 31) * 16, WS_TS + (4 * j + w) * 1024, (u32x4){mk, mk, mk, mk});
            }
          } else if (n < 15) {
            const int i = n - 8;
            st16_early(w, srd, opaque<u32>(l16), WS_UX + (blk_of(w >> 1) * 112 + 14 * j + 7 * (w & 1) + i) * 1024, s4);
          }
        }
        if (KV_SPREAD && it < 3) {
          const int T = 16 + 16 * it + 4 * u;
          dma_pair_late(w, ws + WS_K + T * 1024, kvo, lb + L_K + T * 1024 + (w & 3) * 1024);
        }
      });
      FENCE();
      if (it == 9) STAMP(7);
    });
    STAMP(8);
    MEAS(8);
    __builtin_amdgcn_s_setprio(0);
    ka = L_K + l16;
    float l = o[8];
    if (!__all(l <= 1.8446744e19f)) {
      float m = -INFINITY;
      o = (f32x16){};
      u32 vb = isv ? L_V + h2 * 256 + r32 * 16 : L_ONES;
      for (int t = 0; t < 64; ++t) {
        f32x16 z = {};
        const f32x16 sx = mfma32(ldsr<bf16x8>(ka + t * 1024), qf, z);
        float mt = sx[0];
#pragma unroll
        for (int i = 1; i < 16; ++i) mt = fmaxf(mt, sx[i]);
        const auto rr = __builtin_amdgcn_permlane32_swap(__float_as_uint(mt), __float_as_uint(mt), false, false);
        mt = fmaxf(fmaxf(__uint_as_float(rr[0]), __uint_as_float(rr[1])), m);
        const float f = __builtin_amdgcn_exp2f(m - mt);
        m = mt;
        u32 pk[8];
#pragma unroll
        for (int i = 0; i < 8; ++i) pk[i] = cvtpk(__builtin_amdgcn_exp2f(sx[2 * i] - m), __builtin_amdgcn_exp2f(sx[2 * i + 1] - m));
#pragma unroll
        for (int i = 0; i < 16; ++i) o[i] *= f;
        o = mfma32(ldsr<bf16x8>(vb), asbf((u32x4){pk[0], pk[1], pk[2], pk[3]}), o);
        o = mfma32(ldsr<bf16x8>(vb + 512), asbf((u32x4){pk[4], pk[5], pk[6], pk[7]}), o);
        vb += isv ? 1024u : 0u;
      }
      MFMA_GAP32(o);
      l = o[8];
    }
    const float rl = __builtin_amdgcn_rcpf(l);
#pragma unroll
    for (int i = 0; i < 4; ++i) opk[i] = canon(cvtpk(o[2 * i] * rl, o[2 * i + 1] * rl));
  }
  st16(wsr, ox_off(), 0, (u32x4){opk[0], opk[1], opk[2], opk[3]});
  STAMP(9);
  bf16x8 sel;
  auto make_sel = [&]() {
    const u32 l = opaque<u32>(lane), e = ((l >> 2) & 3) != (l >> 4) ? 0u : (l & 1) ? 0x3F800000u : 0x00003F80u;
    const u32 d0 = (l & 2) ? 0u : e, d1 = (l & 2) ? e : 0u;
    sel = asbf((u32x4){d0, d1, d0, d1});
  };
  make_sel();
  const u32 gvo = opaque<u32>(w * 4096 + l16) & 0x73F0u;
  auto pw_of = [&](u32 l) { return L_P + ((l >> 3) & 3) * 256 + (w >> 1) * 1024 + (l & 7) * 32 + (l >> 5) * 16 + (((w ^ (l >> 3)) & 1) * 8); };
  const u32 pw = pw_of(lane);
  const u32 pr = L_P + gvo;
  auto g_load = [&](bf16x8 (&f)[4], u32 src) {
#pragma unroll
    for (int t = 0; t < 4; ++t) f[t] = asbf(ld16(wsr, gvo | (t * 1024), src));
  };
  auto g_wait = [&](bf16x8 (&f)[4], u32 src) {
    if (__builtin_expect(unwritten(f), 0)) {
      do {
        if (timed_out(t0)) break;
        regather4(srd, gvo, src, f[0], f[1], f[2], f[3]);
      } while (unwritten(f));
    }
    asm volatile("" : "+v"(f[0]), "+v"(f[1]), "+v"(f[2]), "+v"(f[3]));
  };
  auto row_stats = [&](int half, const bf16x8 (&f)[4]) {
    float s0 = 0.f, s1 = 0.f, q0 = 0.f, q1 = 0.f;
#pragma unroll
    for (int t = 0; t < 4; ++t) {
      const u32x4 d = asu(f[t]);
      s0 = dot2(d[0], 0x3F803F80u, s0); q0 = dot2(d[0], d[0], q0); s1 = dot2(d[1], 0x3F803F80u, s1); q1 = dot2(d[1], d[1], q1);
      s0 = dot2(d[2], 0x3F803F80u, s0); q0 = dot2(d[2], d[2], q0); s1 = dot2(d[3], 0x3F803F80u, s1); q1 = dot2(d[3], d[3], q1);
    }
    const f32x2 sq = {swap32_add(s0 + s1), swap32_add(q0 + q1)};
    if (lane < 32) ldsw<f32x2>(L_STAT + half * STH + lane * STP + w * 8, sq);
  };
  auto partials = [&](int half, f32x16& acc0, f32x16& acc1, u32 pw) {
#pragma unroll
    for (int a = 0; a < 4; ++a) {
      ldsw<u32x2>(pw + half * 32768 + a * 4096, (u32x2){cvtpk(acc0[4 * a], acc0[4 * a + 1]), cvtpk(acc0[4 * a + 2], acc0[4 * a + 3])});
      ldsw<u32x2>(pw + half * 32768 + (4 + a) * 4096, (u32x2){cvtpk(acc1[4 * a], acc1[4 * a + 1]), cvtpk(acc1[4 * a + 2], acc1[4 * a + 3])});
    }
  };
  const u32 stw = opaque<u32>(L_STAT + r32 * STP + w * 8);
  auto slice_gemm = [&](int half, const bf16x8 (&a)[8], const bf16x8 (&f)[4], bool stats, auto&& mid) {
    f32x16 acc0 = {}, acc1 = {};
    auto stat_block = [&]() {
      float s0 = 0.f, s1 = 0.f, q0 = 0.f, q1 = 0.f;
#pragma unroll
      for (int t = 0; t < 4; ++t) {
        const u32x4 d = asu(f[t]);
        s0 = dot2(d[0], 0x3F803F80u, s0); q0 = dot2(d[0], d[0], q0); s1 = dot2(d[1], 0x3F803F80u, s1); q1 = dot2(d[1], d[1], q1);
        s0 = dot2(d[2], 0x3F803F80u, s0); q0 = dot2(d[2], d[2], q0); s1 = dot2(d[3], 0x3F803F80u, s1); q1 = dot2(d[3], d[3], q1);
      }
      ldsw<f32x2>(stw + half * STH, (f32x2){swap32_add(s0 + s1), swap32_add(q0 + q1)});
      __builtin_amdgcn_sched_barrier(0);
    };
    if (stats && STAT_POS == 0) stat_block();
#pragma unroll
    for (int t = 0; t < 4; ++t) {
      acc0 = mfma32(a[t], f[t], acc0);
      acc1 = mfma32(a[4 + t], f[t], acc1);
    }
    __builtin_amdgcn_sched_barrier(0);
    if (stats && STAT_POS == 1) stat_block();
    mid();
    __builtin_amdgcn_sched_barrier(0);
    partials(half, acc0, acc1, pw);
  };
  auto reduce_at = [&](int half, u32 pr_) -> f32x4 {
    f32x4 acc = {0, 0, 0, 0};
#pragma unroll
    for (int q = 0; q < 4; ++q) acc = mfma16(sel, ldsr<bf16x8>(pr_ + half * 32768 + q * 1024), acc);
    return acc;
  };
  auto reduce = [&](int half) { return reduce_at(half, pr); };
  auto src_of = [&](u32 base, int half) { return base + blk_of(half) * 32768; };
  const u32 hvo = opaque<u32>(PUT16 && (lane & 1) ? OOB : (u32)lane * 8);
  auto put_at = [&](u32 vo, u32 so, u32x2 v) {
    if (PUT16) {
      const u32 n0 = __builtin_amdgcn_mov_dpp(v[0], 0xB1, 0xF, 0xF, true), n1 = __builtin_amdgcn_mov_dpp(v[1], 0xB1, 0xF, 0xF, true);
      st16(wsr, vo, so, (u32x4){v[0], v[1], n0, n1});
    } else st8(wsr, vo, so, v);
  };
  auto pack4 = [&](f32x4 v) { return (u32x2){canon(cvtpk(v[0], v[1])), canon(cvtpk(v[2], v[3]))}; };
  const u32 a3off = w < 7 ? ((7 * j + w) * 32) * 1024 + l16 : OOB;
  auto a3_load = [&](int t0_, int t1_) {
#pragma unroll
    for (int t = 0; t < 32; ++t) if (t >= t0_ && t < t1_) a3[t] = asbf(ld16(wsr, a3off, WS_W3 + t * 1024));
  };

  auto a3_slot = [&](int k) {
    constexpr int n = 32 - A3_N0;
    a3_load(A3_N0 + k * n / 6, A3_N0 + (k + 1) * n / 6);
  };
  f32x4 cs3p = {0.f, 0.f, 0.f, 0.f};
  auto cs3_acc = [&](int t0_, int t1_) {
#pragma unroll
    for (int t = 0; t < 32; ++t) if (t >= t0_ && t < t1_) cs3p = mfma16(sel, a3[t], cs3p);
  };
  f32x4 hres[2], xp[2], cs1;
  bf16x8 fA[4], fB[4];
  const u32 str = opaque<u32>(L_STAT + (lane >> 1) * STP + (lane & 1) * 32);
  auto back = [&](int half, bool stats, auto&& epi, auto&& hook) {
    bf16x8 p[4];
    f32x4 p0 = {0, 0, 0, 0}, p1 = {0, 0, 0, 0};
#pragma unroll
    for (int q = 0; q < 4; ++q) p[q] = ldsr<bf16x8>(pr + half * 32768 + q * 1024);
    if (stats) { p0 = ldsr<f32x4>(str + half * STH); p1 = ldsr<f32x4>(str + half * STH + 16); }
    hook();
    __builtin_amdgcn_sched_barrier(0);
    f32x4 acc = {0, 0, 0, 0};
#pragma unroll
    for (int q = 0; q < 4; ++q) acc = mfma16(sel, p[q], acc);
    RowStat st = {0.f, 0.f, 0.f};
    if (stats) { const f32x4 p = p0 + p1; st = finish_stat(xor1_add(p[0] + p[2]), xor1_add(p[1] + p[3])); }
    return epi(half, acc, st);
  };
  auto phase = [&](const bf16x8 (&a)[8], u32 src, bool stats, int gb, auto&& first, auto&& epi, auto&& put, auto&& pre, auto&& slotA, auto&& slotB, int sbase) {
    PSTAMP(sbase, 0);
    g_wait(fA, src_of(src, 0));
    STAMP(sbase);
    PSTAMP(sbase, 1);
    slice_gemm(0, a, fA, stats, [&]() {
      if (gb == 0) g_load(fB, src_of(src, 1));
      if (SLOT_POS == 0) slotA();
      first();
    });
    PSTAMP(sbase, 4);
    BAR();
    PSTAMP(sbase, 5);
    const u32x2 vA = back(0, stats, epi, [&]() { if (gb == 1) g_load(fB, src_of(src, 1)); });
    PSTAMP(sbase, 6);
    if (gb == 2) g_load(fB, src_of(src, 1));
    __builtin_amdgcn_sched_barrier(0);
    put(0, vA);
    if (SLOT_POS == 1) slotA();
    __builtin_amdgcn_sched_barrier(0);
    PSTAMP(sbase, 7);
    g_wait(fB, src_of(src, 1));
    STAMP(sbase + 1);
    PSTAMP(sbase, 8);
    slice_gemm(1, a, fB, stats, [&]() { if (SLOT_POS == 0) slotB(); });
    PSTAMP(sbase, 11);
    BAR();
    PSTAMP(sbase, 12);
    const u32x2 vB = back(1, stats, epi, [&]() {
      pre();
    });
    PSTAMP(sbase, 13);
    PSTAMP(sbase, 14);
    __builtin_amdgcn_sched_barrier(0);
    put(1, vB);
    if (SLOT_POS == 1) slotB();
    __builtin_amdgcn_sched_barrier(0);
    PSTAMP(sbase, 15);
  };
  auto prio_late = [&](int p) {
#define PRIO_ASM(P) asm volatile("s_cmp_lt_u32 %0, 4\n\ts_cbranch_scc1 .Lprio%=\n\ts_setprio " #P "\n.Lprio%=:" :: "s"(w) : "scc")
    if (p == 0) PRIO_ASM(0); else if (p == 1) PRIO_ASM(1); else PRIO_ASM(2);
#undef PRIO_ASM
  };
  const u32 a4off = (2 * j * 112 + 14 * w) * 1024 + l16;
  {
    float c[2] = {0.f, 0.f};
#pragma unroll
    for (int t = 0; t < 8; ++t) {
      const u32x4 d = asu(a_1[t]);
#pragma unroll
      for (int e = 0; e < 4; ++e) c[t >> 2] = dot2(d[e], 0x3F803F80u, c[t >> 2]);
    }
    const float c0 = swap32_add(c[0]), c1 = swap32_add(c[1]);
    ldsw<float>(L_CS1P + w * 256 + lane * 4, lane < 32 ? c0 : c1);
  }
  cs3_acc(0, A3_N0);
  __builtin_amdgcn_sched_barrier(0);
  if (O_SLEEP) __builtin_amdgcn_s_sleep(O_SLEEP);
  g_load(fA, src_of(WS_OX, 0));
  g_load(fB, src_of(WS_OX, 1));
  __builtin_amdgcn_sched_barrier(0);
  phase(a_o, WS_OX, false, -1,
        [&]() { BAR(); },
        [&](int half, f32x4 acc, RowStat) {
          if (half == 1) {
            cs1 = (f32x4){0.f, 0.f, 0.f, 0.f};
#pragma unroll
            for (int v = 0; v < 8; ++v) cs1 += ldsr<f32x4>(L_CS1P + v * 256 + w * 32 + (lane & 1) * 16);
            cs1 = opaque(cs1);
          }
          hres[half] = acc + (f32x4){bflo(xres[half][0]), bfhi(xres[half][0]), bflo(xres[half][1]), bfhi(xres[half][1])};
          return pack4(hres[half]);
        },
        [&](int half, u32x2 v) { put_at(hvo, hx_so(WS_HX, half), v); },
        [&]() { g_load(fA, src_of(WS_HX, 0)); },
        [&]() {
          if (W4_WARM == 2) {
#pragma unroll
            for (int i = 0; i < 7; ++i) dma1(ws + WS_W4 + i * 1024, (((u32)(b >> 6) & 3u) * 8 + w) * 7168 + j * 229376 + l16, lb + L_DUM1);
          }
          a3_slot(0);
        },
        [&]() { a3_slot(1); }, 10);
  cs3_acc(A3_N0, A3_N0 + (32 - A3_N0) / 6);
  __builtin_amdgcn_sched_barrier(0);
  STAMP(12);
  MEAS(12);
  DELAY(5);
  const u32 tso[2] = {opaque<u32>(ts_vo(0)), opaque<u32>(ts_vo(1))};
  phase(a_1, WS_HX, true, GB_POS, [&]() { },
        [&](int half, f32x4 acc, RowStat st) {
          const float r2 = __builtin_amdgcn_rsqf(st.var * st.rstd * st.rstd + LN_EPS), rr = st.rstd * r2;
          const f32x4 pre = (acc - st.mu * cs1) * rr;
          const f32x2 g0 = gelu2((f32x2){pre[0], pre[1]}), g1 = gelu2((f32x2){pre[2], pre[3]});
          xp[half] = (hres[half] - st.mu) * st.rstd;
          return (u32x2){canon(cvtpk(g0[0], g0[1])), canon(cvtpk(g1[0], g1[1]))};
        },
        [&](int half, u32x2 v) { put_at(tso[half], hx_so(WS_TS, half) - 32768, v); },
        [&]() { g_load(fA, src_of(WS_TS, 0)); },
        [&]() { a3_slot(2); },
        [&]() { a3_slot(3); dma4_sc1(ws + WS_W4 + 10 * 1024, a4off, lb + L_A4 + w * 8192); }, 13);
  cs3_acc(A3_N0 + (32 - A3_N0) / 6, A3_N0 + 3 * (32 - A3_N0) / 6);
  __builtin_amdgcn_sched_barrier(0);
  STAMP(15);
  phase(a_2, WS_TS, false, GB_POS, [&]() { },
        [&](int half, f32x4 acc, RowStat) {
          const f32x4 x1 = xp[half] + acc;
          ldsw<f32x4>(L_RES + half * 8192 + w * 1024 + l16, hres[half] + x1);
          return pack4(x1);
        },
        [&](int half, u32x2 v) { put_at(hvo, hx_so(WS_X1, half), v); },
        [&]() { dma4_sc1(ws + src_of(WS_X1, 0), gvo, lb + L_P + w * 4096); },
        [&]() { a3_slot(4); dma4_sc1(ws + WS_W4 + 114688 + 10 * 1024, a4off, lb + L_A4 + w * 8192 + 4096); },
        [&]() { a3_slot(5); }, 16);
  cs3_acc(A3_N0 + 3 * (32 - A3_N0) / 6, 32);
  {
    const float c = swap32_add((cs3p[0] + cs3p[1]) + (cs3p[2] + cs3p[3]));
    ldsw<float>(L_CS + w * 128 + r32 * 4, c);
  }
  __builtin_amdgcn_sched_barrier(0);
  STAMP(18);
  MEAS(18);
  DELAY(7);
  bf16x8 a4[10][2];
  auto a4_load = [&](int n) { a4[n >> 1][n & 1] = asbf(ld16(wsr, a4off, WS_W4 + (n & 1) * 114688 + (n >> 1) * 1024)); };
  constexpr int ord[14] = {0, 2, 4, 6, 1, 3, 5, 7, 8, 10, 12, 9, 11, 13};
  u32x4 uf[28];
  auto u_load1 = [&](int k) { uf[k] = ld16(wsr, l16, WS_UX + (blk_of(k / 14) * 112 + w * 14 + ord[k % 14]) * 1024); };
  {
    auto x1_finish = [&](int half) {
#pragma unroll
      for (int t = 0; t < 4; ++t) fA[t] = ldsr<bf16x8>(pr + half * 32768 + t * 1024);
      if (__builtin_expect(unwritten(fA), 0)) {
        do {
          if (timed_out(t0)) break;
          __builtin_amdgcn_s_sleep(1);
          dma4_sc1(ws + src_of(WS_X1, half), gvo, lb + L_P + half * 32768 + w * 4096);
          WAITV(0);
#pragma unroll
          for (int t = 0; t < 4; ++t) fA[t] = ldsr<bf16x8>(pr + half * 32768 + t * 1024);
        } while (unwritten(fA));
      }
      row_stats(half, fA);
    };
    struct UOut { float nmu, k1, k2, ir; f32x4 cs[2]; f32x4 p[4]; u32 up[8]; u32 cb, vo; };
    auto u_stat0 = [&](UOut& u, int half) {
      const u32 l = opaque<u32>(lane), sr = opaque<u32>(L_STAT + half * STH + (l & 31) * STP);
#pragma unroll
      for (int i = 0; i < 4; ++i) u.p[i] = ldsr<f32x4>(sr + 16 * i);
    };
    auto u_stat1 = [&](UOut& u) {
      const f32x4 p = (u.p[0] + u.p[1]) + (u.p[2] + u.p[3]);
      const RowStat st = finish_stat(p[0] + p[2], p[1] + p[3]);
      const float c1 = -2.0f * 0.7978845608028654f * 1.4426950408889634f, c2 = c1 * 0.044715f;
      u.nmu = opaque(-st.mu); u.k1 = opaque(c1 * st.rstd); u.k2 = opaque(c2 * st.rstd * st.rstd * st.rstd); u.ir = opaque(__builtin_amdgcn_sqrtf(st.var + LN_EPS));
    };
    auto u_cs = [&](UOut& u, int a) {
      if (a == 0) {
        const u32 l = opaque<u32>(lane);
        u.cb = opaque<u32>(L_CS + w * 128 + (l >> 5) * 16);
        u.vo = opaque<u32>(w < 7 ? l * 16 : OOB);
      }
      u.cs[a & 1] = ldsr<f32x4>(u.cb + a * 32);
    };
    auto u_store = [&](UOut& u, int half, int p) {
      if (p == 3 || p == 7) {
        const u32 so = WS_UX + (blk_of(half) * 112 + j * 14 + 2 * w) * 1024;
        if (p == 3) st16(wsr, u.vo, so, (u32x4){u.up[0], u.up[1], u.up[2], u.up[3]});
        else st16(wsr, u.vo, so + 1024, (u32x4){u.up[4], u.up[5], u.up[6], u.up[7]});
      }
    };
    auto u_quad = [&](UOut& u, int half, const f32x16& acc, int p) {
      float t[4], e[4];
#pragma unroll
      for (int i = 0; i < 4; ++i) { const int v = 2 * p + i; t[i] = opaque(__builtin_fmaf(u.nmu, u.cs[(v >> 2) & 1][v & 3], acc[v])); }
#pragma unroll
      for (int i = 0; i < 4; ++i) e[i] = __builtin_amdgcn_exp2f(t[i] * __builtin_fmaf(t[i] * t[i], u.k2, u.k1));
#pragma unroll
      for (int i = 0; i < 4; ++i) t[i] = opaque(t[i] * __builtin_amdgcn_rcpf(__builtin_fmaf(e[i], u.ir, u.ir)));
      u.up[p] = canon(cvtpk(t[0], t[1])); u.up[p + 1] = canon(cvtpk(t[2], t[3]));
      u_store(u, half, p + 1);
    };
    auto u_pair = [&](UOut& u, int half, const f32x16& acc, int p) {
      const int a = p >> 1, e = 2 * (p & 1);
      const f32x2 t = (f32x2){u.cs[a & 1][e], u.cs[a & 1][e + 1]} * u.nmu + (f32x2){acc[2 * p], acc[2 * p + 1]};
      const f32x2 ar = t * ((t * t) * u.k2 + u.k1);
      const f32x2 d = (f32x2){__builtin_amdgcn_exp2f(ar[0]), __builtin_amdgcn_exp2f(ar[1])} * u.ir + u.ir;
      const f32x2 g = t * (f32x2){__builtin_amdgcn_rcpf(d[0]), __builtin_amdgcn_rcpf(d[1])};
      u.up[p] = canon(cvtpk(g[0], g[1]));
    };
    WAITV(1);
    x1_finish(0);
    BAR();
    STAMP(19);
    dma4_sc1(ws + src_of(WS_X1, 1), gvo, lb + L_P + 32768 + w * 4096);
    const u32 xb = L_P + (w < 7 ? l16 : 0u);
    f32x16 accA = {}, accB = {};
    bf16x8 bq[4];
    UOut uA, uB;
    if (M_PRIO) __builtin_amdgcn_s_setprio(1);
    {
#pragma unroll
      for (int t = 0; t < 4; ++t) bq[t] = ldsr<bf16x8>(xb + t * 1024);
#pragma unroll
      for (int t = 0; t < 32; ++t) {
        accA = mfma32(a3[t], bq[t & 3], accA);
        if (t + 4 < 32) bq[t & 3] = ldsr<bf16x8>(xb + (t + 4) * 1024);
        if (t == 0) u_stat0(uA, 0);
        if (t == 3) u_stat1(uA);
        if (t == 29) u_cs(uA, 0);
        if (t >= 4 && (t & 3) == 0 && t / 4 - 1 < A4_N0) a4_load(t / 4 - 1);
        __builtin_amdgcn_sched_barrier(0);
      }
    }
    if (M_PRIO) __builtin_amdgcn_s_setprio(0);
    STAMP(20);
    auto gelu_half = [&](UOut& u, int half, const f32x16& acc) {
#pragma unroll
      for (int p = 0; p < 8; p += 2) {
        if (p + 2 < 8) u_cs(u, (p + 2) >> 1);
        constexpr int nr = (8 - UE_P) / 2;
        if (half == 1) {
#pragma unroll
          for (int n = 20 - A4_LATE + p / 2 * A4_LATE / 4; n < 20 - A4_LATE + (p / 2 + 1) * A4_LATE / 4; ++n) a4_load(n);
        }
        if (half == 1 && p >= UE_P) {
#pragma unroll
          for (int i = (p - UE_P) / 2 * U_INIT / (nr ? nr : 1); i < ((p - UE_P) / 2 + 1) * U_INIT / (nr ? nr : 1); ++i) u_load1(i);
        }
        if (GB_PK) { u_pair(u, half, acc, p); u_pair(u, half, acc, p + 1); u_store(u, half, p + 1); }
        else u_quad(u, half, acc, p);
        __builtin_amdgcn_sched_barrier(0);
      }
      if (half == 1 && UE_P >= 8) {
#pragma unroll
        for (int i = 0; i < U_INIT; ++i) u_load1(i);
      }
    };
    if (PQ == 0 || w < 4) gelu_half(uA, 0, accA);
    asm volatile("s_waitcnt vmcnt(%0)" :: "n"(A4_N0 + (PQ ? 0 : 2)) : "memory");
    x1_finish(1);
    BAR();
    STAMP(21);
    if (!M_PRIO) prio_late(Q_PRIO);
    if (PQ && w >= 4) gelu_half(uA, 0, accA);
    if (M_PRIO) __builtin_amdgcn_s_setprio(1);
    {
#pragma unroll
      for (int t = 0; t < 4; ++t) bq[t] = ldsr<bf16x8>(xb + 32768 + t * 1024);
#pragma unroll
      for (int t = 0; t < 32; ++t) {
        if (W3B_TOG && (t % (W3B_TOG ? W3B_TOG : 1)) == 0) prio_late(((t / (W3B_TOG ? W3B_TOG : 1)) & 1) ? 0 : 1);
        accB = mfma32(a3[t], bq[t & 3], accB);
        if (t + 4 < 32) bq[t & 3] = ldsr<bf16x8>(xb + 32768 + (t + 4) * 1024);
        if (t == 0) u_stat0(uB, 1);
        if (t == 3) u_stat1(uB);
        if (t == 29) u_cs(uB, 0);
        if (A4_SPREAD == 0) { const int n = A4_N0 + t - 1; if (t >= 1 && n < 20 - A4_LATE) a4_load(n); }
        else if (A4_SPREAD == 1) { const int n = A4_N0 + (t - 1) / 2; if ((t & 1) && n < 20 - A4_LATE) a4_load(n); }
        else { const int n = A4_N0 + (t - 1) / 4; if ((t & 3) == 1 && n < 20 - A4_LATE) a4_load(n); }
        __builtin_amdgcn_sched_barrier(0);
      }
    }
    if (M_PRIO) __builtin_amdgcn_s_setprio(0); else prio_late(GB_PRIO);
    if (!MID_BAR && lane == 0) asm volatile("ds_add_u32 %0, %1" :: "v"(lb + L_CNT + 8), "v"(1u) : "memory");
    STAMP(22);
    gelu_half(uB, 1, accB);
    STAMP(23);
  }
  {
    if (M_PRIO) { __builtin_amdgcn_s_setprio(1); if (W4_PRIO) prio_late(2); } else prio_late(W4_PRIO);
#pragma unroll
    for (int half = 0; half < 2; ++half) {
      const u32 sa = L_A4 + w * 8192 + opaque<u32>(l16);
      f32x16 acc0 = {}, acc1 = {};
      bf16x8 sq[4][2];
      auto step = [&](int s_, u32x4 u) {
        if (s_ + 2 < 14 && ord[s_ + 2] >= 10) { const int k = ord[s_ + 2] - 10; sq[k][0] = ldsr<bf16x8>(sa + k * 1024); sq[k][1] = ldsr<bf16x8>(sa + 4096 + k * 1024); }
        const int t = ord[s_];
        acc0 = mfma32(t < 10 ? a4[t < 10 ? t : 0][0] : sq[t < 10 ? 0 : t - 10][0], asbf(u), acc0);
        acc1 = mfma32(t < 10 ? a4[t < 10 ? t : 0][1] : sq[t < 10 ? 0 : t - 10][1], asbf(u), acc1);
      };
      u32 m = 0;
#pragma unroll
      for (int s_ = 0; s_ < 14; ++s_) {
        const int k = 14 * half + s_;
        step(s_, uf[k]);
        m = opaque(umax(umax(umax(m, uf[k][0]), uf[k][1]), umax(uf[k][2], uf[k][3])));
        if (k + U_WIN < 28 && k + U_WIN >= U_INIT) u_load1(k + U_WIN);
        __builtin_amdgcn_sched_barrier(0);
      }
      if (__builtin_expect(__any(m == SENT), 0)) {
        u32x4 ug[14];
        bool bad;
        do {
          __builtin_amdgcn_s_sleep(1);
          asm volatile("s_nop 7" ::: "memory");
          const u32 so = WS_UX + (blk_of(half) * 112 + w * 14) * 1024, l = opaque<u32>(l16);
#pragma unroll
          for (int i = 0; i < 14; ++i) regather1(srd, l, so + ord[i] * 1024, ug[i]);
          asm volatile("s_waitcnt vmcnt(0)" : "+v"(ug[0]), "+v"(ug[1]), "+v"(ug[2]), "+v"(ug[3]), "+v"(ug[4]), "+v"(ug[5]), "+v"(ug[6]), "+v"(ug[7]), "+v"(ug[8]), "+v"(ug[9]),
                       "+v"(ug[10]), "+v"(ug[11]), "+v"(ug[12]), "+v"(ug[13]) :: "memory");
          u32 mm = 0;
#pragma unroll
          for (int i = 0; i < 14; ++i) mm = umax(mm, max4(ug[i]));
          bad = __any(mm == SENT);
        } while (bad && !timed_out(t0));
        acc0 = (f32x16){}; acc1 = (f32x16){};
#pragma unroll
        for (int s_ = 0; s_ < 14; ++s_) step(s_, ug[s_]);
      }
      STAMP(24 + half);
      if (!MID_BAR && half == 1) {
        while (__builtin_amdgcn_readfirstlane(*(volatile LAS u32*)((LAS char*)smem + L_CNT + 8)) != 8) {
          if (timed_out(t0)) break;
          __builtin_amdgcn_s_sleep(1);
        }
        FENCE();
      }
      partials(half, acc0, acc1, pw_of(opaque<u32>(lane)));
      if (MID_BAR ? half == 0 || !OUT_EARLY : half == 1) make_sel();
      if (half == 1) STAMP(26);
      if (half == 1 || (MID_BAR && OUT_EARLY)) BAR();
      auto out_half = [&](int hf) {
        const u32 l = opaque<u32>(lane);
        const f32x4 v = reduce_at(hf, L_P + w * 4096 + l * 16) + ldsr<f32x4>(L_RES + hf * 8192 + w * 1024 + l * 16);
        *(GAS u32x2*)((char*)out + (u32)((blk_of(hf) * 32 + (l >> 1)) * 1024 + (64 * j + 8 * w + 4 * (l & 1)) * 2)) = (u32x2){cvtpk(v[0], v[1]), cvtpk(v[2], v[3])};
      };
      if (MID_BAR && OUT_EARLY) out_half(half);
      else if (half == 1) { out_half(0); out_half(1); }
      else if (MID_BAR) BAR();
      FENCE();
    }
  }
  if (MEASP >= 0) {
    const u64 te = __builtin_amdgcn_s_memrealtime();
    u64 d = MEASP == 98 ? te - t0 : tmeas - t0;
    if (MEASP == 0) d = t0 - tA;
    if (MEASP == 99) d = (__builtin_amdgcn_s_memtime() - c0) / 20;
    while (__builtin_amdgcn_s_memrealtime() - te < 4 * d) __builtin_amdgcn_s_sleep(4);
  }
  STAMP(27);
}

extern "C" void kernel_launch(void* const* d_in, const int* in_sizes, int n_in, void* d_out, int out_size, void* d_ws, size_t ws_size, hipStream_t stream) {
  (void)in_sizes; (void)n_in; (void)out_size; (void)ws_size;
  fused_block_kernel<<<256, 512, 0, stream>>>((const u16*)d_in[0], (const u16*)d_in[1], (const u16*)d_in[2], (const u16*)d_in[3], (const u16*)d_in[4],
                                              (const u16*)d_in[6], (const u16*)d_in[7], (const u16*)d_in[8], (const u16*)d_in[9], (u16*)d_out, (char*)d_ws);
}
```

```cpp
#include <hip/hip_runtime.h>
#include <stdint.h>
#include <type_traits>

typedef unsigned short u16;
typedef unsigned int u32;
typedef unsigned long long u64;
typedef short bf16x8 __attribute__((ext_vector_type(8)));
typedef short s16x4 __attribute__((ext_vector_type(4)));
typedef float f32x2 __attribute__((ext_vector_type(2)));
typedef float f32x4 __attribute__((ext_vector_type(4)));
typedef float f32x16 __attribute__((ext_vector_type(16)));
typedef u32 u32x2 __attribute__((ext_vector_type(2)));
typedef u32 u32x4 __attribute__((ext_vector_type(4)));
typedef __bf16 bf16x2_t __attribute__((ext_vector_type(2)));
#define LAS __attribute__((address_space(3)))
#define GAS __attribute__((address_space(1)))
#define DI __device__ __forceinline__

constexpr u32 WS_EPOCH = 0x0;
constexpr u32 WS_FKV = 0x1000;
constexpr u32 WS_FW = 0x5000;
constexpr u32 WS_FM = 0x9000;
constexpr u32 WS_K = 0x10000;
constexpr u32 WS_V = 0x20000;
constexpr u32 WS_DBG = 0x1E00000;
constexpr u32 WS_WO = 0x100000, WS_W1 = 0x180000, WS_W2 = 0x200000, WS_W3 = 0x280000, WS_W4 = 0x440000;
constexpr u32 WS_OX = 0x600000, WS_HX = 0x800000, WS_TS = 0xA00000, WS_X1 = 0xC00000, WS_UX = 0xE00000;
constexpr u32 WS_STAMP = 0x2000000;
constexpr u32 WS_END = 0x2100000;
constexpr u32 OOB = 0x80000000u;

constexpr u32 XP = 1040;
constexpr u32 L_XIMG = 0;
constexpr u32 L_WQ = 66560;
constexpr u32 L_WKV = 132096;
constexpr u32 S_PM = 148480, S_PC = 156672, S_PS = 157184, S_PQ = 157696, S_FIN = 158208, S_QS = 158336, S_QC = 159360;
constexpr u32 L_DUM1 = 160384;
constexpr u32 L_CNT = 161408;
constexpr u32 L_K = 0, L_V = 65536, L_ONES = 151552;
constexpr u32 L_STG = 131072;
constexpr u32 L_CS1P = 155904;
constexpr u32 L_P = 0;
constexpr u32 L_STAT = 65536;
constexpr u32 STP = 80, STH = 2560;
constexpr u32 L_CS = 71680;
constexpr u32 L_RES = 73728;
constexpr u32 L_A4 = 90112;
constexpr u32 L_STAMP = 161792;
constexpr u32 LDS_BYTES = 163840;

__shared__ __attribute__((aligned(16))) char smem[LDS_BYTES];

constexpr u32 SENT = 0xFFFFFFFFu;
constexpr float LN_EPS = 1e-5f;
constexpr float QSCALE = 0.25f * 1.4426950408889634f;
#define CODE_KB 45

DI f32x16 mfma32(bf16x8 a, bf16x8 b, f32x16 c) { return __builtin_amdgcn_mfma_f32_32x32x16_bf16(a, b, c, 0, 0, 0); }
DI f32x4 mfma16(bf16x8 a, bf16x8 b, f32x4 c) { return __builtin_amdgcn_mfma_f32_16x16x32_bf16(a, b, c, 0, 0, 0); }
DI u32 cvtpk(float lo, float hi) { f32x2 v = {lo, hi}; bf16x2_t b = __builtin_convertvector(v, bf16x2_t); return __builtin_bit_cast(u32, b); }
DI u32 canon(u32 d) { return d < 0xFFFFFFFEu ? d : 0xFFFFFFFEu; }
DI float bflo(u32 d) { return __uint_as_float(d << 16); }
DI float bfhi(u32 d) { return __uint_as_float(d & 0xFFFF0000u); }
DI float dot2(u32 a, u32 b, float c) { return __builtin_amdgcn_fdot2_f32_bf16(__builtin_bit_cast(bf16x2_t, a), __builtin_bit_cast(bf16x2_t, b), c, false); }
DI float gelu(float x) {
  const float c1 = -2.0f * 0.7978845608028654f * 1.4426950408889634f, c2 = c1 * 0.044715f;
  const float e = __builtin_amdgcn_exp2f(x * (c1 + c2 * x * x));
  return x * __builtin_amdgcn_rcpf(1.0f + e);
}
DI f32x2 gelu2(f32x2 x) {
  const float c1 = -2.0f * 0.7978845608028654f * 1.4426950408889634f, c2 = c1 * 0.044715f;
  const f32x2 a = x * ((x * x) * c2 + c1);
  const f32x2 d = (f32x2){__builtin_amdgcn_exp2f(a[0]), __builtin_amdgcn_exp2f(a[1])} + 1.0f;
  return x * (f32x2){__builtin_amdgcn_rcpf(d[0]), __builtin_amdgcn_rcpf(d[1])};
}
DI float swap32_add(float v) {
  const auto r = __builtin_amdgcn_permlane32_swap(__float_as_uint(v), __float_as_uint(v), false, false);
  return __uint_as_float(r[0]) + __uint_as_float(r[1]);
}
DI float xor1_add(float v) { return v + __int_as_float(__builtin_amdgcn_mov_dpp(__float_as_int(v), 0xB1, 0xF, 0xF, true)); }

#define BAR() asm volatile("s_waitcnt lgkmcnt(0)\n\ts_barrier" ::: "memory")
#define WAITV(n) asm volatile("s_waitcnt vmcnt(" #n ")" ::: "memory")
#define FENCE() asm volatile("" ::: "memory")
template <class T> DI T opaque(T v) { asm volatile("" : "+v"(v)); return v; }

template <class T> DI T ldsr(u32 off) { return *(const LAS T*)((LAS char*)smem + off); }
template <class T> DI void ldsw(u32 off, T v) { *(LAS T*)((LAS char*)smem + off) = v; }
DI s16x4 ldstr(u32 off) {
  typedef short v4i16_t __attribute__((ext_vector_type(4)));
  return __builtin_bit_cast(s16x4, __builtin_amdgcn_ds_read_tr16_b64_v4i16((LAS v4i16_t*)((LAS char*)smem + off)));
}
DI bf16x8 cat(s16x4 a, s16x4 b) { return __builtin_shufflevector(a, b, 0, 1, 2, 3, 4, 5, 6, 7); }
DI u32 lds_base() { return (u32)(uintptr_t)(LAS char*)smem; }

#define DMA_HEAD "s_mov_b32 %[k], m0\n\ts_mov_b32 m0, %[lds]\n\ts_nop 0\n\t"
#define DMA_TAIL "s_mov_b32 m0, %[k]"
DI void dma1(const void* sbase, u32 voff, u32 ldsdst) {
  u32 keep;
  asm volatile(DMA_HEAD "global_load_lds_dwordx4 %[v], %[s]\n\t" DMA_TAIL : [k] "=&s"(keep) : [v] "v"(voff), [s] "s"(sbase), [lds] "s"(ldsdst) : "memory");
}
DI void dma2(const void* sbase, u32 voff, u32 ldsdst) {
  u32 keep;
  asm volatile(DMA_HEAD "global_load_lds_dwordx4 %[v], %[s]\n\tglobal_load_lds_dwordx4 %[v], %[s] offset:1024\n\t" DMA_TAIL
               : [k] "=&s"(keep) : [v] "v"(voff), [s] "s"(sbase), [lds] "s"(ldsdst) : "memory");
}
DI void dma4_rows(const void* sbase, u32 v0, u32 v1, u32 v2, u32 v3, u32 ldsdst) {
  u32 keep;
  asm volatile(DMA_HEAD "global_load_lds_dwordx4 %[a], %[s]\n\tglobal_load_lds_dwordx4 %[b], %[s] offset:1040\n\t"
               "global_load_lds_dwordx4 %[c], %[s] offset:2080\n\tglobal_load_lds_dwordx4 %[d], %[s] offset:3120\n\t" DMA_TAIL
               : [k] "=&s"(keep) : [a] "v"(v0), [b] "v"(v1), [c] "v"(v2), [d] "v"(v3), [s] "s"(sbase), [lds] "s"(ldsdst) : "memory");
}
DI void dma4_sc1(const void* sbase, u32 voff, u32 ldsdst) {
  u32 keep;
  asm volatile(DMA_HEAD "global_load_lds_dwordx4 %[v], %[s] sc1\n\tglobal_load_lds_dwordx4 %[v], %[s] offset:1024 sc1\n\t"
               "global_load_lds_dwordx4 %[v], %[s] offset:2048 sc1\n\tglobal_load_lds_dwordx4 %[v], %[s] offset:3072 sc1\n\t" DMA_TAIL
               : [k] "=&s"(keep) : [v] "v"(voff), [s] "s"(sbase), [lds] "s"(ldsdst) : "memory");
}
DI void dma_pair_late(int w, const void* sbase, u32 voff, u32 ldsdst) {
  u32 keep;
  asm volatile("s_cmp_lt_u32 %[w], 4\n\ts_cbranch_scc1 .Lkv%=\n\t" DMA_HEAD "global_load_lds_dwordx4 %[a], %[s] sc1\n\ts_add_u32 m0, m0, 0x10000\n\ts_nop 0\n\t"
               "global_load_lds_dwordx4 %[b], %[s] sc1\n\t" DMA_TAIL "\n.Lkv%=:"
               : [k] "=&s"(keep) : [w] "s"(w), [a] "v"(voff), [b] "v"(voff + 0x10000u), [s] "s"(sbase), [lds] "s"(ldsdst) : "memory", "scc");
}
template <int N> DI void count_late(int w, u32 addr) {
  u64 keep;
  asm volatile("s_cmp_lt_u32 %[w], 4\n\ts_cbranch_scc1 .Lcl%=\n\ts_waitcnt vmcnt(%[n])\n\ts_mov_b64 %[k], exec\n\ts_mov_b64 exec, 1\n\tds_add_u32 %[a], %[o]\n\ts_mov_b64 exec, %[k]\n.Lcl%=:"
               : [k] "=&s"(keep) : [w] "s"(w), [n] "n"(N), [a] "v"(addr), [o] "v"(1u) : "memory", "scc");
}
DI void st16_early(int w, u32x4 srd, u32 voff, u32 soff, u32x4 v) {
  asm volatile("s_cmp_ge_u32 %[w], 4\n\ts_cbranch_scc1 .Lse%=\n\tbuffer_store_dwordx4 %[d], %[v], %[r], %[s] offen sc1\n\ts_nop 1\n.Lse%=:"
               :: [w] "s"(w), [d] "v"(v), [v] "v"(voff), [r] "s"(srd), [s] "s"(soff) : "memory", "scc");
}
#define DMA_STEP(SC) "global_load_lds_dwordx4 %[v], %[s]" SC "\n\ts_add_u32 m0, m0, %[ls]\n\tv_add_u32 %[v], %[gs], %[v]\n\t"
template <int N, bool SC1, u32 GS>
DI void dma_strided(const void* sbase, u32 voff, u32 ldsdst, u32 lstep) {
  u32 keep;
  static_assert(N == 2 || N == 3 || N == 4 || N == 8, "");
#define DMA_RUN(BODY) asm volatile(DMA_HEAD BODY DMA_TAIL : [k] "=&s"(keep), [v] "+v"(voff) : [s] "s"(sbase), [lds] "s"(ldsdst), [ls] "s"(lstep), [gs] "n"(GS) : "memory", "scc")
  if constexpr (SC1) {
    if constexpr (N == 2) DMA_RUN(DMA_STEP(" sc1") DMA_STEP(" sc1"));
    if constexpr (N == 3) DMA_RUN(DMA_STEP(" sc1") DMA_STEP(" sc1") DMA_STEP(" sc1"));
    if constexpr (N == 4) DMA_RUN(DMA_STEP(" sc1") DMA_STEP(" sc1") DMA_STEP(" sc1") DMA_STEP(" sc1"));
  } else {
    if constexpr (N == 8) DMA_RUN(DMA_STEP("") DMA_STEP("") DMA_STEP("") DMA_STEP("") DMA_STEP("") DMA_STEP("") DMA_STEP("") DMA_STEP(""));
  }
#undef DMA_RUN
}

typedef __amdgpu_buffer_rsrc_t rsrc_t;
DI rsrc_t mkrsrc(const void* p, u32 bytes) { return __builtin_amdgcn_make_buffer_rsrc((void*)p, 0, bytes, 0x00020000); }
DI u32x4 ld16(rsrc_t r, u32 voff, u32 soff) { return __builtin_amdgcn_raw_buffer_load_b128(r, voff, soff, 16); }
DI void st16(rsrc_t r, u32 voff, u32 soff, u32x4 v) {
  __builtin_amdgcn_raw_buffer_store_b128(v, r, voff, soff, 16);
  asm volatile("s_nop 1" :: "v"(v) : "memory");
}
DI void st8(rsrc_t r, u32 voff, u32 soff, u32x2 v) { __builtin_amdgcn_raw_buffer_store_b64(v, r, voff, soff, 16); }
DI void st4(rsrc_t r, u32 voff, u32 soff, u32 v) { __builtin_amdgcn_raw_buffer_store_b32(v, r, voff, soff, 16); }
DI bf16x8 asbf(u32x4 v) { return __builtin_bit_cast(bf16x8, v); }
DI u32x4 asu(bf16x8 v) { return __builtin_bit_cast(u32x4, v); }
DI void regather4(u32x4 srd, u32 voff, u32 soff, bf16x8& f0, bf16x8& f1, bf16x8& f2, bf16x8& f3) {
  asm volatile("s_sleep 1\n\tbuffer_load_dwordx4 %0, %4, %5, %6 offen sc1\n\tbuffer_load_dwordx4 %1, %4, %5, %6 offen offset:1024 sc1\n\t"
               "buffer_load_dwordx4 %2, %4, %5, %6 offen offset:2048 sc1\n\tbuffer_load_dwordx4 %3, %4, %5, %6 offen offset:3072 sc1\n\ts_waitcnt vmcnt(0)"
               : "+v"(f0), "+v"(f1), "+v"(f2), "+v"(f3) : "v"(voff), "s"(srd), "s"(soff) : "memory");
}
DI void regather1(u32x4 srd, u32 voff, u32 soff, u32x4& f0) {
  asm volatile("buffer_load_dwordx4 %0, %1, %2, %3 offen sc1" : "=&v"(f0) : "v"(voff), "s"(srd), "s"(soff) : "memory");
}
DI u32 umax(u32 a, u32 b) { return a > b ? a : b; }
DI u32 max4(u32x4 v) { return umax(umax(v[0], v[1]), umax(v[2], v[3])); }

constexpr u64 SPIN_TICKS = 400000ull;
DI bool timed_out(u64 t0) { return __builtin_amdgcn_s_memrealtime() - t0 > SPIN_TICKS; }

#define STAMP(i) do { } while (0)
#define ASTAMP(it, pos) do { } while (0)
#define PSTAMP(sb, i) do { } while (0)
#define MFMA_GAP16(v) asm volatile("s_nop 7" : "+v"(v))
#define MFMA_GAP32(v) asm volatile("s_nop 7\n\ts_nop 3" : "+v"(v))

DI bool flags_ok(u32x4 a, u32x4 b, u32 lo, u32 hi) {
  return __all(a[0] == lo && a[1] == hi && a[2] == lo && a[3] == hi && b[0] == lo && b[1] == hi && b[2] == lo && b[3] == hi);
}
DI void wait_flags(rsrc_t wsr, u32 base, u32 taglo, u32 taghi, int lane, u64 t0) {
  for (;;) {
    const u32x4 a = ld16(wsr, lane * 32, base), b = ld16(wsr, lane * 32 + 16, base);
    if (flags_ok(a, b, taglo, taghi)) break;
    if (timed_out(t0)) break;
    __builtin_amdgcn_s_sleep(1);
    FENCE();
  }
}

DI bf16x8 ones_frag() { return asbf((u32x4){0x3F803F80u, 0x3F803F80u, 0x3F803F80u, 0x3F803F80u}); }
DI float diag_pick(f32x4 g, int lane) { const int r = lane & 3; float a = (r & 1) ? g[1] : g[0], b = (r & 1) ? g[3] : g[2]; return (r & 2) ? b : a; }

struct RowStat { float mu, rstd, var; };
DI RowStat finish_stat(float s, float q) {
  RowStat r; r.mu = s * (1.0f / 512.0f);
  r.var = fmaxf(q * (1.0f / 512.0f) - r.mu * r.mu, 0.0f);
  r.rstd = __builtin_amdgcn_rsqf(r.var + LN_EPS);
  return r;
}
DI bool unwritten(const bf16x8 (&f)[4]) {
  return __any(umax(umax(max4(asu(f[0])), max4(asu(f[1]))), umax(max4(asu(f[2])), max4(asu(f[3])))) == SENT);
}

template <int I, int N, class F> DI void static_for(F&& f) {
  if constexpr (I < N) { f(std::integral_constant<int, I>{}); static_for<I + 1, N>(f); }
}
struct Job { const char* src; u32 prow; u32 f; bool perm; };
template <int I> DI Job retile_job(int b, int w, const u16* wo, const u16* w1, const u16* w2, const u16* w3, const u16* w4) {
  auto small = [&](const u16* m, u32 g, u32 f0, bool perm) { return Job{(const char*)m + (u32)(16 * (g & 31) * 1024 + (g >> 5) * 64), 1024u, f0 + g, perm}; };
  auto big3 = [&](u32 g) { return Job{(const char*)w3 + (u32)(16 * (g & 31) * 3584 + (g >> 5) * 64), 3584u, 1536 + g, false}; };
  auto big4 = [&](u32 mm) { return Job{(const char*)w4 + (u32)(16 * ((b & 15) * 7 + mm) * 1024 + (b >> 4) * 64), 1024u, 3328 + 7 * b + mm, true}; };
  if constexpr (I == 0) return small(w < 2 ? wo : w1, 2 * b + (w & 1), w < 2 ? 0 : 512, w < 2);
  else if constexpr (I == 1) { const Job s = small(w2, 2 * b + (w & 1), 1024, false), t = big3(7 * b + (w & 1)); return w < 2 ? s : t; }
  else if constexpr (I == 2) return big3(7 * b + 2 + w);
  else if constexpr (I == 3) { const Job s = big3(7 * b + 6), t = big4(w - 1); return w == 0 ? s : t; }
  else return big4(3 + w);
}

#define ATT_PIPE 2
#define PRIO_IT 12
#define KV_SPREAD 2
#define CNT_U 2
#define NOMARK 0
#define MARK_IT 1
#define FLAG_IT 5
#define MARK_P1 0
#define MARK_ST 2
#define FM_IT 10
#define FM_CHK 14
#define KVW 4
#define P1_BAR 0
#define FLAG_POS 0
#define DLY -1
#define DELAY(p) do { if (DLY == (p)) { const u64 d0_ = __builtin_amdgcn_s_memrealtime(); while (__builtin_amdgcn_s_memrealtime() - d0_ < 200) __builtin_amdgcn_s_sleep(1); } } while (0)
#define MEASP 6
#define MEAS(p) do { if (MEASP == (p)) tmeas = __builtin_amdgcn_s_memrealtime(); } while (0)
#define OUT_AUX -1
#define PRE_POS 0
#define PUT16 0
#define GB_POS 1
#define O_SLEEP 16
#define SLOT_POS 0
#define W4_WARM 0
#define FPI 6
#define U_IN 0
#define W4_PRIO Q_PRIO
#define A4_SPREAD 1
#define W3B_TOG 0
#define GB_PRIO Q_PRIO
#define UE_P 0
#define U_WIN 5
#define OUT_EARLY 1
#define MID_BAR 0
#define PQ 1
#define Q_PRIO 1
#ifndef M_PRIO
#define M_PRIO 1
#endif
#define U_INIT U_WIN
#define A4_LATE 0
#define STAT_POS 0
#define GB_PK 0
#define A3_N0 14
static_assert(33 + A3_N0 + (W4_WARM == 1 ? 7 : 0) <= 8 * FPI, "fetch schedule");
#define A4_N0 6

constexpr int marks_before(int it) { const int n = it <= MARK_IT ? 0 : (it - MARK_IT) * (4 / MARK_ST); return n < 15 ? n : 15; }
static_assert(MARK_P1 != 2 || (marks_before(FM_IT - 1) == 15 && FM_CHK > FM_IT && FM_CHK > 8 && FLAG_IT < 8), "mark schedule");

__global__ __launch_bounds__(512, 2) void fused_block_kernel(const u16* __restrict__ x, const u16* __restrict__ wq, const u16* __restrict__ wk,
                                                             const u16* __restrict__ wv, const u16* __restrict__ wo, const u16* __restrict__ w1,
                                                             const u16* __restrict__ w2, const u16* __restrict__ w3, const u16* __restrict__ w4,
                                                             u16* __restrict__ out, char* ws) {
  const u64 tA = MEASP == 0 ? __builtin_amdgcn_s_memrealtime() : 0;
  if (MEASP == 0) asm volatile("" :: "s"(tA));
  const int tid = threadIdx.x, lane = tid & 63, w = __builtin_amdgcn_readfirstlane(tid >> 6);
  const int b = blockIdx.x;
  const int cl = (b & 7) * 4 + (b >> 6), j = (b >> 3) & 7;
  const u32 lb = lds_base();
  const u32 l16 = lane * 16;
  {
    u64 pc;
    asm volatile("s_getpc_b64 %0" : "=s"(pc));
    dma1((const void*)(pc & ~63ull), (u32)((((b >> 3) * 8 + w) % CODE_KB) * 1024) + l16, lb + L_DUM1);
  }
  asm volatile("" :: "s"(x), "s"(wq), "s"(wk), "s"(wv), "s"(wo), "s"(w1), "s"(w2), "s"(w3), "s"(w4), "s"(out), "s"(ws));
  const u64 t0 = __builtin_amdgcn_s_memrealtime();
  u64 tmeas = t0;
  const u64 c0 = MEASP == 99 ? __builtin_amdgcn_s_memtime() : 0;
  const int i16 = lane & 15, g4 = lane >> 4, r32 = lane & 31, h2 = lane >> 5;
  STAMP(0);

  auto blk_of = [&](int half) { return half * 32 + cl; };
  const int dlt = (j >> 1) == 0 ? 1 : (j >> 1) == 1 ? -1 : (j >> 1) == 2 ? 2 : -2;
  const int kvrt = j >> 1, kvmat = j & 1;
  auto grow = [&](int rho) { return (rho < 32 ? 0 : 1024 - 32) + 32 * cl + rho; };
  const int R0 = grow(16 * kvrt);
  u32x4 xk0, xk1;
  u64 tag;
  {
    const u32 xo = (u32)(i16 * 1024 + g4 * 32);
    asm volatile("global_load_dwordx4 %0, %2, %3\n\tglobal_load_dwordx4 %1, %2, %3 offset:16"
                 : "=&v"(xk0), "=&v"(xk1) : "v"(xo), "s"((const char*)x + R0 * 1024 + w * 128) : "memory");
    dma2((const char*)(kvmat ? wv : wk) + w * 2048, l16, lb + L_WKV + w * 2048);
    asm volatile("s_load_dwordx2 %0, %1, 0x0" : "=s"(tag) : "s"(ws + WS_EPOCH) : "memory");
    if (P1_BAR) asm volatile("s_barrier" ::: "memory");
    const char* xr = (const char*)x + grow(8 * w) * 1024 - 48;
    dma4_rows(xr, l16 + 48, l16 + 32, l16 + 16, l16, lb + L_XIMG + 8 * w * XP);
    dma4_rows(xr + 4096, l16 + 48, l16 + 32, l16 + 16, l16, lb + L_XIMG + (8 * w + 4) * XP);
    dma_strided<8, false, 0x2000>((const char*)wq + w * 65536 + j * 128, (u32)((lane >> 3) * 1024 + (((lane & 7) ^ (4 * ((lane >> 4) & 1))) * 16)), lb + L_WQ + w * 8192, 1024);
    asm volatile("s_waitcnt vmcnt(16) lgkmcnt(0)" : "+v"(xk0), "+v"(xk1), "+s"(tag) :: "memory");
  }
  tag += 1ull;
  const u32 taglo = (u32)tag, taghi = (u32)(tag >> 32);
  const rsrc_t wsr = mkrsrc(ws, WS_END);
  const u32x4 srd = {(u32)(uintptr_t)ws, (u32)((uintptr_t)ws >> 32) & 0xFFFFu, WS_END, 0x00020000u};
  const bf16x8 one = ones_frag();
  STAMP(1);
  MEAS(1);
  DELAY(6);

  {
    f32x4 am = {0, 0, 0, 0}, ac = {0, 0, 0, 0}, as = {0, 0, 0, 0}, aq = {0, 0, 0, 0};
    const u32 wa = L_WKV + w * 2048 + (16 * g4 + (i16 >> 2)) * 32 + (i16 & 3) * 8;
    const bf16x8 wf0 = cat(ldstr(wa), ldstr(wa + 128)), wf1 = cat(ldstr(wa + 256), ldstr(wa + 384));
    const bf16x8 xf0 = asbf(xk0), xf1 = asbf(xk1);
    as = mfma16(one, xf0, as); aq = mfma16(xf0, xf0, aq); as = mfma16(one, xf1, as); aq = mfma16(xf1, xf1, aq);
    if (kvmat) {
      am = mfma16(xf0, wf0, am); ac = mfma16(one, wf0, ac); am = mfma16(xf1, wf1, am); ac = mfma16(one, wf1, ac);
      MFMA_GAP16(am); MFMA_GAP16(ac);
      if (lane < 16) ldsw<float>(S_PC + (w * 16 + lane) * 4, ac[0]);
    } else {
      am = mfma16(wf0, xf0, am); ac = mfma16(wf0, one, ac); am = mfma16(wf1, xf1, am); ac = mfma16(wf1, one, ac);
      MFMA_GAP16(am); MFMA_GAP16(ac);
      if (i16 == 0) ldsw<f32x4>(S_PC + (w * 16 + 4 * g4) * 4, ac);
    }
    ldsw<f32x4>(S_PM + w * 1024 + l16, am);
    if (lane < 16) ldsw<float>(S_PS + (lane * 8 + w) * 4, as[0]);
    if ((i16 >> 2) == g4) ldsw<float>(S_PQ + (i16 * 8 + w) * 4, diag_pick(aq, lane));
    BAR();
  }
  if (w == KVW) {
    {
      const f32x4 s0 = ldsr<f32x4>(S_PS + i16 * 32), s1 = ldsr<f32x4>(S_PS + i16 * 32 + 16);
      const f32x4 q0 = ldsr<f32x4>(S_PQ + i16 * 32), q1 = ldsr<f32x4>(S_PQ + i16 * 32 + 16);
      const f32x4 s = s0 + s1, q = q0 + q1;
      const RowStat st = finish_stat((s[0] + s[1]) + (s[2] + s[3]), (q[0] + q[1]) + (q[2] + q[3]));
      if (lane < 16) { ldsw<float>(S_FIN + lane * 4, st.mu); ldsw<float>(S_FIN + 64 + lane * 4, st.rstd); }
    }
    f32x4 m = {0, 0, 0, 0};
#pragma unroll
    for (int ww = 0; ww < 8; ++ww) m += ldsr<f32x4>(S_PM + ww * 1024 + l16);
    f32x4 v;
    u32 off;
    if (kvmat) {
      float c1 = 0.f;
#pragma unroll
      for (int ww = 0; ww < 8; ++ww) c1 += ldsr<float>(S_PC + (ww * 16 + i16) * 4);
      asm volatile("s_waitcnt lgkmcnt(0)" ::: "memory");
      const f32x4 mu = ldsr<f32x4>(S_FIN + g4 * 16), rs = ldsr<f32x4>(S_FIN + 64 + g4 * 16);
      v = rs * (m - mu * c1);
      off = WS_V + (R0 >> 5) * 1024 + ((R0 >> 4) & 1) * 512 + (g4 & 1) * 256 + i16 * 16 + (g4 >> 1) * 8;
    } else {
      f32x4 c4 = {0, 0, 0, 0};
#pragma unroll
      for (int ww = 0; ww < 8; ++ww) c4 += ldsr<f32x4>(S_PC + (ww * 16 + 4 * g4) * 4);
      asm volatile("s_waitcnt lgkmcnt(0)" ::: "memory");
      const float mu = ldsr<float>(S_FIN + i16 * 4), rs = ldsr<float>(S_FIN + 64 + i16 * 4);
      v = rs * (m - mu * c4);
      off = WS_K + (R0 >> 5) * 1024 + (g4 & 1) * 512 + ((R0 & 31) + i16) * 16 + (g4 >> 1) * 8;
    }
    st8(wsr, off, 0, (u32x2){cvtpk(v[0], v[1]), cvtpk(v[2], v[3])});
    WAITV(1);
  } else if (MARK_P1 == 1 && w >= 4) {
      const rsrc_t mr = mkrsrc(ws, WS_END);
      const u32x4 s4 = {SENT, SENT, SENT, SENT};
      const int wm = w & 3;
#pragma unroll
      for (int half = 0; half < 2; ++half) {
        const u32 po = blk_of(half) * 32768 + (4 * j + wm) * 1024;
        __builtin_amdgcn_raw_buffer_store_b128(s4, mr, l16, WS_OX + po, 16);
        __builtin_amdgcn_raw_buffer_store_b128(s4, mr, l16, WS_HX + po, 16);
        __builtin_amdgcn_raw_buffer_store_b128(s4, mr, l16, WS_X1 + po, 16);
        const int R = blk_of(half) * 32 + (lane & 31) + dlt;
        const u32 mk = (u32)R < 2048u ? SENT : 0u;
        st16(mr, ((R & 2047) >> 5) * 32768 + (lane >> 5) * 512 + (R & 31) * 16, WS_TS + (4 * j + wm) * 1024, (u32x4){mk, mk, mk, mk});
      }
      const u32 uo = WS_UX + (blk_of(wm >> 1) * 112 + 14 * j + 7 * (wm & 1)) * 1024;
#pragma unroll
      for (int i = 0; i < 7; ++i) st16(mr, l16 + (i & 3) * 1024, uo + (i >> 2) * 4096, s4);
    WAITV(15);
  } else {
    WAITV(0);
  }
#define JOB(i) retile_job<i>(b, w, wo, w1, w2, w3, w4)
  u32x4 rts[5];
  if (w < 4) {
    static_for<0, 5>([&](auto I) {
      const Job jb = JOB(I.value);
      rts[I.value] = *(const GAS u32x4*)(jb.src + (u32)((lane >> 2) * jb.prow + (lane & 3) * 16));
    });
  }
  FENCE();
  STAMP(2);
  const int q_hp = w & 1, q_rt = (w >> 1) & 1, q_kh = w >> 2;
  const int head = 2 * q_hp + q_kh;
  bf16x8 qf;
  {
    BAR();
    STAMP(3);
    MEAS(3);
    DELAY(1);
    {
      f32x4 as = {0, 0, 0, 0}, aq = {0, 0, 0, 0};
      const int rt = w & 3, kh = w >> 2;
      const u32 sa = L_XIMG + (16 * rt + i16) * XP + kh * 512 + g4 * 16;
#pragma unroll
      for (int t = 0; t < 8; ++t) {
        const bf16x8 xf = ldsr<bf16x8>(sa + t * 64);
        as = mfma16(one, xf, as); aq = mfma16(xf, xf, aq);
      }
      MFMA_GAP16(as); MFMA_GAP16(aq);
      if (lane < 16) ldsw<float>(S_QS + ((16 * rt + lane) * 2 + kh) * 4, as[0]);
      if ((i16 >> 2) == g4) ldsw<float>(S_QS + 512 + ((16 * rt + i16) * 2 + kh) * 4, diag_pick(aq, lane));
    }
    auto kv_flag = [&]() {
      if (w == KVW) {
        if (KVW < 4) WAITV(5); else WAITV(0);
        if (lane < 8) __hip_atomic_store((GAS u64*)(ws + WS_FKV + lane * 2048 + b * 8), tag, __ATOMIC_RELAXED, __HIP_MEMORY_SCOPE_AGENT);
      }
    };
    if (FLAG_POS == 0) kv_flag();
    if (tid < 272) ldsw<u32x4>(L_ONES + tid * 16, (u32x4){0x3F803F80u, 0x3F803F80u, 0x3F803F80u, 0x3F803F80u});
    if (tid == 511) ldsw<u32x4>(L_CNT, (u32x4){0u, 0u, 0u, 0u});
    f32x16 acc = {}, accc = {};
    const int q = i16 >> 2, p = i16 & 3, cb = g4 & 1;
    const u32 wa = L_WQ + (256 * q_kh + 8 * h2 + q) * 128 + (((4 * q_hp + 2 * cb + (p >> 1)) ^ (4 * ((q >> 1) & 1))) * 16) + (p & 1) * 8;
    const u32 xa = L_XIMG + (32 * q_rt + r32) * XP + q_kh * 512 + h2 * 16;
#pragma unroll
    for (int t = 0; t < 16; ++t) {
      const bf16x8 af = cat(ldstr(wa + t * 2048), ldstr(wa + t * 2048 + 512));
      acc = mfma32(af, ldsr<bf16x8>(xa + t * 32), acc);
      if (FLAG_POS == 1 && t == 3) kv_flag();
    }
    u32x4 fka, fkb;
    if (w == 1) { fka = ld16(wsr, lane * 32, WS_FKV + (b & 7) * 2048); fkb = ld16(wsr, lane * 32 + 16, WS_FKV + (b & 7) * 2048); }
    const u32 wb = wa + q_rt * 16384;
#pragma unroll
    for (int t = 0; t < 8; ++t) accc = mfma32(cat(ldstr(wb + t * 2048), ldstr(wb + t * 2048 + 512)), one, accc);
    if (FLAG_POS == 2) kv_flag();
    MFMA_GAP32(acc); MFMA_GAP32(accc);
    f32x4 k0, k1, s0, s1;
    if (q_kh) {
      k0 = (f32x4){acc[8], acc[9], acc[10], acc[11]}; k1 = (f32x4){acc[12], acc[13], acc[14], acc[15]};
      s0 = (f32x4){acc[0], acc[1], acc[2], acc[3]}; s1 = (f32x4){acc[4], acc[5], acc[6], acc[7]};
    } else {
      k0 = (f32x4){acc[0], acc[1], acc[2], acc[3]}; k1 = (f32x4){acc[4], acc[5], acc[6], acc[7]};
      s0 = (f32x4){acc[8], acc[9], acc[10], acc[11]}; s1 = (f32x4){acc[12], acc[13], acc[14], acc[15]};
    }
    ldsw<f32x4>(L_WKV + w * 2048 + l16, s0); ldsw<f32x4>(L_WKV + w * 2048 + 1024 + l16, s1);
    if (r32 == 0) {
#pragma unroll
      for (int a = 0; a < 4; ++a) ldsw<f32x4>(S_QC + (((q_hp * 2 + q_rt) * 2 + q_kh) * 32 + 8 * a + 4 * h2) * 4, (f32x4){accc[4 * a], accc[4 * a + 1], accc[4 * a + 2], accc[4 * a + 3]});
    }
    if (w == 1) {
      if (!flags_ok(fka, fkb, taglo, taghi)) wait_flags(wsr, WS_FKV + (b & 7) * 2048, taglo, taghi, lane, t0);
      if (b == 0 && lane == 0) __hip_atomic_store((GAS u64*)(ws + WS_EPOCH), tag, __ATOMIC_RELAXED, __HIP_MEMORY_SCOPE_AGENT);
    }
    BAR();
    STAMP(5);
    MEAS(5);
    dma_strided<KV_SPREAD ? 2 : 3, true, 0x2000>(ws + WS_K, w * 1024 + l16, lb + L_K + w * 1024, 8192);
    dma_strided<2, true, 0x2000>(ws + WS_V, w * 1024 + l16, lb + L_V + w * 1024, 8192);
    k0 += ldsr<f32x4>(L_WKV + (w ^ 4) * 2048 + l16); k1 += ldsr<f32x4>(L_WKV + (w ^ 4) * 2048 + 1024 + l16);
    f32x4 c0 = {0, 0, 0, 0}, c1 = {0, 0, 0, 0};
#pragma unroll
    for (int s = 0; s < 4; ++s) {
      c0 += ldsr<f32x4>(S_QC + ((q_hp * 4 + s) * 32 + 16 * q_kh + 4 * h2) * 4);
      c1 += ldsr<f32x4>(S_QC + ((q_hp * 4 + s) * 32 + 16 * q_kh + 8 + 4 * h2) * 4);
    }
    const f32x2 sp = ldsr<f32x2>(S_QS + (32 * q_rt + r32) * 8), qp = ldsr<f32x2>(S_QS + 512 + (32 * q_rt + r32) * 8);
    const RowStat st = finish_stat(sp[0] + sp[1], qp[0] + qp[1]);
    const float sc = st.rstd * QSCALE;
    k0 = (k0 - st.mu * c0) * sc; k1 = (k1 - st.mu * c1) * sc;
    qf = opaque(asbf((u32x4){cvtpk(k0[0], k0[1]), cvtpk(k0[2], k0[3]), cvtpk(k1[0], k1[1]), cvtpk(k1[2], k1[3])}));
  }
  STAMP(4);
  const u32 pc_in = (u32)((4 * j + (w >> 1)) * 1024 + (w & 1) * 512);
  auto hx_so = [&](u32 base, int half) { return base + blk_of(half) * 32768 + pc_in; };
  auto ts_vo = [&](int half) {
    const u32 l = opaque<u32>(lane);
    const int rr = (int)(l >> 1) + dlt;
    const u32 R = (u32)(blk_of(half) * 32 + rr);
    const u32 o = (u32)((rr >> 5) + 1) * 32768 + (u32)(rr & 31) * 16 + (l & 1) * 8;
    return R < 2048u && !(PUT16 && (l & 1)) ? o : OOB;
  };
  auto ox_off = [&]() { return WS_OX + blk_of(q_rt) * 32768 + (4 * j + head) * 1024 + l16; };
  {
    WAITV(0);
    asm volatile("" : "+v"(rts[0]), "+v"(rts[1]), "+v"(rts[2]), "+v"(rts[3]), "+v"(rts[4]));
    BAR();
    STAMP(6);
    MEAS(6);
    DELAY(2);
  }
  u32 opk[4];
  bf16x8 a_o[8], a_1[8], a_2[8], a3[32];
  u32x2 xres[2];
  {
    u32 ka = L_K + l16;
    const bool isv = r32 < 16;
    u32 va = isv ? L_V + h2 * 256 + r32 * 16 : L_ONES + ((r32 >= 20 && r32 < 28) ? 64 : 0);
    const u32 vinc = isv ? 4096u : 0u;
    f32x16 o = {};
    f32x16 negm;
    {
      const bf16x8 kf = ldsr<bf16x8>(ka);
      f32x16 z = {};
      const f32x16 s = mfma32(kf, qf, z);
      float m = fmaxf(fmaxf(s[0], s[1]), fmaxf(s[2], s[3]));
#pragma unroll
      for (int i = 4; i < 16; i += 2) m = fmaxf(m, fmaxf(s[i], s[i + 1]));
      const auto rr = __builtin_amdgcn_permlane32_swap(__float_as_uint(m), __float_as_uint(m), false, false);
      m = fmaxf(__uint_as_float(rr[0]), __uint_as_float(rr[1]));
#pragma unroll
      for (int i = 0; i < 16; ++i) negm[i] = -m;
    }
    f32x16 s = mfma32(ldsr<bf16x8>(ka), qf, negm);
    bf16x8 kf = ldsr<bf16x8>(ka + 1024), v0 = ldsr<bf16x8>(va), v1 = ldsr<bf16x8>(va + 512);
#define SB() __builtin_amdgcn_sched_barrier(0)
    auto tiles4 = [&](auto&& hook) {
#pragma unroll
      for (int u = 0; u < 4; ++u) {
        float e[16];
        u32 pk[8];
        f32x16 sn;
        auto ex = [&](int a, int b_) {
#pragma unroll
          for (int i = a; i < b_; ++i) e[i] = opaque(__builtin_amdgcn_exp2f(s[i]));
        };
        auto cv = [&](int a, int b_) {
#pragma unroll
          for (int i = a; i < b_; ++i) pk[i] = opaque(cvtpk(e[2 * i], e[2 * i + 1]));
        };
        auto ms = [&]() { sn = mfma32(kf, qf, negm); asm volatile("" : "+v"(sn)); kf = ldsr<bf16x8>(ka + (u + 2) * 1024); };
        if (u == 3) va += vinc;
        const u32 vn = va + ((u + 1) & 3) * 1024;
        auto m0 = [&]() { o = mfma32(v0, asbf((u32x4){pk[0], pk[1], pk[2], pk[3]}), o); asm volatile("" : "+v"(o)); v0 = ldsr<bf16x8>(vn); };
        auto m1 = [&]() { o = mfma32(v1, asbf((u32x4){pk[4], pk[5], pk[6], pk[7]}), o); asm volatile("" : "+v"(o)); v1 = ldsr<bf16x8>(vn + 512); };
        if (ATT_PIPE == 1) {
          ex(0, 4); SB(); ms(); hook(u); SB(); ex(4, 8); cv(0, 4); SB(); m0(); SB(); ex(8, 16); cv(4, 8); SB(); m1(); SB();
        } else if (ATT_PIPE == 2) {
          ex(0, 4); SB(); ms(); hook(u); SB(); ex(4, 8); cv(0, 4); ex(8, 10); SB(); m0(); SB(); ex(10, 16); cv(4, 8); SB(); m1(); SB();
        } else if (ATT_PIPE == 3) {
          ms(); hook(u); SB(); ex(0, 8); cv(0, 4); SB(); m0(); SB(); ex(8, 16); cv(4, 8); SB(); m1(); SB();
        } else if (ATT_PIPE == 4) {
          ex(0, 6); cv(0, 2); SB(); ms(); hook(u); SB(); ex(6, 8); cv(2, 4); ex(8, 12); SB(); m0(); SB(); ex(12, 16); cv(4, 8); SB(); m1(); SB();
        } else {
          ex(0, 4); cv(0, 2); SB(); ms(); hook(u); SB(); ex(4, 8); cv(2, 4); SB(); m0(); SB(); ex(8, 12); cv(4, 6); ex(12, 16); cv(6, 8); SB(); m1(); SB();
        }
        s = sn;
      }
      ka += 4096;
    };
    if (w < 4) __builtin_amdgcn_s_setprio(2); else __builtin_amdgcn_s_setprio(1);
    auto kv_rest = [&](u32 img, u32 limg, int c0, auto n) {
      dma_strided<n.value, true, 4096>(ws + img + c0 * 8192, opaque<u32>((w & 3) * 1024 + l16), lb + limg + c0 * 8192 + (w & 3) * 1024, 4096);
    };
    using N2 = std::integral_constant<int, 2>; using N4 = std::integral_constant<int, 4>;
    auto lds_count = [&](u32 off) {
      u32 n;
      asm volatile("ds_add_rtn_u32 %0, %1, %2\n\ts_waitcnt lgkmcnt(0)" : "=v"(n) : "v"(lb + off), "v"(lane == 0 ? 1u : 0u) : "memory");
      return (u32)__builtin_amdgcn_readfirstlane(n);
    };
    const u32x4 s4 = {SENT, SENT, SENT, SENT};
    const u32 kvo = opaque<u32>((w & 3) * 1024 + l16);
    static_assert(WS_V - WS_K == 0x10000 && L_V - L_K == 0x10000, "K and V images");
    u32x4 fa, fb;
    auto fetch = [&](int p, u32 l) {
      const u32 fo = (2 * j * 32 + 4 * w) * 1024;
      if (p < 24) {
        const int t = p & 7;
        const bf16x8 v = asbf(ld16(wsr, l + (t & 3) * 1024, (p < 8 ? WS_WO : p < 16 ? WS_W1 : WS_W2) + fo + (t >> 2) * 32768));
        if (p < 8) a_o[t] = v; else if (p < 16) a_1[t] = v; else a_2[t] = v;
      } else if (p < 26) {
        xres[p - 24] = *(const GAS u32x2*)((const char*)x + (u32)((blk_of(p - 24) * 32 + (l >> 5)) * 1024 + (64 * j + 8 * w) * 2 + ((l >> 4) & 1) * 8));
      } else if (p < 26 + A3_N0) {
        a3[p - 26] = asbf(ld16(wsr, w < 7 ? ((7 * j + w) * 32) * 1024 + l : OOB, WS_W3 + (p - 26) * 1024));
      } else if (p < 33 + A3_N0) {
        dma1(ws + WS_W3 + (p - 26 - A3_N0) * 1024, (((u32)(b >> 6) & 3u) * 8 + w) * 7168 + j * 229376 + l, lb + L_DUM1);
      } else if (W4_WARM == 1 && p < 40 + A3_N0) {
        dma1(ws + WS_W4 + (p - 33 - A3_N0) * 1024, (((u32)(b >> 6) & 3u) * 8 + w) * 7168 + j * 229376 + l, lb + L_DUM1);
      }
    };
    static_for<0, 16>([&](auto IT) {
      constexpr int it = IT.value;
      ASTAMP(it, 0);
      if (it == 6) DELAY(3);
      if (it == 10) DELAY(4);
      if (w < 4) {
        if (it == 0) {
          const u32 l = opaque<u32>(lane);
          const u32 sb = L_STG + w * 5120;
#pragma unroll
          for (int i = 0; i < 5; ++i) ldsw<u32x4>(sb + i * 1024 + l * 16, rts[i]);
          const u32 tb = sb + ((l & 15) >> 2) * 64 + ((l >> 4) & 1) * 32 + (l & 3) * 8;
          static_for<0, 5>([&](auto I) {
            const Job jb = JOB(I.value);
            const u32 t0a = tb + I.value * 1024 + (l >> 5) * (jb.perm ? 256 : 512);
            st16(wsr, l * 16, WS_WO + jb.f * 1024, asu(cat(ldstr(t0a), ldstr(t0a + (jb.perm ? 512 : 256)))));
          });
        } else if (it == MARK_IT && !NOMARK && MARK_P1 == 0) {
          const u32 l = opaque<u32>(lane);
#pragma unroll
          for (int half = 0; half < 2; ++half) {
            const u32 po = blk_of(half) * 32768 + (4 * j + w) * 1024;
            st16(wsr, l * 16, WS_OX + po, s4); st16(wsr, l * 16, WS_HX + po, s4); st16(wsr, l * 16, WS_X1 + po, s4);
            const int R = blk_of(half) * 32 + (int)(l & 31) + dlt;
            const u32 mk = (u32)R < 2048u ? SENT : 0u;
            st16(wsr, ((R & 2047) >> 5) * 32768 + (l >> 5) * 512 + (R & 31) * 16, WS_TS + (4 * j + w) * 1024, (u32x4){mk, mk, mk, mk});
          }
        } else if (it == MARK_IT + 1 && !NOMARK && MARK_P1 == 0) {
          const u32 uo = WS_UX + (blk_of(w >> 1) * 112 + 14 * j + 7 * (w & 1)) * 1024;
          const u32 l = opaque<u32>(l16);
#pragma unroll
          for (int i = 0; i < 7; ++i) st16(wsr, l + (i & 3) * 1024, uo + (i >> 2) * 4096, s4);
        } else if (it == FLAG_IT) {
          asm volatile("s_waitcnt vmcnt(%0)" :: "n"(MARK_P1 == 2 ? marks_before(FLAG_IT) : 0) : "memory");
          if (lds_count(L_CNT + 4) == 3 && lane < 8) __hip_atomic_store((GAS u64*)(ws + WS_FW + lane * 2048 + b * 8), tag, __ATOMIC_RELAXED, __HIP_MEMORY_SCOPE_AGENT);
        } else if (MARK_P1 == 2 && it == FM_IT) {
          asm volatile("s_waitcnt vmcnt(%0)" :: "n"(FM_IT - 1 >= 8 ? FPI : 0) : "memory");
          if (lds_count(L_CNT + 12) == 3 && lane < 8) __hip_atomic_store((GAS u64*)(ws + WS_FM + lane * 2048 + b * 8), tag, __ATOMIC_RELAXED, __HIP_MEMORY_SCOPE_AGENT);
        }
      } else {
        if (KV_SPREAD) {
          if (KV_SPREAD == 1 && it >= 1 && it <= 3) {
            WAITV(0);
            if (lane == 0) asm volatile("ds_add_u32 %0, %1" :: "v"(lb + L_CNT), "v"(1u) : "memory");
          }
        } else if (it == 0) {
          kv_rest(WS_V, L_V, 2, N4{}); kv_rest(WS_K, L_K, 3, N4{}); kv_rest(WS_V, L_V, 4, N4{}); kv_rest(WS_K, L_K, 5, N4{});
          kv_rest(WS_V, L_V, 6, N4{}); kv_rest(WS_K, L_K, 7, N2{});
        } else if (it == 1) {
          WAITV(0);
          lds_count(L_CNT);
        }
      }
      if (KV_SPREAD ? (it & 3) == 3 && it < 12 : it == (ATT_PIPE ? 3 : 4)) {
        while ((u32)__builtin_amdgcn_readfirstlane(*(volatile LAS u32*)((LAS char*)smem + L_CNT)) < (KV_SPREAD ? (u32)(it + 1) : 4u)) {
          if (timed_out(t0)) break;
          __builtin_amdgcn_s_sleep(1);
        }
        FENCE();
      }
      if (it == 7) {
        const u32 l = opaque<u32>(l16);
        fa = ld16(wsr, l * 2, WS_FW + (b & 7) * 2048); fb = ld16(wsr, l * 2 + 16, WS_FW + (b & 7) * 2048);
      }
      if (it == 8) {
        if (!flags_ok(fa, fb, taglo, taghi)) wait_flags(wsr, WS_FW + (b & 7) * 2048, taglo, taghi, lane, t0);
      }
      if (MARK_P1 == 2 && it == FM_CHK - 1) {
        const u32 l = opaque<u32>(l16);
        fa = ld16(wsr, l * 2, WS_FM + (b & 7) * 2048); fb = ld16(wsr, l * 2 + 16, WS_FM + (b & 7) * 2048);
      }
      if (MARK_P1 == 2 && it == FM_CHK) {
        if (!flags_ok(fa, fb, taglo, taghi)) wait_flags(wsr, WS_FM + (b & 7) * 2048, taglo, taghi, lane, t0);
      }
      if (it >= 8) {
        const u32 l = opaque<u32>(l16);
#pragma unroll
        for (int p = FPI * (it - 8); p < FPI * (it - 7); ++p) fetch(p, l);
      }
      if (it == PRIO_IT) asm volatile("s_cmp_ge_u32 %0, 4\n\ts_cbranch_scc1 .Lpe%=\n\ts_setprio 0\n.Lpe%=:" :: "s"(w) : "scc");
      FENCE();
      ASTAMP(it, 1);
      tiles4([&](int u) {
        if (KV_SPREAD == 2 && it >= 1 && it <= 3 && u == CNT_U) count_late<(it < 3 ? 2 * CNT_U : 0)>(w, lb + L_CNT);
        if (MARK_P1 == 2 && it >= MARK_IT && (u % MARK_ST) == 0) {
          const int n = marks_before(it) + u / MARK_ST;
          if (n < 8) {
            const int half = n >> 2, kind = n & 3;
            const u32 l = opaque<u32>(lane);
            if (kind < 3) st16_early(w, srd, l * 16, (kind == 0 ? WS_OX : kind == 1 ? WS_HX : WS_X1) + blk_of(half) * 32768 + (4 * j + w) * 1024, s4);
            else {
              const int R = blk_of(half) * 32 + (int)(l & 31) + dlt;
              const u32 mk = (u32)R < 2048u ? SENT : 0u;
              st16_early(w, srd, ((R & 2047) >> 5) * 32768 + (l >> 5) * 512 + (R & 31) * 16, WS_TS + (4 * j + w) * 1024, (u32x4){mk, mk, mk, mk});
            }
          } else if (n < 15) {
            const int i = n - 8;
            st16_early(w, srd, opaque<u32>(l16), WS_UX + (blk_of(w >> 1) * 112 + 14 * j + 7 * (w & 1) + i) * 1024, s4);
          }
        }
        if (KV_SPREAD && it < 3) {
          const int T = 16 + 16 * it + 4 * u;
          dma_pair_late(w, ws + WS_K + T * 1024, kvo, lb + L_K + T * 1024 + (w & 3) * 1024);
        }
      });
      FENCE();
      if (it == 9) STAMP(7);
    });
    STAMP(8);
    MEAS(8);
    __builtin_amdgcn_s_setprio(0);
    ka = L_K + l16;
    float l = o[8];
    if (!__all(l <= 1.8446744e19f)) {
      float m = -INFINITY;
      o = (f32x16){};
      u32 vb = isv ? L_V + h2 * 256 + r32 * 16 : L_ONES;
      for (int t = 0; t < 64; ++t) {
        f32x16 z = {};
        const f32x16 sx = mfma32(ldsr<bf16x8>(ka + t * 1024), qf, z);
        float mt = sx[0];
#pragma unroll
        for (int i = 1; i < 16; ++i) mt = fmaxf(mt, sx[i]);
        const auto rr = __builtin_amdgcn_permlane32_swap(__float_as_uint(mt), __float_as_uint(mt), false, false);
        mt = fmaxf(fmaxf(__uint_as_float(rr[0]), __uint_as_float(rr[1])), m);
        const float f = __builtin_amdgcn_exp2f(m - mt);
        m = mt;
        u32 pk[8];
#pragma unroll
        for (int i = 0; i < 8; ++i) pk[i] = cvtpk(__builtin_amdgcn_exp2f(sx[2 * i] - m), __builtin_amdgcn_exp2f(sx[2 * i + 1] - m));
#pragma unroll
        for (int i = 0; i < 16; ++i) o[i] *= f;
        o = mfma32(ldsr<bf16x8>(vb), asbf((u32x4){pk[0], pk[1], pk[2], pk[3]}), o);
        o = mfma32(ldsr<bf16x8>(vb + 512), asbf((u32x4){pk[4], pk[5], pk[6], pk[7]}), o);
        vb += isv ? 1024u : 0u;
      }
      MFMA_GAP32(o);
      l = o[8];
    }
    const float rl = __builtin_amdgcn_rcpf(l);
#pragma unroll
    for (int i = 0; i < 4; ++i) opk[i] = canon(cvtpk(o[2 * i] * rl, o[2 * i + 1] * rl));
  }
  st16(wsr, ox_off(), 0, (u32x4){opk[0], opk[1], opk[2], opk[3]});
  STAMP(9);
  bf16x8 sel;
  auto make_sel = [&]() {
    const u32 l = opaque<u32>(lane), e = ((l >> 2) & 3) != (l >> 4) ? 0u : (l & 1) ? 0x3F800000u : 0x00003F80u;
    const u32 d0 = (l & 2) ? 0u : e, d1 = (l & 2) ? e : 0u;
    sel = asbf((u32x4){d0, d1, d0, d1});
  };
  make_sel();
  const u32 gvo = opaque<u32>(w * 4096 + l16) & 0x73F0u;
  auto pw_of = [&](u32 l) { return L_P + ((l >> 3) & 3) * 256 + (w >> 1) * 1024 + (l & 7) * 32 + (l >> 5) * 16 + (((w ^ (l >> 3)) & 1) * 8); };
  const u32 pw = pw_of(lane);
  const u32 pr = L_P + gvo;
  auto g_load = [&](bf16x8 (&f)[4], u32 src) {
#pragma unroll
    for (int t = 0; t < 4; ++t) f[t] = asbf(ld16(wsr, gvo | (t * 1024), src));
  };
  auto g_wait = [&](bf16x8 (&f)[4], u32 src) {
    if (__builtin_expect(unwritten(f), 0)) {
      do {
        if (timed_out(t0)) break;
        regather4(srd, gvo, src, f[0], f[1], f[2], f[3]);
      } while (unwritten(f));
    }
    asm volatile("" : "+v"(f[0]), "+v"(f[1]), "+v"(f[2]), "+v"(f[3]));
  };
  auto row_stats = [&](int half, const bf16x8 (&f)[4]) {
    float s0 = 0.f, s1 = 0.f, q0 = 0.f, q1 = 0.f;
#pragma unroll
    for (int t = 0; t < 4; ++t) {
      const u32x4 d = asu(f[t]);
      s0 = dot2(d[0], 0x3F803F80u, s0); q0 = dot2(d[0], d[0], q0); s1 = dot2(d[1], 0x3F803F80u, s1); q1 = dot2(d[1], d[1], q1);
      s0 = dot2(d[2], 0x3F803F80u, s0); q0 = dot2(d[2], d[2], q0); s1 = dot2(d[3], 0x3F803F80u, s1); q1 = dot2(d[3], d[3], q1);
    }
    const f32x2 sq = {swap32_add(s0 + s1), swap32_add(q0 + q1)};
    if (lane < 32) ldsw<f32x2>(L_STAT + half * STH + lane * STP + w * 8, sq);
  };
  auto partials = [&](int half, f32x16& acc0, f32x16& acc1, u32 pw) {
#pragma unroll
    for (int a = 0; a < 4; ++a) {
      ldsw<u32x2>(pw + half * 32768 + a * 4096, (u32x2){cvtpk(acc0[4 * a], acc0[4 * a + 1]), cvtpk(acc0[4 * a + 2], acc0[4 * a + 3])});
      ldsw<u32x2>(pw + half * 32768 + (4 + a) * 4096, (u32x2){cvtpk(acc1[4 * a], acc1[4 * a + 1]), cvtpk(acc1[4 * a + 2], acc1[4 * a + 3])});
    }
  };
  const u32 stw = opaque<u32>(L_STAT + r32 * STP + w * 8);
  auto slice_gemm = [&](int half, const bf16x8 (&a)[8], const bf16x8 (&f)[4], bool stats, auto&& mid) {
    f32x16 acc0 = {}, acc1 = {};
    auto stat_block = [&]() {
      float s0 = 0.f, s1 = 0.f, q0 = 0.f, q1 = 0.f;
#pragma unroll
      for (int t = 0; t < 4; ++t) {
        const u32x4 d = asu(f[t]);
        s0 = dot2(d[0], 0x3F803F80u, s0); q0 = dot2(d[0], d[0], q0); s1 = dot2(d[1], 0x3F803F80u, s1); q1 = dot2(d[1], d[1], q1);
        s0 = dot2(d[2], 0x3F803F80u, s0); q0 = dot2(d[2], d[2], q0); s1 = dot2(d[3], 0x3F803F80u, s1); q1 = dot2(d[3], d[3], q1);
      }
      ldsw<f32x2>(stw + half * STH, (f32x2){swap32_add(s0 + s1), swap32_add(q0 + q1)});
      __builtin_amdgcn_sched_barrier(0);
    };
    if (stats && STAT_POS == 0) stat_block();
#pragma unroll
    for (int t = 0; t < 4; ++t) {
      acc0 = mfma32(a[t], f[t], acc0);
      acc1 = mfma32(a[4 + t], f[t], acc1);
    }
    __builtin_amdgcn_sched_barrier(0);
    if (stats && STAT_POS == 1) stat_block();
    mid();
    __builtin_amdgcn_sched_barrier(0);
    partials(half, acc0, acc1, pw);
  };
  auto reduce_at = [&](int half, u32 pr_) -> f32x4 {
    f32x4 acc = {0, 0, 0, 0};
#pragma unroll
    for (int q = 0; q < 4; ++q) acc = mfma16(sel, ldsr<bf16x8>(pr_ + half * 32768 + q * 1024), acc);
    return acc;
  };
  auto reduce = [&](int half) { return reduce_at(half, pr); };
  auto src_of = [&](u32 base, int half) { return base + blk_of(half) * 32768; };
  const u32 hvo = opaque<u32>(PUT16 && (lane & 1) ? OOB : (u32)lane * 8);
  auto put_at = [&](u32 vo, u32 so, u32x2 v) {
    if (PUT16) {
      const u32 n0 = __builtin_amdgcn_mov_dpp(v[0], 0xB1, 0xF, 0xF, true), n1 = __builtin_amdgcn_mov_dpp(v[1], 0xB1, 0xF, 0xF, true);
      st16(wsr, vo, so, (u32x4){v[0], v[1], n0, n1});
    } else st8(wsr, vo, so, v);
  };
  auto pack4 = [&](f32x4 v) { return (u32x2){canon(cvtpk(v[0], v[1])), canon(cvtpk(v[2], v[3]))}; };
  const u32 a3off = w < 7 ? ((7 * j + w) * 32) * 1024 + l16 : OOB;
  auto a3_load = [&](int t0_, int t1_) {
#pragma unroll
    for (int t = 0; t < 32; ++t) if (t >= t0_ && t < t1_) a3[t] = asbf(ld16(wsr, a3off, WS_W3 + t * 1024));
  };

  auto a3_slot = [&](int k) {
    constexpr int n = 32 - A3_N0;
    a3_load(A3_N0 + k * n / 6, A3_N0 + (k + 1) * n / 6);
  };
  f32x4 cs3p = {0.f, 0.f, 0.f, 0.f};
  auto cs3_acc = [&](int t0_, int t1_) {
#pragma unroll
    for (int t = 0; t < 32; ++t) if (t >= t0_ && t < t1_) cs3p = mfma16(sel, a3[t], cs3p);
  };
  f32x4 hres[2], xp[2], cs1;
  bf16x8 fA[4], fB[4];
  const u32 str = opaque<u32>(L_STAT + (lane >> 1) * STP + (lane & 1) * 32);
  auto back = [&](int half, bool stats, auto&& epi, auto&& hook) {
    bf16x8 p[4];
    f32x4 p0 = {0, 0, 0, 0}, p1 = {0, 0, 0, 0};
#pragma unroll
    for (int q = 0; q < 4; ++q) p[q] = ldsr<bf16x8>(pr + half * 32768 + q * 1024);
    if (stats) { p0 = ldsr<f32x4>(str + half * STH); p1 = ldsr<f32x4>(str + half * STH + 16); }
    hook();
    __builtin_amdgcn_sched_barrier(0);
    f32x4 acc = {0, 0, 0, 0};
#pragma unroll
    for (int q = 0; q < 4; ++q) acc = mfma16(sel, p[q], acc);
    RowStat st = {0.f, 0.f, 0.f};
    if (stats) { const f32x4 p = p0 + p1; st = finish_stat(xor1_add(p[0] + p[2]), xor1_add(p[1] + p[3])); }
    return epi(half, acc, st);
  };
  auto phase = [&](const bf16x8 (&a)[8], u32 src, bool stats, int gb, auto&& first, auto&& epi, auto&& put, auto&& pre, auto&& slotA, auto&& slotB, int sbase) {
    PSTAMP(sbase, 0);
    g_wait(fA, src_of(src, 0));
    STAMP(sbase);
    PSTAMP(sbase, 1);
    slice_gemm(0, a, fA, stats, [&]() {
      if (gb == 0) g_load(fB, src_of(src, 1));
      if (SLOT_POS == 0) slotA();
      first();
    });
    PSTAMP(sbase, 4);
    BAR();
    PSTAMP(sbase, 5);
    const u32x2 vA = back(0, stats, epi, [&]() { if (gb == 1) g_load(fB, src_of(src, 1)); });
    PSTAMP(sbase, 6);
    if (gb == 2) g_load(fB, src_of(src, 1));
    __builtin_amdgcn_sched_barrier(0);
    put(0, vA);
    if (SLOT_POS == 1) slotA();
    __builtin_amdgcn_sched_barrier(0);
    PSTAMP(sbase, 7);
    g_wait(fB, src_of(src, 1));
    STAMP(sbase + 1);
    PSTAMP(sbase, 8);
    slice_gemm(1, a, fB, stats, [&]() { if (SLOT_POS == 0) slotB(); });
    PSTAMP(sbase, 11);
    BAR();
    PSTAMP(sbase, 12);
    const u32x2 vB = back(1, stats, epi, [&]() {
      pre();
    });
    PSTAMP(sbase, 13);
    PSTAMP(sbase, 14);
    __builtin_amdgcn_sched_barrier(0);
    put(1, vB);
    if (SLOT_POS == 1) slotB();
    __builtin_amdgcn_sched_barrier(0);
    PSTAMP(sbase, 15);
  };
  auto prio_late = [&](int p) {
#define PRIO_ASM(P) asm volatile("s_cmp_lt_u32 %0, 4\n\ts_cbranch_scc1 .Lprio%=\n\ts_setprio " #P "\n.Lprio%=:" :: "s"(w) : "scc")
    if (p == 0) PRIO_ASM(0); else if (p == 1) PRIO_ASM(1); else PRIO_ASM(2);
#undef PRIO_ASM
  };
  const u32 a4off = (2 * j * 112 + 14 * w) * 1024 + l16;
  {
    float c[2] = {0.f, 0.f};
#pragma unroll
    for (int t = 0; t < 8; ++t) {
      const u32x4 d = asu(a_1[t]);
#pragma unroll
      for (int e = 0; e < 4; ++e) c[t >> 2] = dot2(d[e], 0x3F803F80u, c[t >> 2]);
    }
    const float c0 = swap32_add(c[0]), c1 = swap32_add(c[1]);
    ldsw<float>(L_CS1P + w * 256 + lane * 4, lane < 32 ? c0 : c1);
  }
  cs3_acc(0, A3_N0);
  __builtin_amdgcn_sched_barrier(0);
  if (O_SLEEP) __builtin_amdgcn_s_sleep(O_SLEEP);
  g_load(fA, src_of(WS_OX, 0));
  g_load(fB, src_of(WS_OX, 1));
  __builtin_amdgcn_sched_barrier(0);
  phase(a_o, WS_OX, false, -1,
        [&]() { BAR(); },
        [&](int half, f32x4 acc, RowStat) {
          if (half == 1) {
            cs1 = (f32x4){0.f, 0.f, 0.f, 0.f};
#pragma unroll
            for (int v = 0; v < 8; ++v) cs1 += ldsr<f32x4>(L_CS1P + v * 256 + w * 32 + (lane & 1) * 16);
            cs1 = opaque(cs1);
          }
          hres[half] = acc + (f32x4){bflo(xres[half][0]), bfhi(xres[half][0]), bflo(xres[half][1]), bfhi(xres[half][1])};
          return pack4(hres[half]);
        },
        [&](int half, u32x2 v) { put_at(hvo, hx_so(WS_HX, half), v); },
        [&]() { g_load(fA, src_of(WS_HX, 0)); },
        [&]() {
          if (W4_WARM == 2) {
#pragma unroll
            for (int i = 0; i < 7; ++i) dma1(ws + WS_W4 + i * 1024, (((u32)(b >> 6) & 3u) * 8 + w) * 7168 + j * 229376 + l16, lb + L_DUM1);
          }
          a3_slot(0);
        },
        [&]() { a3_slot(1); }, 10);
  cs3_acc(A3_N0, A3_N0 + (32 - A3_N0) / 6);
  __builtin_amdgcn_sched_barrier(0);
  STAMP(12);
  MEAS(12);
  DELAY(5);
  const u32 tso[2] = {opaque<u32>(ts_vo(0)), opaque<u32>(ts_vo(1))};
  phase(a_1, WS_HX, true, GB_POS, [&]() { },
        [&](int half, f32x4 acc, RowStat st) {
          const float r2 = __builtin_amdgcn_rsqf(st.var * st.rstd * st.rstd + LN_EPS), rr = st.rstd * r2;
          const f32x4 pre = (acc - st.mu * cs1) * rr;
          const f32x2 g0 = gelu2((f32x2){pre[0], pre[1]}), g1 = gelu2((f32x2){pre[2], pre[3]});
          xp[half] = (hres[half] - st.mu) * st.rstd;
          return (u32x2){canon(cvtpk(g0[0], g0[1])), canon(cvtpk(g1[0], g1[1]))};
        },
        [&](int half, u32x2 v) { put_at(tso[half], hx_so(WS_TS, half) - 32768, v); },
        [&]() { g_load(fA, src_of(WS_TS, 0)); },
        [&]() { a3_slot(2); },
        [&]() { a3_slot(3); dma4_sc1(ws + WS_W4 + 10 * 1024, a4off, lb + L_A4 + w * 8192); }, 13);
  cs3_acc(A3_N0 + (32 - A3_N0) / 6, A3_N0 + 3 * (32 - A3_N0) / 6);
  __builtin_amdgcn_sched_barrier(0);
  STAMP(15);
  phase(a_2, WS_TS, false, GB_POS, [&]() { },
        [&](int half, f32x4 acc, RowStat) {
          const f32x4 x1 = xp[half] + acc;
          ldsw<f32x4>(L_RES + half * 8192 + w * 1024 + l16, hres[half] + x1);
          return pack4(x1);
        },
        [&](int half, u32x2 v) { put_at(hvo, hx_so(WS_X1, half), v); },
        [&]() { dma4_sc1(ws + src_of(WS_X1, 0), gvo, lb + L_P + w * 4096); },
        [&]() { a3_slot(4); dma4_sc1(ws + WS_W4 + 114688 + 10 * 1024, a4off, lb + L_A4 + w * 8192 + 4096); },
        [&]() { a3_slot(5); }, 16);
  cs3_acc(A3_N0 + 3 * (32 - A3_N0) / 6, 32);
  {
    const float c = swap32_add((cs3p[0] + cs3p[1]) + (cs3p[2] + cs3p[3]));
    ldsw<float>(L_CS + w * 128 + r32 * 4, c);
  }
  __builtin_amdgcn_sched_barrier(0);
  STAMP(18);
  MEAS(18);
  DELAY(7);
  bf16x8 a4[10][2];
  auto a4_load = [&](int n) { a4[n >> 1][n & 1] = asbf(ld16(wsr, a4off, WS_W4 + (n & 1) * 114688 + (n >> 1) * 1024)); };
  constexpr int ord[14] = {0, 2, 4, 6, 1, 3, 5, 7, 8, 10, 12, 9, 11, 13};
  u32x4 uf[28];
  auto u_load1 = [&](int k) { uf[k] = ld16(wsr, l16, WS_UX + (blk_of(k / 14) * 112 + w * 14 + ord[k % 14]) * 1024); };
  {
    auto x1_finish = [&](int half) {
#pragma unroll
      for (int t = 0; t < 4; ++t) fA[t] = ldsr<bf16x8>(pr + half * 32768 + t * 1024);
      if (__builtin_expect(unwritten(fA), 0)) {
        do {
          if (timed_out(t0)) break;
          __builtin_amdgcn_s_sleep(1);
          dma4_sc1(ws + src_of(WS_X1, half), gvo, lb + L_P + half * 32768 + w * 4096);
          WAITV(0);
#pragma unroll
          for (int t = 0; t < 4; ++t) fA[t] = ldsr<bf16x8>(pr + half * 32768 + t * 1024);
        } while (unwritten(fA));
      }
      row_stats(half, fA);
    };
    struct UOut { float nmu, k1, k2, ir; f32x4 cs[2]; f32x4 p[4]; u32 up[8]; u32 cb, vo; };
    auto u_stat0 = [&](UOut& u, int half) {
      const u32 l = opaque<u32>(lane), sr = opaque<u32>(L_STAT + half * STH + (l & 31) * STP);
#pragma unroll
      for (int i = 0; i < 4; ++i) u.p[i] = ldsr<f32x4>(sr + 16 * i);
    };
    auto u_stat1 = [&](UOut& u) {
      const f32x4 p = (u.p[0] + u.p[1]) + (u.p[2] + u.p[3]);
      const RowStat st = finish_stat(p[0] + p[2], p[1] + p[3]);
      const float c1 = -2.0f * 0.7978845608028654f * 1.4426950408889634f, c2 = c1 * 0.044715f;
      u.nmu = opaque(-st.mu); u.k1 = opaque(c1 * st.rstd); u.k2 = opaque(c2 * st.rstd * st.rstd * st.rstd); u.ir = opaque(__builtin_amdgcn_sqrtf(st.var + LN_EPS));
    };
    auto u_cs = [&](UOut& u, int a) {
      if (a == 0) {
        const u32 l = opaque<u32>(lane);
        u.cb = opaque<u32>(L_CS + w * 128 + (l >> 5) * 16);
        u.vo = opaque<u32>(w < 7 ? l * 16 : OOB);
      }
      u.cs[a & 1] = ldsr<f32x4>(u.cb + a * 32);
    };
    auto u_store = [&](UOut& u, int half, int p) {
      if (p == 3 || p == 7) {
        const u32 so = WS_UX + (blk_of(half) * 112 + j * 14 + 2 * w) * 1024;
        if (p == 3) st16(wsr, u.vo, so, (u32x4){u.up[0], u.up[1], u.up[2], u.up[3]});
        else st16(wsr, u.vo, so + 1024, (u32x4){u.up[4], u.up[5], u.up[6], u.up[7]});
      }
    };
    auto u_quad = [&](UOut& u, int half, const f32x16& acc, int p) {
      float t[4], e[4];
#pragma unroll
      for (int i = 0; i < 4; ++i) { const int v = 2 * p + i; t[i] = opaque(__builtin_fmaf(u.nmu, u.cs[(v >> 2) & 1][v & 3], acc[v])); }
#pragma unroll
      for (int i = 0; i < 4; ++i) e[i] = __builtin_amdgcn_exp2f(t[i] * __builtin_fmaf(t[i] * t[i], u.k2, u.k1));
#pragma unroll
      for (int i = 0; i < 4; ++i) t[i] = opaque(t[i] * __builtin_amdgcn_rcpf(__builtin_fmaf(e[i], u.ir, u.ir)));
      u.up[p] = canon(cvtpk(t[0], t[1])); u.up[p + 1] = canon(cvtpk(t[2], t[3]));
      u_store(u, half, p + 1);
    };
    auto u_pair = [&](UOut& u, int half, const f32x16& acc, int p) {
      const int a = p >> 1, e = 2 * (p & 1);
      const f32x2 t = (f32x2){u.cs[a & 1][e], u.cs[a & 1][e + 1]} * u.nmu + (f32x2){acc[2 * p], acc[2 * p + 1]};
      const f32x2 ar = t * ((t * t) * u.k2 + u.k1);
      const f32x2 d = (f32x2){__builtin_amdgcn_exp2f(ar[0]), __builtin_amdgcn_exp2f(ar[1])} * u.ir + u.ir;
      const f32x2 g = t * (f32x2){__builtin_amdgcn_rcpf(d[0]), __builtin_amdgcn_rcpf(d[1])};
      u.up[p] = canon(cvtpk(g[0], g[1]));
    };
    WAITV(1);
    x1_finish(0);
    BAR();
    STAMP(19);
    dma4_sc1(ws + src_of(WS_X1, 1), gvo, lb + L_P + 32768 + w * 4096);
    const u32 xb = L_P + (w < 7 ? l16 : 0u);
    f32x16 accA = {}, accB = {};
    bf16x8 bq[4];
    UOut uA, uB;
    if (M_PRIO) __builtin_amdgcn_s_setprio(1);
    {
#pragma unroll
      for (int t = 0; t < 4; ++t) bq[t] = ldsr<bf16x8>(xb + t * 1024);
#pragma unroll
      for (int t = 0; t < 32; ++t) {
        accA = mfma32(a3[t], bq[t & 3], accA);
        if (t + 4 < 32) bq[t & 3] = ldsr<bf16x8>(xb + (t + 4) * 1024);
        if (t == 0) u_stat0(uA, 0);
        if (t == 3) u_stat1(uA);
        if (t == 29) u_cs(uA, 0);
        if (t >= 4 && (t & 3) == 0 && t / 4 - 1 < A4_N0) a4_load(t / 4 - 1);
        __builtin_amdgcn_sched_barrier(0);
      }
    }
    if (M_PRIO) __builtin_amdgcn_s_setprio(0);
    STAMP(20);
    auto gelu_half = [&](UOut& u, int half, const f32x16& acc) {
#pragma unroll
      for (int p = 0; p < 8; p += 2) {
        if (p + 2 < 8) u_cs(u, (p + 2) >> 1);
        constexpr int nr = (8 - UE_P) / 2;
        if (half == 1) {
#pragma unroll
          for (int n = 20 - A4_LATE + p / 2 * A4_LATE / 4; n < 20 - A4_LATE + (p / 2 + 1) * A4_LATE / 4; ++n) a4_load(n);
        }
        if (half == 1 && p >= UE_P) {
#pragma unroll
          for (int i = (p - UE_P) / 2 * U_INIT / (nr ? nr : 1); i < ((p - UE_P) / 2 + 1) * U_INIT / (nr ? nr : 1); ++i) u_load1(i);
        }
        if (GB_PK) { u_pair(u, half, acc, p); u_pair(u, half, acc, p + 1); u_store(u, half, p + 1); }
        else u_quad(u, half, acc, p);
        __builtin_amdgcn_sched_barrier(0);
      }
      if (half == 1 && UE_P >= 8) {
#pragma unroll
        for (int i = 0; i < U_INIT; ++i) u_load1(i);
      }
    };
    if (PQ == 0 || w < 4) gelu_half(uA, 0, accA);
    asm volatile("s_waitcnt vmcnt(%0)" :: "n"(A4_N0 + (PQ ? 0 : 2)) : "memory");
    x1_finish(1);
    BAR();
    STAMP(21);
    if (!M_PRIO) prio_late(Q_PRIO);
    if (PQ && w >= 4) gelu_half(uA, 0, accA);
    if (M_PRIO) __builtin_amdgcn_s_setprio(1);
    {
#pragma unroll
      for (int t = 0; t < 4; ++t) bq[t] = ldsr<bf16x8>(xb + 32768 + t * 1024);
#pragma unroll
      for (int t = 0; t < 32; ++t) {
        if (W3B_TOG && (t % (W3B_TOG ? W3B_TOG : 1)) == 0) prio_late(((t / (W3B_TOG ? W3B_TOG : 1)) & 1) ? 0 : 1);
        accB = mfma32(a3[t], bq[t & 3], accB);
        if (t + 4 < 32) bq[t & 3] = ldsr<bf16x8>(xb + 32768 + (t + 4) * 1024);
        if (t == 0) u_stat0(uB, 1);
        if (t == 3) u_stat1(uB);
        if (t == 29) u_cs(uB, 0);
        if (A4_SPREAD == 0) { const int n = A4_N0 + t - 1; if (t >= 1 && n < 20 - A4_LATE) a4_load(n); }
        else if (A4_SPREAD == 1) { const int n = A4_N0 + (t - 1) / 2; if ((t & 1) && n < 20 - A4_LATE) a4_load(n); }
        else { const int n = A4_N0 + (t - 1) / 4; if ((t & 3) == 1 && n < 20 - A4_LATE) a4_load(n); }
        __builtin_amdgcn_sched_barrier(0);
      }
    }
    if (M_PRIO) __builtin_amdgcn_s_setprio(0); else prio_late(GB_PRIO);
    if (!MID_BAR && lane == 0) asm volatile("ds_add_u32 %0, %1" :: "v"(lb + L_CNT + 8), "v"(1u) : "memory");
    STAMP(22);
    gelu_half(uB, 1, accB);
    STAMP(23);
  }
  {
    if (M_PRIO) { __builtin_amdgcn_s_setprio(1); if (W4_PRIO) prio_late(2); } else prio_late(W4_PRIO);
#pragma unroll
    for (int half = 0; half < 2; ++half) {
      const u32 sa = L_A4 + w * 8192 + opaque<u32>(l16);
      f32x16 acc0 = {}, acc1 = {};
      bf16x8 sq[4][2];
      auto step = [&](int s_, u32x4 u) {
        if (s_ + 2 < 14 && ord[s_ + 2] >= 10) { const int k = ord[s_ + 2] - 10; sq[k][0] = ldsr<bf16x8>(sa + k * 1024); sq[k][1] = ldsr<bf16x8>(sa + 4096 + k * 1024); }
        const int t = ord[s_];
        acc0 = mfma32(t < 10 ? a4[t < 10 ? t : 0][0] : sq[t < 10 ? 0 : t - 10][0], asbf(u), acc0);
        acc1 = mfma32(t < 10 ? a4[t < 10 ? t : 0][1] : sq[t < 10 ? 0 : t - 10][1], asbf(u), acc1);
      };
      u32 m = 0;
#pragma unroll
      for (int s_ = 0; s_ < 14; ++s_) {
        const int k = 14 * half + s_;
        step(s_, uf[k]);
        m = opaque(umax(umax(umax(m, uf[k][0]), uf[k][1]), umax(uf[k][2], uf[k][3])));
        if (k + U_WIN < 28 && k + U_WIN >= U_INIT) u_load1(k + U_WIN);
        __builtin_amdgcn_sched_barrier(0);
      }
      if (__builtin_expect(__any(m == SENT), 0)) {
        u32x4 ug[14];
        bool bad;
        do {
          __builtin_amdgcn_s_sleep(1);
          asm volatile("s_nop 7" ::: "memory");
          const u32 so = WS_UX + (blk_of(half) * 112 + w * 14) * 1024, l = opaque<u32>(l16);
#pragma unroll
          for (int i = 0; i < 14; ++i) regather1(srd, l, so + ord[i] * 1024, ug[i]);
          asm volatile("s_waitcnt vmcnt(0)" : "+v"(ug[0]), "+v"(ug[1]), "+v"(ug[2]), "+v"(ug[3]), "+v"(ug[4]), "+v"(ug[5]), "+v"(ug[6]), "+v"(ug[7]), "+v"(ug[8]), "+v"(ug[9]),
                       "+v"(ug[10]), "+v"(ug[11]), "+v"(ug[12]), "+v"(ug[13]) :: "memory");
          u32 mm = 0;
#pragma unroll
          for (int i = 0; i < 14; ++i) mm = umax(mm, max4(ug[i]));
          bad = __any(mm == SENT);
        } while (bad && !timed_out(t0));
        acc0 = (f32x16){}; acc1 = (f32x16){};
#pragma unroll
        for (int s_ = 0; s_ < 14; ++s_) step(s_, ug[s_]);
      }
      STAMP(24 + half);
      if (!MID_BAR && half == 1) {
        while (__builtin_amdgcn_readfirstlane(*(volatile LAS u32*)((LAS char*)smem + L_CNT + 8)) != 8) {
          if (timed_out(t0)) break;
          __builtin_amdgcn_s_sleep(1);
        }
        FENCE();
      }
      partials(half, acc0, acc1, pw_of(opaque<u32>(lane)));
      if (MID_BAR ? half == 0 || !OUT_EARLY : half == 1) make_sel();
      if (half == 1) STAMP(26);
      if (half == 1 || (MID_BAR && OUT_EARLY)) BAR();
      auto out_half = [&](int hf) {
        const u32 l = opaque<u32>(lane);
        const f32x4 v = reduce_at(hf, L_P + w * 4096 + l * 16) + ldsr<f32x4>(L_RES + hf * 8192 + w * 1024 + l * 16);
        const u32 oo = (u32)((blk_of(hf) * 32 + (l >> 1)) * 1024 + (64 * j + 8 * w + 4 * (l & 1)) * 2);
        if (OUT_AUX >= 0) __builtin_amdgcn_raw_buffer_store_b64((u32x2){cvtpk(v[0], v[1]), cvtpk(v[2], v[3])}, mkrsrc(out, 2048 * 1024), oo, 0, OUT_AUX < 0 ? 0 : OUT_AUX);
        else *(GAS u32x2*)((char*)out + oo) = (u32x2){cvtpk(v[0], v[1]), cvtpk(v[2], v[3])};
      };
      if (MID_BAR && OUT_EARLY) out_half(half);
      else if (half == 1) { out_half(0); out_half(1); }
      else if (MID_BAR) BAR();
      FENCE();
    }
  }
  if (MEASP >= 0) {
    const u64 te = __builtin_amdgcn_s_memrealtime();
    u64 d = MEASP == 98 ? te - t0 : tmeas - t0;
    if (MEASP == 0) d = t0 - tA;
    if (MEASP == 99) d = (__builtin_amdgcn_s_memtime() - c0) / 20;
    while (__builtin_amdgcn_s_memrealtime() - te < 4 * d) __builtin_amdgcn_s_sleep(4);
  }
  STAMP(27);
}

extern "C" void kernel_launch(void* const* d_in, const int* in_sizes, int n_in, void* d_out, int out_size, void* d_ws, size_t ws_size, hipStream_t stream) {
  (void)in_sizes; (void)n_in; (void)out_size; (void)ws_size;
  fused_block_kernel<<<256, 512, 0, stream>>>((const u16*)d_in[0], (const u16*)d_in[1], (const u16*)d_in[2], (const u16*)d_in[3], (const u16*)d_in[4],
                                              (const u16*)d_in[6], (const u16*)d_in[7], (const u16*)d_in[8], (const u16*)d_in[9], (u16*)d_out, (char*)d_ws);
}
```

```cpp
#include <hip/hip_runtime.h>
#include <stdint.h>
#include <type_traits>

typedef unsigned short u16;
typedef unsigned int u32;
typedef unsigned long long u64;
typedef short bf16x8 __attribute__((ext_vector_type(8)));
typedef short s16x4 __attribute__((ext_vector_type(4)));
typedef float f32x2 __attribute__((ext_vector_type(2)));
typedef float f32x4 __attribute__((ext_vector_type(4)));
typedef float f32x16 __attribute__((ext_vector_type(16)));
typedef u32 u32x2 __attribute__((ext_vector_type(2)));
typedef u32 u32x4 __attribute__((ext_vector_type(4)));
typedef __bf16 bf16x2_t __attribute__((ext_vector_type(2)));
#define LAS __attribute__((address_space(3)))
#define GAS __attribute__((address_space(1)))
#define DI __device__ __forceinline__

constexpr u32 WS_EPOCH = 0x0;
constexpr u32 WS_FKV = 0x1000;
constexpr u32 WS_FW = 0x5000;
constexpr u32 WS_FM = 0x9000;
constexpr u32 WS_K = 0x10000;
constexpr u32 WS_V = 0x20000;
constexpr u32 WS_DBG = 0x1E00000;
constexpr u32 WS_WO = 0x100000, WS_W1 = 0x180000, WS_W2 = 0x200000, WS_W3 = 0x280000, WS_W4 = 0x440000;
constexpr u32 WS_OX = 0x600000, WS_HX = 0x800000, WS_TS = 0xA00000, WS_X1 = 0xC00000, WS_UX = 0xE00000;
constexpr u32 WS_STAMP = 0x2000000;
constexpr u32 WS_END = 0x2100000;
constexpr u32 OOB = 0x80000000u;

constexpr u32 XP = 1040;
constexpr u32 L_XIMG = 0;
constexpr u32 L_WQ = 66560;
constexpr u32 L_WKV = 132096;
constexpr u32 S_PM = 148480, S_PC = 156672, S_PS = 157184, S_PQ = 157696, S_FIN = 158208, S_QS = 158336, S_QC = 159360, S_QQ = 156672;
constexpr u32 L_DUM1 = 160384;
constexpr u32 L_CNT = 161408;
constexpr u32 S_KF = 161440;
constexpr u32 L_K = 0, L_V = 65536, L_ONES = 151552;
constexpr u32 L_STG = 131072;
constexpr u32 L_CS1P = 155904;
constexpr u32 L_P = 0;
constexpr u32 L_STAT = 65536;
constexpr u32 STP = 80, STH = 2560;
constexpr u32 L_CS = 71680;
constexpr u32 L_RES = 73728;
constexpr u32 L_A4 = 90112;
constexpr u32 L_STAMP = 161792;
constexpr u32 LDS_BYTES = 163840;

__shared__ __attribute__((aligned(16))) char smem[LDS_BYTES];

constexpr u32 SENT = 0xFFFFFFFFu;
constexpr float LN_EPS = 1e-5f;
constexpr float QSCALE = 0.25f * 1.4426950408889634f;
#define CODE_KB 48

DI f32x16 mfma32(bf16x8 a, bf16x8 b, f32x16 c) { return __builtin_amdgcn_mfma_f32_32x32x16_bf16(a, b, c, 0, 0, 0); }
DI f32x4 mfma16(bf16x8 a, bf16x8 b, f32x4 c) { return __builtin_amdgcn_mfma_f32_16x16x32_bf16(a, b, c, 0, 0, 0); }
DI u32 cvtpk(float lo, float hi) { f32x2 v = {lo, hi}; bf16x2_t b = __builtin_convertvector(v, bf16x2_t); return __builtin_bit_cast(u32, b); }
DI u32 canon(u32 d) { return d < 0xFFFFFFFEu ? d : 0xFFFFFFFEu; }
DI float bflo(u32 d) { return __uint_as_float(d << 16); }
DI float bfhi(u32 d) { return __uint_as_float(d & 0xFFFF0000u); }
DI float dot2(u32 a, u32 b, float c) { return __builtin_amdgcn_fdot2_f32_bf16(__builtin_bit_cast(bf16x2_t, a), __builtin_bit_cast(bf16x2_t, b), c, false); }
DI float gelu(float x) {
  const float c1 = -2.0f * 0.7978845608028654f * 1.4426950408889634f, c2 = c1 * 0.044715f;
  const float e = __builtin_amdgcn_exp2f(x * (c1 + c2 * x * x));
  return x * __builtin_amdgcn_rcpf(1.0f + e);
}
DI f32x2 gelu2(f32x2 x) {
  const float c1 = -2.0f * 0.7978845608028654f * 1.4426950408889634f, c2 = c1 * 0.044715f;
  const f32x2 a = x * ((x * x) * c2 + c1);
  const f32x2 d = (f32x2){__builtin_amdgcn_exp2f(a[0]), __builtin_amdgcn_exp2f(a[1])} + 1.0f;
  return x * (f32x2){__builtin_amdgcn_rcpf(d[0]), __builtin_amdgcn_rcpf(d[1])};
}
DI float swap32_add(float v) {
  const auto r = __builtin_amdgcn_permlane32_swap(__float_as_uint(v), __float_as_uint(v), false, false);
  return __uint_as_float(r[0]) + __uint_as_float(r[1]);
}
DI float xor1_add(float v) { return v + __int_as_float(__builtin_amdgcn_mov_dpp(__float_as_int(v), 0xB1, 0xF, 0xF, true)); }

#define BAR() asm volatile("s_waitcnt lgkmcnt(0)\n\ts_barrier" ::: "memory")
#define WAITV(n) asm volatile("s_waitcnt vmcnt(" #n ")" ::: "memory")
#define FENCE() asm volatile("" ::: "memory")
template <class T> DI T opaque(T v) { asm volatile("" : "+v"(v)); return v; }

template <class T> DI T ldsr(u32 off) { return *(const LAS T*)((LAS char*)smem + off); }
template <class T> DI void ldsw(u32 off, T v) { *(LAS T*)((LAS char*)smem + off) = v; }
DI s16x4 ldstr(u32 off) {
  typedef short v4i16_t __attribute__((ext_vector_type(4)));
  return __builtin_bit_cast(s16x4, __builtin_amdgcn_ds_read_tr16_b64_v4i16((LAS v4i16_t*)((LAS char*)smem + off)));
}
DI bf16x8 cat(s16x4 a, s16x4 b) { return __builtin_shufflevector(a, b, 0, 1, 2, 3, 4, 5, 6, 7); }
DI u32 lds_base() { return (u32)(uintptr_t)(LAS char*)smem; }

#define DMA_HEAD "s_mov_b32 %[k], m0\n\ts_mov_b32 m0, %[lds]\n\ts_nop 0\n\t"
#define DMA_TAIL "s_mov_b32 m0, %[k]"
DI void dma1(const void* sbase, u32 voff, u32 ldsdst) {
  u32 keep;
  asm volatile(DMA_HEAD "global_load_lds_dwordx4 %[v], %[s]\n\t" DMA_TAIL : [k] "=&s"(keep) : [v] "v"(voff), [s] "s"(sbase), [lds] "s"(ldsdst) : "memory");
}
DI void dma2(const void* sbase, u32 voff, u32 ldsdst) {
  u32 keep;
  asm volatile(DMA_HEAD "global_load_lds_dwordx4 %[v], %[s]\n\tglobal_load_lds_dwordx4 %[v], %[s] offset:1024\n\t" DMA_TAIL
               : [k] "=&s"(keep) : [v] "v"(voff), [s] "s"(sbase), [lds] "s"(ldsdst) : "memory");
}
DI void dma4_rows(const void* sbase, u32 v0, u32 v1, u32 v2, u32 v3, u32 ldsdst) {
  u32 keep;
  asm volatile(DMA_HEAD "global_load_lds_dwordx4 %[a], %[s]\n\tglobal_load_lds_dwordx4 %[b], %[s] offset:1040\n\t"
               "global_load_lds_dwordx4 %[c], %[s] offset:2080\n\tglobal_load_lds_dwordx4 %[d], %[s] offset:3120\n\t" DMA_TAIL
               : [k] "=&s"(keep) : [a] "v"(v0), [b] "v"(v1), [c] "v"(v2), [d] "v"(v3), [s] "s"(sbase), [lds] "s"(ldsdst) : "memory");
}
DI void dma4_sc1(const void* sbase, u32 voff, u32 ldsdst) {
  u32 keep;
  asm volatile(DMA_HEAD "global_load_lds_dwordx4 %[v], %[s] sc1\n\tglobal_load_lds_dwordx4 %[v], %[s] offset:1024 sc1\n\t"
               "global_load_lds_dwordx4 %[v], %[s] offset:2048 sc1\n\tglobal_load_lds_dwordx4 %[v], %[s] offset:3072 sc1\n\t" DMA_TAIL
               : [k] "=&s"(keep) : [v] "v"(voff), [s] "s"(sbase), [lds] "s"(ldsdst) : "memory");
}
DI void dma_pair_late(int w, const void* sbase, u32 voff, u32 ldsdst) {
  u32 keep;
  asm volatile("s_cmp_lt_u32 %[w], 4\n\ts_cbranch_scc1 .Lkv%=\n\t" DMA_HEAD "global_load_lds_dwordx4 %[a], %[s] sc1\n\ts_add_u32 m0, m0, 0x10000\n\ts_nop 0\n\t"
               "global_load_lds_dwordx4 %[b], %[s] sc1\n\t" DMA_TAIL "\n.Lkv%=:"
               : [k] "=&s"(keep) : [w] "s"(w), [a] "v"(voff), [b] "v"(voff + 0x10000u), [s] "s"(sbase), [lds] "s"(ldsdst) : "memory", "scc");
}
template <int N> DI void count_late(int w, u32 addr) {
  u64 keep;
  asm volatile("s_cmp_lt_u32 %[w], 4\n\ts_cbranch_scc1 .Lcl%=\n\ts_waitcnt vmcnt(%[n])\n\ts_mov_b64 %[k], exec\n\ts_mov_b64 exec, 1\n\tds_add_u32 %[a], %[o]\n\ts_mov_b64 exec, %[k]\n.Lcl%=:"
               : [k] "=&s"(keep) : [w] "s"(w), [n] "n"(N), [a] "v"(addr), [o] "v"(1u) : "memory", "scc");
}
DI void st16_early(int w, u32x4 srd, u32 voff, u32 soff, u32x4 v) {
  asm volatile("s_cmp_ge_u32 %[w], 4\n\ts_cbranch_scc1 .Lse%=\n\tbuffer_store_dwordx4 %[d], %[v], %[r], %[s] offen sc1\n\ts_nop 1\n.Lse%=:"
               :: [w] "s"(w), [d] "v"(v), [v] "v"(voff), [r] "s"(srd), [s] "s"(soff) : "memory", "scc");
}
#define DMA_STEP(SC) "global_load_lds_dwordx4 %[v], %[s]" SC "\n\ts_add_u32 m0, m0, %[ls]\n\tv_add_u32 %[v], %[gs], %[v]\n\t"
template <int N, bool SC1, u32 GS>
DI void dma_strided(const void* sbase, u32 voff, u32 ldsdst, u32 lstep) {
  u32 keep;
  static_assert(N == 2 || N == 3 || N == 4 || N == 8, "");
#define DMA_RUN(BODY) asm volatile(DMA_HEAD BODY DMA_TAIL : [k] "=&s"(keep), [v] "+v"(voff) : [s] "s"(sbase), [lds] "s"(ldsdst), [ls] "s"(lstep), [gs] "n"(GS) : "memory", "scc")
  if constexpr (SC1) {
    if constexpr (N == 2) DMA_RUN(DMA_STEP(" sc1") DMA_STEP(" sc1"));
    if constexpr (N == 3) DMA_RUN(DMA_STEP(" sc1") DMA_STEP(" sc1") DMA_STEP(" sc1"));
    if constexpr (N == 4) DMA_RUN(DMA_STEP(" sc1") DMA_STEP(" sc1") DMA_STEP(" sc1") DMA_STEP(" sc1"));
  } else {
    if constexpr (N == 8) DMA_RUN(DMA_STEP("") DMA_STEP("") DMA_STEP("") DMA_STEP("") DMA_STEP("") DMA_STEP("") DMA_STEP("") DMA_STEP(""));
  }
#undef DMA_RUN
}

typedef __amdgpu_buffer_rsrc_t rsrc_t;
DI rsrc_t mkrsrc(const void* p, u32 bytes) { return __builtin_amdgcn_make_buffer_rsrc((void*)p, 0, bytes, 0x00020000); }
DI u32x4 ld16(rsrc_t r, u32 voff, u32 soff) { return __builtin_amdgcn_raw_buffer_load_b128(r, voff, soff, 16); }
DI void st16(rsrc_t r, u32 voff, u32 soff, u32x4 v) {
  __builtin_amdgcn_raw_buffer_store_b128(v, r, voff, soff, 16);
  asm volatile("s_nop 1" :: "v"(v) : "memory");
}
DI void st8(rsrc_t r, u32 voff, u32 soff, u32x2 v) { __builtin_amdgcn_raw_buffer_store_b64(v, r, voff, soff, 16); }
DI void st4(rsrc_t r, u32 voff, u32 soff, u32 v) { __builtin_amdgcn_raw_buffer_store_b32(v, r, voff, soff, 16); }
DI bf16x8 asbf(u32x4 v) { return __builtin_bit_cast(bf16x8, v); }
DI u32x4 asu(bf16x8 v) { return __builtin_bit_cast(u32x4, v); }
DI void regather4(u32x4 srd, u32 voff, u32 soff, bf16x8& f0, bf16x8& f1, bf16x8& f2, bf16x8& f3) {
  asm volatile("s_sleep 1\n\tbuffer_load_dwordx4 %0, %4, %5, %6 offen sc1\n\tbuffer_load_dwordx4 %1, %4, %5, %6 offen offset:1024 sc1\n\t"
               "buffer_load_dwordx4 %2, %4, %5, %6 offen offset:2048 sc1\n\tbuffer_load_dwordx4 %3, %4, %5, %6 offen offset:3072 sc1\n\ts_waitcnt vmcnt(0)"
               : "+v"(f0), "+v"(f1), "+v"(f2), "+v"(f3) : "v"(voff), "s"(srd), "s"(soff) : "memory");
}
DI void regather1(u32x4 srd, u32 voff, u32 soff, u32x4& f0) {
  asm volatile("buffer_load_dwordx4 %0, %1, %2, %3 offen sc1" : "=&v"(f0) : "v"(voff), "s"(srd), "s"(soff) : "memory");
}
DI u32 umax(u32 a, u32 b) { return a > b ? a : b; }
DI u32 max4(u32x4 v) { return umax(umax(v[0], v[1]), umax(v[2], v[3])); }

constexpr u64 SPIN_TICKS = 400000ull;
DI bool timed_out(u64 t0) { return __builtin_amdgcn_s_memrealtime() - t0 > SPIN_TICKS; }

#define STAMP(i) do { } while (0)
#define ASTAMP(it, pos) do { } while (0)
#define PSTAMP(sb, i) do { } while (0)
#define MFMA_GAP16(v) asm volatile("s_nop 7" : "+v"(v))
#define MFMA_GAP32(v) asm volatile("s_nop 7\n\ts_nop 3" : "+v"(v))

DI bool flags_ok(u32x4 a, u32x4 b, u32 lo, u32 hi) {
  return __all(a[0] == lo && a[1] == hi && a[2] == lo && a[3] == hi && b[0] == lo && b[1] == hi && b[2] == lo && b[3] == hi);
}
DI void wait_flags(rsrc_t wsr, u32 base, u32 taglo, u32 taghi, int lane, u64 t0) {
  for (;;) {
    const u32x4 a = ld16(wsr, lane * 32, base), b = ld16(wsr, lane * 32 + 16, base);
    if (flags_ok(a, b, taglo, taghi)) break;
    if (timed_out(t0)) break;
    __builtin_amdgcn_s_sleep(1);
    FENCE();
  }
}

DI bf16x8 ones_frag() { return asbf((u32x4){0x3F803F80u, 0x3F803F80u, 0x3F803F80u, 0x3F803F80u}); }
DI float diag_pick(f32x4 g, int lane) { const int r = lane & 3; float a = (r & 1) ? g[1] : g[0], b = (r & 1) ? g[3] : g[2]; return (r & 2) ? b : a; }

struct RowStat { float mu, rstd, var; };
DI RowStat finish_stat(float s, float q) {
  RowStat r; r.mu = s * (1.0f / 512.0f);
  r.var = fmaxf(q * (1.0f / 512.0f) - r.mu * r.mu, 0.0f);
  r.rstd = __builtin_amdgcn_rsqf(r.var + LN_EPS);
  return r;
}
DI bool unwritten(const bf16x8 (&f)[4]) {
  return __any(umax(umax(max4(asu(f[0])), max4(asu(f[1]))), umax(max4(asu(f[2])), max4(asu(f[3])))) == SENT);
}

template <int I, int N, class F> DI void static_for(F&& f) {
  if constexpr (I < N) { f(std::integral_constant<int, I>{}); static_for<I + 1, N>(f); }
}
struct Job { const char* src; u32 prow; u32 f; bool perm; };
template <int I> DI Job retile_job(int b, int w, const u16* wo, const u16* w1, const u16* w2, const u16* w3, const u16* w4) {
  auto small = [&](const u16* m, u32 g, u32 f0, bool perm) { return Job{(const char*)m + (u32)(16 * (g & 31) * 1024 + (g >> 5) * 64), 1024u, f0 + g, perm}; };
  auto big3 = [&](u32 g) { return Job{(const char*)w3 + (u32)(16 * (g & 31) * 3584 + (g >> 5) * 64), 3584u, 1536 + g, false}; };
  auto big4 = [&](u32 mm) { return Job{(const char*)w4 + (u32)(16 * ((b & 15) * 7 + mm) * 1024 + (b >> 4) * 64), 1024u, 3328 + 7 * b + mm, true}; };
  if constexpr (I == 0) return small(w < 2 ? wo : w1, 2 * b + (w & 1), w < 2 ? 0 : 512, w < 2);
  else if constexpr (I == 1) { const Job s = small(w2, 2 * b + (w & 1), 1024, false), t = big3(7 * b + (w & 1)); return w < 2 ? s : t; }
  else if constexpr (I == 2) return big3(7 * b + 2 + w);
  else if constexpr (I == 3) { const Job s = big3(7 * b + 6), t = big4(w - 1); return w == 0 ? s : t; }
  else return big4(3 + w);
}

#define ATT_PIPE 2
#define PRIO_IT 12
#define KV_SPREAD 2
#define CNT_U 2
#define NOMARK 0
#define MARK_IT 1
#define FLAG_IT 2
#define MARK_P1 2
#define MARK_ST 2
#define FM_IT 10
#define FM_CHK 14
#define KVW 0
#define P1_BAR 0
#define FLAG_AT 1
#define LD47 1
#define RTS_AT 2
#define KV_PRIO 0
#define KV_SLP 0
#define XSTAT 1
#define PFM 0
#define POLL_T 2
#define DLY -1
#define DELAY(p) do { if (DLY == (p)) { const u64 d0_ = __builtin_amdgcn_s_memrealtime(); while (__builtin_amdgcn_s_memrealtime() - d0_ < 200) __builtin_amdgcn_s_sleep(1); } } while (0)
#define MEASP -1
#define MEAS(p) do { if (MEASP == (p)) tmeas = __builtin_amdgcn_s_memrealtime(); } while (0)
#define OUT_AUX 2
#define OUT_ROWS 1
#define PRE_POS 0
#define PUT16 0
#define GB_POS 1
#define O_SLEEP 16
#define SLOT_POS 0
#define W4_WARM 0
#define FPI 6
#define U_IN 0
#define W4_PRIO Q_PRIO
#define A4_SPREAD 1
#define W3B_TOG 0
#define GB_PRIO Q_PRIO
#define UE_P 0
#define U_WIN 5
#define OUT_EARLY 1
#define MID_BAR 0
#define PQ 1
#define Q_PRIO 1
#ifndef M_PRIO
#define M_PRIO 1
#endif
#define U_INIT U_WIN
#define A4_LATE 0
#define STAT_POS 0
#define GB_PK 0
#define A3_N0 14
static_assert(33 + A3_N0 + (W4_WARM == 1 ? 7 : 0) <= 8 * FPI, "fetch schedule");
#define A4_N0 6

constexpr int marks_before(int it) { const int n = it <= MARK_IT ? 0 : (it - MARK_IT) * (4 / MARK_ST); return n < 15 ? n : 15; }
static_assert(MARK_P1 != 2 || (marks_before(FM_IT - 1) == 15 && FM_CHK > FM_IT && FM_CHK > 8 && FLAG_IT < 8), "mark schedule");

__global__ __launch_bounds__(512, 2) void fused_block_kernel(const u16* __restrict__ x, const u16* __restrict__ wq, const u16* __restrict__ wk,
                                                             const u16* __restrict__ wv, const u16* __restrict__ wo, const u16* __restrict__ w1,
                                                             const u16* __restrict__ w2, const u16* __restrict__ w3, const u16* __restrict__ w4,
                                                             u16* __restrict__ out, char* ws) {
  const u64 tA = MEASP == 0 ? __builtin_amdgcn_s_memrealtime() : 0;
  if (MEASP == 0) asm volatile("" :: "s"(tA));
  const int tid = threadIdx.x, lane = tid & 63, w = __builtin_amdgcn_readfirstlane(tid >> 6);
  const int b = blockIdx.x;
  const int cl = (b & 7) * 4 + (b >> 6), j = (b >> 3) & 7;
  const u32 lb = lds_base();
  const u32 l16 = lane * 16;
  u64 pc;
  asm volatile("s_getpc_b64 %0" : "=s"(pc));
  auto code_pf = [&]() { dma1((const void*)(pc & ~63ull), (u32)(((PFM == 1 ? (b >> 3) * 4 + (w & 3) : (b >> 3) * 8 + w) % CODE_KB) * 1024) + l16, lb + L_DUM1); };
  if (PFM == 0 || w >= 4) code_pf();
  asm volatile("" :: "s"(x), "s"(wq), "s"(wk), "s"(wv), "s"(wo), "s"(w1), "s"(w2), "s"(w3), "s"(w4), "s"(out), "s"(ws));
  const u64 t0 = __builtin_amdgcn_s_memrealtime();
  u64 tmeas = t0;
  const u64 c0 = MEASP == 99 ? __builtin_amdgcn_s_memtime() : 0;
  const int i16 = lane & 15, g4 = lane >> 4, r32 = lane & 31, h2 = lane >> 5;
  STAMP(0);

  auto blk_of = [&](int half) { return half * 32 + cl; };
  const int dlt = (j >> 1) == 0 ? 1 : (j >> 1) == 1 ? -1 : (j >> 1) == 2 ? 2 : -2;
  const int kvrt = j >> 1, kvmat = j & 1;
  auto grow = [&](int rho) { return (rho < 32 ? 0 : 1024 - 32) + 32 * cl + rho; };
  const int R0 = grow(16 * kvrt);
  u32x4 xk0, xk1, xk2, xk3;
  u64 tag;
  {
    if (KV_PRIO && w < 4) __builtin_amdgcn_s_setprio(KV_PRIO);
    if (KV_SLP && w >= 4) __builtin_amdgcn_s_sleep(KV_SLP);
    if (w < 4) {
      const u32 xo = (u32)(i16 * 1024 + g4 * 32);
      asm volatile("global_load_dwordx4 %0, %4, %5\n\tglobal_load_dwordx4 %1, %4, %5 offset:16\n\tglobal_load_dwordx4 %2, %4, %5 offset:512\n\tglobal_load_dwordx4 %3, %4, %5 offset:528"
                   : "=&v"(xk0), "=&v"(xk1), "=&v"(xk2), "=&v"(xk3) : "v"(xo), "s"((const char*)x + R0 * 1024 + w * 128) : "memory");
      dma2((const char*)(kvmat ? wv : wk) + w * 2048, l16, lb + L_WKV + w * 2048);
      dma2((const char*)(kvmat ? wv : wk) + (w + 4) * 2048, l16, lb + L_WKV + (w + 4) * 2048);
      if (PFM == 2) code_pf();
    }
    asm volatile("s_load_dwordx2 %0, %1, 0x0" : "=s"(tag) : "s"(ws + WS_EPOCH) : "memory");
#pragma unroll
    for (int pp = 0; pp < (XSTAT ? 4 : 2); ++pp) {
      if (LD47 ? w >= 4 : pp == 0) {
        const int v = LD47 ? (w & 3) + 4 * (pp & 1) : w;
        if (!XSTAT || pp < 2) {
          const char* xr = (const char*)x + grow(8 * v) * 1024 - 48;
          dma4_rows(xr, l16 + 48, l16 + 32, l16 + 16, l16, lb + L_XIMG + 8 * v * XP);
          dma4_rows(xr + 4096, l16 + 48, l16 + 32, l16 + 16, l16, lb + L_XIMG + (8 * v + 4) * XP);
        }
        if (!XSTAT || pp >= 2)
          dma_strided<8, false, 0x2000>((const char*)wq + v * 65536 + j * 128, (u32)((lane >> 3) * 1024 + (((lane & 7) ^ (4 * ((lane >> 4) & 1))) * 16)), lb + L_WQ + v * 8192, 1024);
      }
    }
    asm volatile("s_waitcnt lgkmcnt(0)" : "+s"(tag) :: "memory");
  }
  tag += 1ull;
  const u32 taglo = (u32)tag, taghi = (u32)(tag >> 32);
  const u64 kft = (tag << 16) | 0x5AA5ull;
  const rsrc_t wsr = mkrsrc(ws, WS_END);
  const u32x4 srd = {(u32)(uintptr_t)ws, (u32)((uintptr_t)ws >> 32) & 0xFFFFu, WS_END, 0x00020000u};
  const bf16x8 one = ones_frag();
  STAMP(1);
  MEAS(1);
  DELAY(6);

  if (w < 4) {
    asm volatile("s_waitcnt vmcnt(%4)" : "+v"(xk0), "+v"(xk1), "+v"(xk2), "+v"(xk3) : "n"((LD47 ? 0 : 16) + (PFM == 2)) : "memory");
    f32x4 am = {0, 0, 0, 0}, ac = {0, 0, 0, 0}, as = {0, 0, 0, 0}, aq = {0, 0, 0, 0};
    const u32 wa = L_WKV + w * 2048 + (16 * g4 + (i16 >> 2)) * 32 + (i16 & 3) * 8;
    bf16x8 wf[4];
#pragma unroll
    for (int t = 0; t < 4; ++t) { const u32 a = wa + (t >> 1) * 8192 + (t & 1) * 256; wf[t] = cat(ldstr(a), ldstr(a + 128)); }
    const bf16x8 xf[4] = {asbf(xk0), asbf(xk1), asbf(xk2), asbf(xk3)};
#pragma unroll
    for (int t = 0; t < 4; ++t) { as = mfma16(one, xf[t], as); aq = mfma16(xf[t], xf[t], aq); }
    if (kvmat) {
#pragma unroll
      for (int t = 0; t < 4; ++t) { am = mfma16(xf[t], wf[t], am); ac = mfma16(one, wf[t], ac); }
      MFMA_GAP16(am); MFMA_GAP16(ac);
      if (lane < 16) ldsw<float>(S_PC + (w * 16 + lane) * 4, ac[0]);
    } else {
#pragma unroll
      for (int t = 0; t < 4; ++t) { am = mfma16(wf[t], xf[t], am); ac = mfma16(wf[t], one, ac); }
      MFMA_GAP16(am); MFMA_GAP16(ac);
      if (i16 == 0) ldsw<f32x4>(S_PC + (w * 16 + 4 * g4) * 4, ac);
    }
    ldsw<f32x4>(S_PM + w * 1024 + l16, am);
    if (lane < 16) ldsw<float>(S_PS + (lane * 4 + w) * 4, as[0]);
    if ((i16 >> 2) == g4) ldsw<float>(S_PQ + (i16 * 4 + w) * 4, diag_pick(aq, lane));
    if (lane == 0) ldsw<u64>(S_KF + w * 8, kft);
  }
  if (w == KVW) {
    for (;;) {
      const u32x4 a = *(volatile LAS u32x4*)((LAS char*)smem + S_KF), c = *(volatile LAS u32x4*)((LAS char*)smem + S_KF + 16);
      if (flags_ok(a, c, (u32)kft, (u32)(kft >> 32)) || timed_out(t0)) break;
    }
    FENCE();
    const f32x4 s = ldsr<f32x4>(S_PS + i16 * 16), q = ldsr<f32x4>(S_PQ + i16 * 16);
    f32x4 m = {0, 0, 0, 0};
#pragma unroll
    for (int ww = 0; ww < 4; ++ww) m += ldsr<f32x4>(S_PM + ww * 1024 + l16);
    const RowStat st = finish_stat((s[0] + s[1]) + (s[2] + s[3]), (q[0] + q[1]) + (q[2] + q[3]));
    f32x4 v;
    u32 off;
    if (kvmat) {
      float c1 = 0.f;
#pragma unroll
      for (int ww = 0; ww < 4; ++ww) c1 += ldsr<float>(S_PC + (ww * 16 + i16) * 4);
      if (lane < 16) { ldsw<float>(S_FIN + lane * 4, st.mu); ldsw<float>(S_FIN + 64 + lane * 4, st.rstd); }
      asm volatile("s_waitcnt lgkmcnt(0)" ::: "memory");
      const f32x4 mu = ldsr<f32x4>(S_FIN + g4 * 16), rs = ldsr<f32x4>(S_FIN + 64 + g4 * 16);
      v = rs * (m - mu * c1);
      off = WS_V + (R0 >> 5) * 1024 + ((R0 >> 4) & 1) * 512 + (g4 & 1) * 256 + i16 * 16 + (g4 >> 1) * 8;
    } else {
      f32x4 c4 = {0, 0, 0, 0};
#pragma unroll
      for (int ww = 0; ww < 4; ++ww) c4 += ldsr<f32x4>(S_PC + (ww * 16 + 4 * g4) * 4);
      v = st.rstd * (m - st.mu * c4);
      off = WS_K + (R0 >> 5) * 1024 + (g4 & 1) * 512 + ((R0 & 31) + i16) * 16 + (g4 >> 1) * 8;
    }
    st8(wsr, off, 0, (u32x2){cvtpk(v[0], v[1]), cvtpk(v[2], v[3])});
    WAITV(1);
  } else if (MARK_P1 == 1 && w >= 4) {
      const rsrc_t mr = mkrsrc(ws, WS_END);
      const u32x4 s4 = {SENT, SENT, SENT, SENT};
      const int wm = w & 3;
#pragma unroll
      for (int half = 0; half < 2; ++half) {
        const u32 po = blk_of(half) * 32768 + (4 * j + wm) * 1024;
        __builtin_amdgcn_raw_buffer_store_b128(s4, mr, l16, WS_OX + po, 16);
        __builtin_amdgcn_raw_buffer_store_b128(s4, mr, l16, WS_HX + po, 16);
        __builtin_amdgcn_raw_buffer_store_b128(s4, mr, l16, WS_X1 + po, 16);
        const int R = blk_of(half) * 32 + (lane & 31) + dlt;
        const u32 mk = (u32)R < 2048u ? SENT : 0u;
        st16(mr, ((R & 2047) >> 5) * 32768 + (lane >> 5) * 512 + (R & 31) * 16, WS_TS + (4 * j + wm) * 1024, (u32x4){mk, mk, mk, mk});
      }
      const u32 uo = WS_UX + (blk_of(wm >> 1) * 112 + 14 * j + 7 * (wm & 1)) * 1024;
#pragma unroll
      for (int i = 0; i < 7; ++i) st16(mr, l16 + (i & 3) * 1024, uo + (i >> 2) * 4096, s4);
    WAITV(15);
  } else {
    if (XSTAT && w >= 4) {
      WAITV(16);
      const u32 row = 8 * (w & 3) + (i16 & 7) + 32 * (i16 >> 3), xs = L_XIMG + row * XP + g4 * 16;
      bf16x8 xf[16];
#pragma unroll
      for (int t = 0; t < 16; ++t) xf[t] = ldsr<bf16x8>(xs + t * 64);
      f32x4 as = {0, 0, 0, 0}, aq = {0, 0, 0, 0};
#pragma unroll
      for (int t = 0; t < 16; ++t) { as = mfma16(one, xf[t], as); aq = mfma16(xf[t], xf[t], aq); }
      MFMA_GAP16(as); MFMA_GAP16(aq);
      if (lane < 16) ldsw<float>(S_QS + row * 16, as[0]);
      if ((i16 >> 2) == g4) ldsw<float>(S_QS + row * 16 + 4, diag_pick(aq, lane));
    }
    WAITV(0);
  }
  const u32 pv = (((u32)lane >> 4) ^ (u32)lane) & 1 ? 0u : 0x3F803F80u;
  const bf16x8 par = asbf((u32x4){pv, pv, pv, pv});
  auto put_stat = [&](const f32x4& rsum, const f32x16& gram, u32 as_, u32 aq_) {
    const u32 dump = L_DUM1 + l16;
    ldsw<float>(lane < 32 ? as_ : dump, __uint_as_float(__builtin_amdgcn_permlane16_swap(__float_as_uint(rsum[0]), __float_as_uint(rsum[1]), false, false)[0]));
    float d[4];
#pragma unroll
    for (int a = 0; a < 4; ++a) d[a] = diag_pick((f32x4){gram[4 * a], gram[4 * a + 1], gram[4 * a + 2], gram[4 * a + 3]}, lane);
    const float lo = (lane & 8) ? d[1] : d[0], hi = (lane & 8) ? d[3] : d[2];
    ldsw<float>(h2 == ((lane >> 2) & 1) ? aq_ : dump, (lane & 16) ? hi : lo);
  };
#define JOB(i) retile_job<i>(b, w, wo, w1, w2, w3, w4)
  u32x4 rts[5];
  auto rts_load = [&]() {
    if (w < 4) {
      static_for<0, 5>([&](auto I) {
        const Job jb = JOB(I.value);
        rts[I.value] = *(const GAS u32x4*)(jb.src + (u32)((lane >> 2) * jb.prow + (lane & 3) * 16));
      });
    }
  };
  if (RTS_AT == 0) rts_load();
  FENCE();
  STAMP(2);
  const int q_hp = w & 1, q_rt = (w >> 1) & 1, q_kh = w >> 2;
  const int head = 2 * q_hp + q_kh;
  bf16x8 qf;
  u32x4 fka, fkb;
  {
    auto kv_flag = [&]() {
      u64 keep;
      asm volatile("s_cmp_lg_u32 %[w], %[k]\n\ts_cbranch_scc1 .Lfl%=\n\ts_waitcnt vmcnt(%[n])\n\ts_mov_b64 %[e], exec\n\ts_mov_b64 exec, 0xff\n\t"
                   "global_store_dwordx2 %[v], %[t], %[s] sc1\n\ts_mov_b64 exec, %[e]\n.Lfl%=:"
                   : [e] "=&s"(keep) : [w] "s"(w), [k] "n"(KVW), [n] "n"(KVW < 4 && (RTS_AT == 0 || (RTS_AT == 1 && FLAG_AT >= 1)) ? 5 : 0), [v] "v"((u32)lane * 2048u), [t] "v"(tag), [s] "s"(ws + WS_FKV + b * 8) : "scc", "memory");
    };
    if (FLAG_AT == 0) kv_flag();
    if (KV_PRIO && w < 4) __builtin_amdgcn_s_setprio(0);
    if (RTS_AT == 1) rts_load();
    BAR();
    if (RTS_AT == 2 && FLAG_AT <= 1) { if (FLAG_AT == 1) kv_flag(); rts_load(); }
    STAMP(3);
    MEAS(3);
    DELAY(1);
    if (FLAG_AT == 1 && RTS_AT != 2) kv_flag();
    if (tid < 272) ldsw<u32x4>(L_ONES + tid * 16, (u32x4){0x3F803F80u, 0x3F803F80u, 0x3F803F80u, 0x3F803F80u});
    if (tid == 511) ldsw<u32x4>(L_CNT, (u32x4){0u, 0u, 0u, 0u});
    if (tid >= 508) ldsw<u64>(S_KF + (tid - 508) * 8, ~kft);
    f32x16 acc = {}, gram = {};
    f32x4 rsum = {0, 0, 0, 0}, csum = {0, 0, 0, 0};
    const int q = i16 >> 2, p = i16 & 3, cb = g4 & 1, rho = q_hp ^ q_rt;
    const u32 wa = L_WQ + (256 * q_kh + 8 * h2 + q) * 128 + (((4 * q_hp + 2 * cb + (p >> 1)) ^ (4 * ((q >> 1) & 1))) * 16) + (p & 1) * 8;
    const u32 xa = L_XIMG + (32 * q_rt + r32) * XP + q_kh * 512 + h2 * 16;
    const u32 wa1 = wa + rho * 16384, xa1 = xa + rho * 256, wa2 = wa + (1 - rho) * 16384, xa2 = xa + (1 - rho) * 256;
#pragma unroll
    for (int t = 0; t < 8; ++t) {
      const bf16x8 af = cat(ldstr(wa1 + t * 2048), ldstr(wa1 + t * 2048 + 512)), xf = ldsr<bf16x8>(xa1 + t * 32);
      acc = mfma32(af, xf, acc);
      if (!XSTAT) {
        rsum = mfma16(par, xf, rsum);
        gram = mfma32(xf, xf, gram);
      }
      csum = mfma16(af, par, csum);
    }
    bf16x8 af2[8], xf2[8];
#pragma unroll
    for (int t = 0; t < 8; ++t) { af2[t] = cat(ldstr(wa2 + t * 2048), ldstr(wa2 + t * 2048 + 512)); xf2[t] = ldsr<bf16x8>(xa2 + t * 32); }
    __builtin_amdgcn_sched_barrier(0);
    if (FLAG_AT == 2) kv_flag();
    RowStat st = {0.f, 0.f, 0.f};
#pragma unroll
    for (int t = 0; t < 8; ++t) {
      acc = mfma32(af2[t], xf2[t], acc);
      if (t == POLL_T) {
        const u32 po = w == 1 ? (u32)lane * 32 : OOB;
        fka = ld16(wsr, po, WS_FKV + (b & 7) * 2048); fkb = ld16(wsr, po + 16, WS_FKV + (b & 7) * 2048);
      }
      if (XSTAT && t == 4) {
        const f32x2 sp = ldsr<f32x2>(S_QS + (32 * q_rt + r32) * 16);
        st = finish_stat(sp[0], sp[1]);
      }
      if (t == 1) {
        const u32 slot = q_kh * 2 + q_hp, dump = L_DUM1 + l16;
        if (!XSTAT) {
          MFMA_GAP32(gram); MFMA_GAP16(rsum);
          put_stat(rsum, gram, S_QS + ((32 * q_rt + r32) * 4 + slot) * 4, S_QQ + ((32 * q_rt + r32) * 4 + slot) * 4);
        }
        MFMA_GAP16(csum);
        ldsw<f32x4>(i16 < 2 ? S_QC + (((q_hp * 2 + q_rt) * 2 + q_kh) * 32 + 16 * i16 + 4 * g4) * 4 : dump, csum);
      }
      __builtin_amdgcn_sched_barrier(0);
    }
    MFMA_GAP32(acc);
    f32x4 k0, k1, s0, s1;
    if (q_kh) {
      k0 = (f32x4){acc[8], acc[9], acc[10], acc[11]}; k1 = (f32x4){acc[12], acc[13], acc[14], acc[15]};
      s0 = (f32x4){acc[0], acc[1], acc[2], acc[3]}; s1 = (f32x4){acc[4], acc[5], acc[6], acc[7]};
    } else {
      k0 = (f32x4){acc[0], acc[1], acc[2], acc[3]}; k1 = (f32x4){acc[4], acc[5], acc[6], acc[7]};
      s0 = (f32x4){acc[8], acc[9], acc[10], acc[11]}; s1 = (f32x4){acc[12], acc[13], acc[14], acc[15]};
    }
    ldsw<f32x4>(L_WKV + w * 2048 + l16, s0); ldsw<f32x4>(L_WKV + w * 2048 + 1024 + l16, s1);
    if (w == 1) {
      if (!flags_ok(fka, fkb, taglo, taghi)) wait_flags(wsr, WS_FKV + (b & 7) * 2048, taglo, taghi, lane, t0);
      if (b == 0 && lane == 0) __hip_atomic_store((GAS u64*)(ws + WS_EPOCH), tag, __ATOMIC_RELAXED, __HIP_MEMORY_SCOPE_AGENT);
    }
    const u32 ca = opaque<u32>(S_QC + (q_hp * 4 * 32 + 16 * q_kh + 4 * h2) * 4), ea = opaque<u32>(L_WKV + (w ^ 4) * 2048 + l16);
    BAR();
    STAMP(5);
    MEAS(5);
    const f32x4 e0 = ldsr<f32x4>(ea), e1 = ldsr<f32x4>(ea + 1024);
    f32x4 cp[8];
#pragma unroll
    for (int s = 0; s < 4; ++s) { cp[s] = ldsr<f32x4>(ca + s * 128); cp[4 + s] = ldsr<f32x4>(ca + s * 128 + 32); }
    dma_strided<KV_SPREAD ? 2 : 3, true, 0x2000>(ws + WS_K, w * 1024 + l16, lb + L_K + w * 1024, 8192);
    dma_strided<2, true, 0x2000>(ws + WS_V, w * 1024 + l16, lb + L_V + w * 1024, 8192);
    __builtin_amdgcn_sched_barrier(0);
    k0 += e0; k1 += e1;
    const f32x4 c0 = (cp[0] + cp[1]) + (cp[2] + cp[3]), c1 = (cp[4] + cp[5]) + (cp[6] + cp[7]);
    if (!XSTAT) {
      const f32x4 sp = ldsr<f32x4>(S_QS + (32 * q_rt + r32) * 16), qp = ldsr<f32x4>(S_QQ + (32 * q_rt + r32) * 16);
      st = finish_stat((sp[0] + sp[1]) + (sp[2] + sp[3]), (qp[0] + qp[1]) + (qp[2] + qp[3]));
    }
    const float sc = st.rstd * QSCALE;
    k0 = (k0 - st.mu * c0) * sc; k1 = (k1 - st.mu * c1) * sc;
    qf = opaque(asbf((u32x4){cvtpk(k0[0], k0[1]), cvtpk(k0[2], k0[3]), cvtpk(k1[0], k1[1]), cvtpk(k1[2], k1[3])}));
  }
  STAMP(4);
  const u32 pc_in = (u32)((4 * j + (w >> 1)) * 1024 + (w & 1) * 512);
  auto hx_so = [&](u32 base, int half) { return base + blk_of(half) * 32768 + pc_in; };
  auto ts_vo = [&](int half) {
    const u32 l = opaque<u32>(lane);
    const int rr = (int)(l >> 1) + dlt;
    const u32 R = (u32)(blk_of(half) * 32 + rr);
    const u32 o = (u32)((rr >> 5) + 1) * 32768 + (u32)(rr & 31) * 16 + (l & 1) * 8;
    return R < 2048u && !(PUT16 && (l & 1)) ? o : OOB;
  };
  auto ox_off = [&]() { return WS_OX + blk_of(q_rt) * 32768 + (4 * j + head) * 1024 + l16; };
  {
    WAITV(0);
    asm volatile("" : "+v"(rts[0]), "+v"(rts[1]), "+v"(rts[2]), "+v"(rts[3]), "+v"(rts[4]), "+v"(fka), "+v"(fkb));
    BAR();
    STAMP(6);
    MEAS(6);
    DELAY(2);
  }
  u32 opk[4];
  bf16x8 a_o[8], a_1[8], a_2[8], a3[32];
  u32x2 xres[2];
  {
    u32 ka = L_K + l16;
    const bool isv = r32 < 16;
    u32 va = isv ? L_V + h2 * 256 + r32 * 16 : L_ONES + ((r32 >= 20 && r32 < 28) ? 64 : 0);
    const u32 vinc = isv ? 4096u : 0u;
    f32x16 o = {};
    f32x16 negm;
    {
      const bf16x8 kf = ldsr<bf16x8>(ka);
      f32x16 z = {};
      const f32x16 s = mfma32(kf, qf, z);
      float m = fmaxf(fmaxf(s[0], s[1]), fmaxf(s[2], s[3]));
#pragma unroll
      for (int i = 4; i < 16; i += 2) m = fmaxf(m, fmaxf(s[i], s[i + 1]));
      const auto rr = __builtin_amdgcn_permlane32_swap(__float_as_uint(m), __float_as_uint(m), false, false);
      m = fmaxf(__uint_as_float(rr[0]), __uint_as_float(rr[1]));
#pragma unroll
      for (int i = 0; i < 16; ++i) negm[i] = -m;
    }
    f32x16 s = mfma32(ldsr<bf16x8>(ka), qf, negm);
    bf16x8 kf = ldsr<bf16x8>(ka + 1024), v0 = ldsr<bf16x8>(va), v1 = ldsr<bf16x8>(va + 512);
#define SB() __builtin_amdgcn_sched_barrier(0)
    auto tiles4 = [&](auto&& hook) {
#pragma unroll
      for (int u = 0; u < 4; ++u) {
        float e[16];
        u32 pk[8];
        f32x16 sn;
        auto ex = [&](int a, int b_) {
#pragma unroll
          for (int i = a; i < b_; ++i) e[i] = opaque(__builtin_amdgcn_exp2f(s[i]));
        };
        auto cv = [&](int a, int b_) {
#pragma unroll
          for (int i = a; i < b_; ++i) pk[i] = opaque(cvtpk(e[2 * i], e[2 * i + 1]));
        };
        auto ms = [&]() { sn = mfma32(kf, qf, negm); asm volatile("" : "+v"(sn)); kf = ldsr<bf16x8>(ka + (u + 2) * 1024); };
        if (u == 3) va += vinc;
        const u32 vn = va + ((u + 1) & 3) * 1024;
        auto m0 = [&]() { o = mfma32(v0, asbf((u32x4){pk[0], pk[1], pk[2], pk[3]}), o); asm volatile("" : "+v"(o)); v0 = ldsr<bf16x8>(vn); };
        auto m1 = [&]() { o = mfma32(v1, asbf((u32x4){pk[4], pk[5], pk[6], pk[7]}), o); asm volatile("" : "+v"(o)); v1 = ldsr<bf16x8>(vn + 512); };
        if (ATT_PIPE == 1) {
          ex(0, 4); SB(); ms(); hook(u); SB(); ex(4, 8); cv(0, 4); SB(); m0(); SB(); ex(8, 16); cv(4, 8); SB(); m1(); SB();
        } else if (ATT_PIPE == 2) {
          ex(0, 4); SB(); ms(); hook(u); SB(); ex(4, 8); cv(0, 4); ex(8, 10); SB(); m0(); SB(); ex(10, 16); cv(4, 8); SB(); m1(); SB();
        } else if (ATT_PIPE == 3) {
          ms(); hook(u); SB(); ex(0, 8); cv(0, 4); SB(); m0(); SB(); ex(8, 16); cv(4, 8); SB(); m1(); SB();
        } else if (ATT_PIPE == 4) {
          ex(0, 6); cv(0, 2); SB(); ms(); hook(u); SB(); ex(6, 8); cv(2, 4); ex(8, 12); SB(); m0(); SB(); ex(12, 16); cv(4, 8); SB(); m1(); SB();
        } else {
          ex(0, 4); cv(0, 2); SB(); ms(); hook(u); SB(); ex(4, 8); cv(2, 4); SB(); m0(); SB(); ex(8, 12); cv(4, 6); ex(12, 16); cv(6, 8); SB(); m1(); SB();
        }
        s = sn;
      }
      ka += 4096;
    };
    if (w < 4) __builtin_amdgcn_s_setprio(2); else __builtin_amdgcn_s_setprio(1);
    auto kv_rest = [&](u32 img, u32 limg, int c0, auto n) {
      dma_strided<n.value, true, 4096>(ws + img + c0 * 8192, opaque<u32>((w & 3) * 1024 + l16), lb + limg + c0 * 8192 + (w & 3) * 1024, 4096);
    };
    using N2 = std::integral_constant<int, 2>; using N4 = std::integral_constant<int, 4>;
    auto lds_count = [&](u32 off) {
      u32 n;
      asm volatile("ds_add_rtn_u32 %0, %1, %2\n\ts_waitcnt lgkmcnt(0)" : "=v"(n) : "v"(lb + off), "v"(lane == 0 ? 1u : 0u) : "memory");
      return (u32)__builtin_amdgcn_readfirstlane(n);
    };
    const u32x4 s4 = {SENT, SENT, SENT, SENT};
    const u32 kvo = opaque<u32>((w & 3) * 1024 + l16);
    static_assert(WS_V - WS_K == 0x10000 && L_V - L_K == 0x10000, "K and V images");
    u32x4 fa, fb;
    auto fetch = [&](int p, u32 l) {
      const u32 fo = (2 * j * 32 + 4 * w) * 1024;
      if (p < 24) {
        const int t = p & 7;
        const bf16x8 v = asbf(ld16(wsr, l + (t & 3) * 1024, (p < 8 ? WS_WO : p < 16 ? WS_W1 : WS_W2) + fo + (t >> 2) * 32768));
        if (p < 8) a_o[t] = v; else if (p < 16) a_1[t] = v; else a_2[t] = v;
      } else if (p < 26) {
        xres[p - 24] = *(const GAS u32x2*)((const char*)x + (u32)((blk_of(p - 24) * 32 + (l >> 5)) * 1024 + (64 * j + 8 * w) * 2 + ((l >> 4) & 1) * 8));
      } else if (p < 26 + A3_N0) {
        a3[p - 26] = asbf(ld16(wsr, w < 7 ? ((7 * j + w) * 32) * 1024 + l : OOB, WS_W3 + (p - 26) * 1024));
      } else if (p < 33 + A3_N0) {
        dma1(ws + WS_W3 + (p - 26 - A3_N0) * 1024, (((u32)(b >> 6) & 3u) * 8 + w) * 7168 + j * 229376 + l, lb + L_DUM1);
      } else if (W4_WARM == 1 && p < 40 + A3_N0) {
        dma1(ws + WS_W4 + (p - 33 - A3_N0) * 1024, (((u32)(b >> 6) & 3u) * 8 + w) * 7168 + j * 229376 + l, lb + L_DUM1);
      }
    };
    static_for<0, 16>([&](auto IT) {
      constexpr int it = IT.value;
      ASTAMP(it, 0);
      if (it == 6) DELAY(3);
      if (it == 10) DELAY(4);
      if (w < 4) {
        if (it == 0) {
          const u32 l = opaque<u32>(lane);
          const u32 sb = L_STG + w * 5120;
#pragma unroll
          for (int i = 0; i < 5; ++i) ldsw<u32x4>(sb + i * 1024 + l * 16, rts[i]);
          const u32 tb = sb + ((l & 15) >> 2) * 64 + ((l >> 4) & 1) * 32 + (l & 3) * 8;
          static_for<0, 5>([&](auto I) {
            const Job jb = JOB(I.value);
            const u32 t0a = tb + I.value * 1024 + (l >> 5) * (jb.perm ? 256 : 512);
            st16(wsr, l * 16, WS_WO + jb.f * 1024, asu(cat(ldstr(t0a), ldstr(t0a + (jb.perm ? 512 : 256)))));
          });
        } else if (it == MARK_IT && !NOMARK && MARK_P1 == 0) {
          const u32 l = opaque<u32>(lane);
#pragma unroll
          for (int half = 0; half < 2; ++half) {
            const u32 po = blk_of(half) * 32768 + (4 * j + w) * 1024;
            st16(wsr, l * 16, WS_OX + po, s4); st16(wsr, l * 16, WS_HX + po, s4); st16(wsr, l * 16, WS_X1 + po, s4);
            const int R = blk_of(half) * 32 + (int)(l & 31) + dlt;
            const u32 mk = (u32)R < 2048u ? SENT : 0u;
            st16(wsr, ((R & 2047) >> 5) * 32768 + (l >> 5) * 512 + (R & 31) * 16, WS_TS + (4 * j + w) * 1024, (u32x4){mk, mk, mk, mk});
          }
        } else if (it == MARK_IT + 1 && !NOMARK && MARK_P1 == 0) {
          const u32 uo = WS_UX + (blk_of(w >> 1) * 112 + 14 * j + 7 * (w & 1)) * 1024;
          const u32 l = opaque<u32>(l16);
#pragma unroll
          for (int i = 0; i < 7; ++i) st16(wsr, l + (i & 3) * 1024, uo + (i >> 2) * 4096, s4);
        } else if (it == FLAG_IT) {
          asm volatile("s_waitcnt vmcnt(%0)" :: "n"(MARK_P1 == 2 ? marks_before(FLAG_IT) : 0) : "memory");
          if (lds_count(L_CNT + 4) == 3 && lane < 8) __hip_atomic_store((GAS u64*)(ws + WS_FW + lane * 2048 + b * 8), tag, __ATOMIC_RELAXED, __HIP_MEMORY_SCOPE_AGENT);
        } else if (MARK_P1 == 2 && it == FM_IT) {
          asm volatile("s_waitcnt vmcnt(%0)" :: "n"(FM_IT - 1 >= 8 ? FPI : 0) : "memory");
          if (lds_count(L_CNT + 12) == 3 && lane < 8) __hip_atomic_store((GAS u64*)(ws + WS_FM + lane * 2048 + b * 8), tag, __ATOMIC_RELAXED, __HIP_MEMORY_SCOPE_AGENT);
        }
      } else {
        if (KV_SPREAD) {
          if (KV_SPREAD == 1 && it >= 1 && it <= 3) {
            WAITV(0);
            if (lane == 0) asm volatile("ds_add_u32 %0, %1" :: "v"(lb + L_CNT), "v"(1u) : "memory");
          }
        } else if (it == 0) {
          kv_rest(WS_V, L_V, 2, N4{}); kv_rest(WS_K, L_K, 3, N4{}); kv_rest(WS_V, L_V, 4, N4{}); kv_rest(WS_K, L_K, 5, N4{});
          kv_rest(WS_V, L_V, 6, N4{}); kv_rest(WS_K, L_K, 7, N2{});
        } else if (it == 1) {
          WAITV(0);
          lds_count(L_CNT);
        }
      }
      if (KV_SPREAD ? (it & 3) == 3 && it < 12 : it == (ATT_PIPE ? 3 : 4)) {
        while ((u32)__builtin_amdgcn_readfirstlane(*(volatile LAS u32*)((LAS char*)smem + L_CNT)) < (KV_SPREAD ? (u32)(it + 1) : 4u)) {
          if (timed_out(t0)) break;
          __builtin_amdgcn_s_sleep(1);
        }
        FENCE();
      }
      if (it == 7) {
        const u32 l = opaque<u32>(l16);
        fa = ld16(wsr, l * 2, WS_FW + (b & 7) * 2048); fb = ld16(wsr, l * 2 + 16, WS_FW + (b & 7) * 2048);
      }
      if (it == 8) {
        if (!flags_ok(fa, fb, taglo, taghi)) wait_flags(wsr, WS_FW + (b & 7) * 2048, taglo, taghi, lane, t0);
      }
      if (MARK_P1 == 2 && it == FM_CHK - 1) {
        const u32 l = opaque<u32>(l16);
        fa = ld16(wsr, l * 2, WS_FM + (b & 7) * 2048); fb = ld16(wsr, l * 2 + 16, WS_FM + (b & 7) * 2048);
      }
      if (MARK_P1 == 2 && it == FM_CHK) {
        if (!flags_ok(fa, fb, taglo, taghi)) wait_flags(wsr, WS_FM + (b & 7) * 2048, taglo, taghi, lane, t0);
      }
      if (it >= 8) {
        const u32 l = opaque<u32>(l16);
#pragma unroll
        for (int p = FPI * (it - 8); p < FPI * (it - 7); ++p) fetch(p, l);
      }
      if (it == PRIO_IT) asm volatile("s_cmp_ge_u32 %0, 4\n\ts_cbranch_scc1 .Lpe%=\n\ts_setprio 0\n.Lpe%=:" :: "s"(w) : "scc");
      FENCE();
      ASTAMP(it, 1);
      tiles4([&](int u) {
        if (KV_SPREAD == 3 && it == 1 && u == 0) count_late<0>(w, lb + L_CNT);
        if (KV_SPREAD >= 2 && it >= (KV_SPREAD == 3 ? 2 : 1) && it <= 3 && u == CNT_U) count_late<(it < 3 ? 2 * CNT_U : 0)>(w, lb + L_CNT);
        if (MARK_P1 == 2 && it >= MARK_IT && (u % MARK_ST) == 0) {
          const int n = marks_before(it) + u / MARK_ST;
          if (n < 8) {
            const int half = n >> 2, kind = n & 3;
            const u32 l = opaque<u32>(lane);
            if (kind < 3) st16_early(w, srd, l * 16, (kind == 0 ? WS_OX : kind == 1 ? WS_HX : WS_X1) + blk_of(half) * 32768 + (4 * j + w) * 1024, s4);
            else {
              const int R = blk_of(half) * 32 + (int)(l & 31) + dlt;
              const u32 mk = (u32)R < 2048u ? SENT : 0u;
              st16_early(w, srd, ((R & 2047) >> 5) * 32768 + (l >> 5) * 512 + (R & 31) * 16, WS_TS + (4 * j + w) * 1024, (u32x4){mk, mk, mk, mk});
            }
          } else if (n < 15) {
            const int i = n - 8;
            st16_early(w, srd, opaque<u32>(l16), WS_UX + (blk_of(w >> 1) * 112 + 14 * j + 7 * (w & 1) + i) * 1024, s4);
          }
        }
        if (KV_SPREAD == 3 && it == 0) {
          if (u == 0) {
#pragma unroll
            for (int uu = 0; uu < 4; ++uu) dma_pair_late(w, ws + WS_K + (16 + 4 * uu) * 1024, kvo, lb + L_K + (16 + 4 * uu) * 1024 + (w & 3) * 1024);
          }
        } else if (KV_SPREAD && it < 3) {
          const int T = 16 + 16 * it + 4 * u;
          dma_pair_late(w, ws + WS_K + T * 1024, kvo, lb + L_K + T * 1024 + (w & 3) * 1024);
        }
      });
      FENCE();
      if (it == 9) STAMP(7);
    });
    STAMP(8);
    MEAS(8);
    __builtin_amdgcn_s_setprio(0);
    ka = L_K + l16;
    float l = o[8];
    if (!__all(l <= 1.8446744e19f)) {
      float m = -INFINITY;
      o = (f32x16){};
      u32 vb = isv ? L_V + h2 * 256 + r32 * 16 : L_ONES;
      for (int t = 0; t < 64; ++t) {
        f32x16 z = {};
        const f32x16 sx = mfma32(ldsr<bf16x8>(ka + t * 1024), qf, z);
        float mt = sx[0];
#pragma unroll
        for (int i = 1; i < 16; ++i) mt = fmaxf(mt, sx[i]);
        const auto rr = __builtin_amdgcn_permlane32_swap(__float_as_uint(mt), __float_as_uint(mt), false, false);
        mt = fmaxf(fmaxf(__uint_as_float(rr[0]), __uint_as_float(rr[1])), m);
        const float f = __builtin_amdgcn_exp2f(m - mt);
        m = mt;
        u32 pk[8];
#pragma unroll
        for (int i = 0; i < 8; ++i) pk[i] = cvtpk(__builtin_amdgcn_exp2f(sx[2 * i] - m), __builtin_amdgcn_exp2f(sx[2 * i + 1] - m));
#pragma unroll
        for (int i = 0; i < 16; ++i) o[i] *= f;
        o = mfma32(ldsr<bf16x8>(vb), asbf((u32x4){pk[0], pk[1], pk[2], pk[3]}), o);
        o = mfma32(ldsr<bf16x8>(vb + 512), asbf((u32x4){pk[4], pk[5], pk[6], pk[7]}), o);
        vb += isv ? 1024u : 0u;
      }
      MFMA_GAP32(o);
      l = o[8];
    }
    const float rl = __builtin_amdgcn_rcpf(l);
#pragma unroll
    for (int i = 0; i < 4; ++i) opk[i] = canon(cvtpk(o[2 * i] * rl, o[2 * i + 1] * rl));
  }
  st16(wsr, ox_off(), 0, (u32x4){opk[0], opk[1], opk[2], opk[3]});
  STAMP(9);
  bf16x8 sel;
  auto make_sel = [&]() {
    const u32 l = opaque<u32>(lane), e = ((l >> 2) & 3) != (l >> 4) ? 0u : (l & 1) ? 0x3F800000u : 0x00003F80u;
    const u32 d0 = (l & 2) ? 0u : e, d1 = (l & 2) ? e : 0u;
    sel = asbf((u32x4){d0, d1, d0, d1});
  };
  make_sel();
  const u32 gvo = opaque<u32>(w * 4096 + l16) & 0x73F0u;
  auto pw_of = [&](u32 l) { return L_P + ((l >> 3) & 3) * 256 + (w >> 1) * 1024 + (l & 7) * 32 + (l >> 5) * 16 + (((w ^ (l >> 3)) & 1) * 8); };
  const u32 pw = pw_of(lane);
  const u32 pr = L_P + gvo;
  auto g_load = [&](bf16x8 (&f)[4], u32 src) {
#pragma unroll
    for (int t = 0; t < 4; ++t) f[t] = asbf(ld16(wsr, gvo | (t * 1024), src));
  };
  auto g_wait = [&](bf16x8 (&f)[4], u32 src) {
    if (__builtin_expect(unwritten(f), 0)) {
      do {
        if (timed_out(t0)) break;
        regather4(srd, gvo, src, f[0], f[1], f[2], f[3]);
      } while (unwritten(f));
    }
    asm volatile("" : "+v"(f[0]), "+v"(f[1]), "+v"(f[2]), "+v"(f[3]));
  };
  auto row_stats = [&](int half, const bf16x8 (&f)[4]) {
    float s0 = 0.f, s1 = 0.f, q0 = 0.f, q1 = 0.f;
#pragma unroll
    for (int t = 0; t < 4; ++t) {
      const u32x4 d = asu(f[t]);
      s0 = dot2(d[0], 0x3F803F80u, s0); q0 = dot2(d[0], d[0], q0); s1 = dot2(d[1], 0x3F803F80u, s1); q1 = dot2(d[1], d[1], q1);
      s0 = dot2(d[2], 0x3F803F80u, s0); q0 = dot2(d[2], d[2], q0); s1 = dot2(d[3], 0x3F803F80u, s1); q1 = dot2(d[3], d[3], q1);
    }
    const f32x2 sq = {swap32_add(s0 + s1), swap32_add(q0 + q1)};
    if (lane < 32) ldsw<f32x2>(L_STAT + half * STH + lane * STP + w * 8, sq);
  };
  auto partials = [&](int half, f32x16& acc0, f32x16& acc1, u32 pw) {
#pragma unroll
    for (int a = 0; a < 4; ++a) {
      ldsw<u32x2>(pw + half * 32768 + a * 4096, (u32x2){cvtpk(acc0[4 * a], acc0[4 * a + 1]), cvtpk(acc0[4 * a + 2], acc0[4 * a + 3])});
      ldsw<u32x2>(pw + half * 32768 + (4 + a) * 4096, (u32x2){cvtpk(acc1[4 * a], acc1[4 * a + 1]), cvtpk(acc1[4 * a + 2], acc1[4 * a + 3])});
    }
  };
  const u32 stw = opaque<u32>(L_STAT + r32 * STP + w * 8);
  auto slice_gemm = [&](int half, const bf16x8 (&a)[8], const bf16x8 (&f)[4], bool stats, auto&& mid) {
    f32x16 acc0 = {}, acc1 = {};
    auto stat_block = [&]() {
      float s0 = 0.f, s1 = 0.f, q0 = 0.f, q1 = 0.f;
#pragma unroll
      for (int t = 0; t < 4; ++t) {
        const u32x4 d = asu(f[t]);
        s0 = dot2(d[0], 0x3F803F80u, s0); q0 = dot2(d[0], d[0], q0); s1 = dot2(d[1], 0x3F803F80u, s1); q1 = dot2(d[1], d[1], q1);
        s0 = dot2(d[2], 0x3F803F80u, s0); q0 = dot2(d[2], d[2], q0); s1 = dot2(d[3], 0x3F803F80u, s1); q1 = dot2(d[3], d[3], q1);
      }
      ldsw<f32x2>(stw + half * STH, (f32x2){swap32_add(s0 + s1), swap32_add(q0 + q1)});
      __builtin_amdgcn_sched_barrier(0);
    };
    if (stats && STAT_POS == 0) stat_block();
#pragma unroll
    for (int t = 0; t < 4; ++t) {
      acc0 = mfma32(a[t], f[t], acc0);
      acc1 = mfma32(a[4 + t], f[t], acc1);
    }
    __builtin_amdgcn_sched_barrier(0);
    if (stats && STAT_POS == 1) stat_block();
    mid();
    __builtin_amdgcn_sched_barrier(0);
    partials(half, acc0, acc1, pw);
  };
  auto reduce_at = [&](int half, u32 pr_) -> f32x4 {
    f32x4 acc = {0, 0, 0, 0};
#pragma unroll
    for (int q = 0; q < 4; ++q) acc = mfma16(sel, ldsr<bf16x8>(pr_ + half * 32768 + q * 1024), acc);
    return acc;
  };
  auto reduce = [&](int half) { return reduce_at(half, pr); };
  auto src_of = [&](u32 base, int half) { return base + blk_of(half) * 32768; };
  const u32 hvo = opaque<u32>(PUT16 && (lane & 1) ? OOB : (u32)lane * 8);
  auto put_at = [&](u32 vo, u32 so, u32x2 v) {
    if (PUT16) {
      const u32 n0 = __builtin_amdgcn_mov_dpp(v[0], 0xB1, 0xF, 0xF, true), n1 = __builtin_amdgcn_mov_dpp(v[1], 0xB1, 0xF, 0xF, true);
      st16(wsr, vo, so, (u32x4){v[0], v[1], n0, n1});
    } else st8(wsr, vo, so, v);
  };
  auto pack4 = [&](f32x4 v) { return (u32x2){canon(cvtpk(v[0], v[1])), canon(cvtpk(v[2], v[3]))}; };
  const u32 a3off = w < 7 ? ((7 * j + w) * 32) * 1024 + l16 : OOB;
  auto a3_load = [&](int t0_, int t1_) {
#pragma unroll
    for (int t = 0; t < 32; ++t) if (t >= t0_ && t < t1_) a3[t] = asbf(ld16(wsr, a3off, WS_W3 + t * 1024));
  };

  auto a3_slot = [&](int k) {
    constexpr int n = 32 - A3_N0;
    a3_load(A3_N0 + k * n / 6, A3_N0 + (k + 1) * n / 6);
  };
  f32x4 cs3p = {0.f, 0.f, 0.f, 0.f};
  auto cs3_acc = [&](int t0_, int t1_) {
#pragma unroll
    for (int t = 0; t < 32; ++t) if (t >= t0_ && t < t1_) cs3p = mfma16(sel, a3[t], cs3p);
  };
  f32x4 hres[2], xp[2], cs1;
  bf16x8 fA[4], fB[4];
  const u32 str = opaque<u32>(L_STAT + (lane >> 1) * STP + (lane & 1) * 32);
  auto back = [&](int half, bool stats, auto&& epi, auto&& hook) {
    bf16x8 p[4];
    f32x4 p0 = {0, 0, 0, 0}, p1 = {0, 0, 0, 0};
#pragma unroll
    for (int q = 0; q < 4; ++q) p[q] = ldsr<bf16x8>(pr + half * 32768 + q * 1024);
    if (stats) { p0 = ldsr<f32x4>(str + half * STH); p1 = ldsr<f32x4>(str + half * STH + 16); }
    hook();
    __builtin_amdgcn_sched_barrier(0);
    f32x4 acc = {0, 0, 0, 0};
#pragma unroll
    for (int q = 0; q < 4; ++q) acc = mfma16(sel, p[q], acc);
    RowStat st = {0.f, 0.f, 0.f};
    if (stats) { const f32x4 p = p0 + p1; st = finish_stat(xor1_add(p[0] + p[2]), xor1_add(p[1] + p[3])); }
    return epi(half, acc, st);
  };
  auto phase = [&](const bf16x8 (&a)[8], u32 src, bool stats, int gb, auto&& first, auto&& epi, auto&& put, auto&& pre, auto&& slotA, auto&& slotB, int sbase) {
    PSTAMP(sbase, 0);
    g_wait(fA, src_of(src, 0));
    STAMP(sbase);
    PSTAMP(sbase, 1);
    slice_gemm(0, a, fA, stats, [&]() {
      if (gb == 0) g_load(fB, src_of(src, 1));
      if (SLOT_POS == 0) slotA();
      first();
    });
    PSTAMP(sbase, 4);
    BAR();
    PSTAMP(sbase, 5);
    const u32x2 vA = back(0, stats, epi, [&]() { if (gb == 1) g_load(fB, src_of(src, 1)); });
    PSTAMP(sbase, 6);
    if (gb == 2) g_load(fB, src_of(src, 1));
    __builtin_amdgcn_sched_barrier(0);
    put(0, vA);
    if (SLOT_POS == 1) slotA();
    __builtin_amdgcn_sched_barrier(0);
    PSTAMP(sbase, 7);
    g_wait(fB, src_of(src, 1));
    STAMP(sbase + 1);
    PSTAMP(sbase, 8);
    slice_gemm(1, a, fB, stats, [&]() { if (SLOT_POS == 0) slotB(); });
    PSTAMP(sbase, 11);
    BAR();
    PSTAMP(sbase, 12);
    const u32x2 vB = back(1, stats, epi, [&]() {
      pre();
    });
    PSTAMP(sbase, 13);
    PSTAMP(sbase, 14);
    __builtin_amdgcn_sched_barrier(0);
    put(1, vB);
    if (SLOT_POS == 1) slotB();
    __builtin_amdgcn_sched_barrier(0);
    PSTAMP(sbase, 15);
  };
  auto prio_late = [&](int p) {
#define PRIO_ASM(P) asm volatile("s_cmp_lt_u32 %0, 4\n\ts_cbranch_scc1 .Lprio%=\n\ts_setprio " #P "\n.Lprio%=:" :: "s"(w) : "scc")
    if (p == 0) PRIO_ASM(0); else if (p == 1) PRIO_ASM(1); else PRIO_ASM(2);
#undef PRIO_ASM
  };
  const u32 a4off = (2 * j * 112 + 14 * w) * 1024 + l16;
  {
    float c[2] = {0.f, 0.f};
#pragma unroll
    for (int t = 0; t < 8; ++t) {
      const u32x4 d = asu(a_1[t]);
#pragma unroll
      for (int e = 0; e < 4; ++e) c[t >> 2] = dot2(d[e], 0x3F803F80u, c[t >> 2]);
    }
    const float c0 = swap32_add(c[0]), c1 = swap32_add(c[1]);
    ldsw<float>(L_CS1P + w * 256 + lane * 4, lane < 32 ? c0 : c1);
  }
  cs3_acc(0, A3_N0);
  __builtin_amdgcn_sched_barrier(0);
  if (O_SLEEP) __builtin_amdgcn_s_sleep(O_SLEEP);
  g_load(fA, src_of(WS_OX, 0));
  g_load(fB, src_of(WS_OX, 1));
  __builtin_amdgcn_sched_barrier(0);
  phase(a_o, WS_OX, false, -1,
        [&]() { BAR(); },
        [&](int half, f32x4 acc, RowStat) {
          hres[half] = acc + (f32x4){bflo(xres[half][0]), bfhi(xres[half][0]), bflo(xres[half][1]), bfhi(xres[half][1])};
          return pack4(hres[half]);
        },
        [&](int half, u32x2 v) { put_at(hvo, hx_so(WS_HX, half), v); },
        [&]() { g_load(fA, src_of(WS_HX, 0)); },
        [&]() {
          if (W4_WARM == 2) {
#pragma unroll
            for (int i = 0; i < 7; ++i) dma1(ws + WS_W4 + i * 1024, (((u32)(b >> 6) & 3u) * 8 + w) * 7168 + j * 229376 + l16, lb + L_DUM1);
          }
          a3_slot(0);
        },
        [&]() { a3_slot(1); }, 10);
  {
    const u32 ca = opaque<u32>(L_CS1P + w * 32 + (lane & 1) * 16);
    f32x4 cp[8];
#pragma unroll
    for (int v = 0; v < 8; ++v) cp[v] = ldsr<f32x4>(ca + v * 256);
    cs1 = opaque(((cp[0] + cp[1]) + (cp[2] + cp[3])) + ((cp[4] + cp[5]) + (cp[6] + cp[7])));
  }
  cs3_acc(A3_N0, A3_N0 + (32 - A3_N0) / 6);
  __builtin_amdgcn_sched_barrier(0);
  STAMP(12);
  MEAS(12);
  DELAY(5);
  const u32 tso[2] = {opaque<u32>(ts_vo(0)), opaque<u32>(ts_vo(1))};
  phase(a_1, WS_HX, true, GB_POS, [&]() { },
        [&](int half, f32x4 acc, RowStat st) {
          const float r2 = __builtin_amdgcn_rsqf(st.var * st.rstd * st.rstd + LN_EPS), rr = st.rstd * r2;
          const f32x4 pre = (acc - st.mu * cs1) * rr;
          const f32x2 g0 = gelu2((f32x2){pre[0], pre[1]}), g1 = gelu2((f32x2){pre[2], pre[3]});
          xp[half] = (hres[half] - st.mu) * st.rstd;
          return (u32x2){canon(cvtpk(g0[0], g0[1])), canon(cvtpk(g1[0], g1[1]))};
        },
        [&](int half, u32x2 v) { put_at(tso[half], hx_so(WS_TS, half) - 32768, v); },
        [&]() { g_load(fA, src_of(WS_TS, 0)); },
        [&]() { a3_slot(2); },
        [&]() { a3_slot(3); dma4_sc1(ws + WS_W4 + 10 * 1024, a4off, lb + L_A4 + w * 8192); }, 13);
  cs3_acc(A3_N0 + (32 - A3_N0) / 6, A3_N0 + 3 * (32 - A3_N0) / 6);
  __builtin_amdgcn_sched_barrier(0);
  STAMP(15);
  phase(a_2, WS_TS, false, GB_POS, [&]() { },
        [&](int half, f32x4 acc, RowStat) {
          const f32x4 x1 = xp[half] + acc;
          ldsw<f32x4>(L_RES + half * 8192 + w * 1024 + l16, hres[half] + x1);
          return pack4(x1);
        },
        [&](int half, u32x2 v) { put_at(hvo, hx_so(WS_X1, half), v); },
        [&]() { dma4_sc1(ws + src_of(WS_X1, 0), gvo, lb + L_P + w * 4096); },
        [&]() { a3_slot(4); dma4_sc1(ws + WS_W4 + 114688 + 10 * 1024, a4off, lb + L_A4 + w * 8192 + 4096); },
        [&]() { a3_slot(5); }, 16);
  cs3_acc(A3_N0 + 3 * (32 - A3_N0) / 6, 32);
  {
    const float c = swap32_add((cs3p[0] + cs3p[1]) + (cs3p[2] + cs3p[3]));
    ldsw<float>(L_CS + w * 128 + r32 * 4, c);
  }
  __builtin_amdgcn_sched_barrier(0);
  STAMP(18);
  MEAS(18);
  DELAY(7);
  bf16x8 a4[10][2];
  auto a4_load = [&](int n) { a4[n >> 1][n & 1] = asbf(ld16(wsr, a4off, WS_W4 + (n & 1) * 114688 + (n >> 1) * 1024)); };
  constexpr int ord[14] = {0, 2, 4, 6, 1, 3, 5, 7, 8, 10, 12, 9, 11, 13};
  u32x4 uf[28];
  auto u_load1 = [&](int k) { uf[k] = ld16(wsr, l16, WS_UX + (blk_of(k / 14) * 112 + w * 14 + ord[k % 14]) * 1024); };
  {
    auto x1_finish = [&](int half) {
#pragma unroll
      for (int t = 0; t < 4; ++t) fA[t] = ldsr<bf16x8>(pr + half * 32768 + t * 1024);
      if (__builtin_expect(unwritten(fA), 0)) {
        do {
          if (timed_out(t0)) break;
          __builtin_amdgcn_s_sleep(1);
          dma4_sc1(ws + src_of(WS_X1, half), gvo, lb + L_P + half * 32768 + w * 4096);
          WAITV(0);
#pragma unroll
          for (int t = 0; t < 4; ++t) fA[t] = ldsr<bf16x8>(pr + half * 32768 + t * 1024);
        } while (unwritten(fA));
      }
      row_stats(half, fA);
    };
    struct UOut { float nmu, k1, k2, ir; f32x4 cs[2]; f32x4 p[4]; u32 up[8]; u32 cb, vo; };
    auto u_stat0 = [&](UOut& u, int half) {
      const u32 l = opaque<u32>(lane), sr = opaque<u32>(L_STAT + half * STH + (l & 31) * STP);
#pragma unroll
      for (int i = 0; i < 4; ++i) u.p[i] = ldsr<f32x4>(sr + 16 * i);
    };
    auto u_stat1 = [&](UOut& u) {
      const f32x4 p = (u.p[0] + u.p[1]) + (u.p[2] + u.p[3]);
      const RowStat st = finish_stat(p[0] + p[2], p[1] + p[3]);
      const float c1 = -2.0f * 0.7978845608028654f * 1.4426950408889634f, c2 = c1 * 0.044715f;
      u.nmu = opaque(-st.mu); u.k1 = opaque(c1 * st.rstd); u.k2 = opaque(c2 * st.rstd * st.rstd * st.rstd); u.ir = opaque(__builtin_amdgcn_sqrtf(st.var + LN_EPS));
    };
    auto u_cs = [&](UOut& u, int a) {
      if (a == 0) {
        const u32 l = opaque<u32>(lane);
        u.cb = opaque<u32>(L_CS + w * 128 + (l >> 5) * 16);
        u.vo = opaque<u32>(w < 7 ? l * 16 : OOB);
      }
      u.cs[a & 1] = ldsr<f32x4>(u.cb + a * 32);
    };
    auto u_store = [&](UOut& u, int half, int p) {
      if (p == 3 || p == 7) {
        const u32 so = WS_UX + (blk_of(half) * 112 + j * 14 + 2 * w) * 1024;
        if (p == 3) st16(wsr, u.vo, so, (u32x4){u.up[0], u.up[1], u.up[2], u.up[3]});
        else st16(wsr, u.vo, so + 1024, (u32x4){u.up[4], u.up[5], u.up[6], u.up[7]});
      }
    };
    auto u_quad = [&](UOut& u, int half, const f32x16& acc, int p) {
      float t[4], e[4];
#pragma unroll
      for (int i = 0; i < 4; ++i) { const int v = 2 * p + i; t[i] = opaque(__builtin_fmaf(u.nmu, u.cs[(v >> 2) & 1][v & 3], acc[v])); }
#pragma unroll
      for (int i = 0; i < 4; ++i) e[i] = __builtin_amdgcn_exp2f(t[i] * __builtin_fmaf(t[i] * t[i], u.k2, u.k1));
#pragma unroll
      for (int i = 0; i < 4; ++i) t[i] = opaque(t[i] * __builtin_amdgcn_rcpf(__builtin_fmaf(e[i], u.ir, u.ir)));
      u.up[p] = canon(cvtpk(t[0], t[1])); u.up[p + 1] = canon(cvtpk(t[2], t[3]));
      u_store(u, half, p + 1);
    };
    auto u_pair = [&](UOut& u, int half, const f32x16& acc, int p) {
      const int a = p >> 1, e = 2 * (p & 1);
      const f32x2 t = (f32x2){u.cs[a & 1][e], u.cs[a & 1][e + 1]} * u.nmu + (f32x2){acc[2 * p], acc[2 * p + 1]};
      const f32x2 ar = t * ((t * t) * u.k2 + u.k1);
      const f32x2 d = (f32x2){__builtin_amdgcn_exp2f(ar[0]), __builtin_amdgcn_exp2f(ar[1])} * u.ir + u.ir;
      const f32x2 g = t * (f32x2){__builtin_amdgcn_rcpf(d[0]), __builtin_amdgcn_rcpf(d[1])};
      u.up[p] = canon(cvtpk(g[0], g[1]));
    };
    WAITV(1);
    x1_finish(0);
    BAR();
    STAMP(19);
    dma4_sc1(ws + src_of(WS_X1, 1), gvo, lb + L_P + 32768 + w * 4096);
    const u32 xb = L_P + (w < 7 ? l16 : 0u);
    f32x16 accA = {}, accB = {};
    bf16x8 bq[4];
    UOut uA, uB;
    if (M_PRIO) __builtin_amdgcn_s_setprio(1);
    {
#pragma unroll
      for (int t = 0; t < 4; ++t) bq[t] = ldsr<bf16x8>(xb + t * 1024);
#pragma unroll
      for (int t = 0; t < 32; ++t) {
        accA = mfma32(a3[t], bq[t & 3], accA);
        if (t + 4 < 32) bq[t & 3] = ldsr<bf16x8>(xb + (t + 4) * 1024);
        if (t == 0) u_stat0(uA, 0);
        if (t == 3) u_stat1(uA);
        if (t == 29) u_cs(uA, 0);
        if (t >= 4 && (t & 3) == 0 && t / 4 - 1 < A4_N0) a4_load(t / 4 - 1);
        __builtin_amdgcn_sched_barrier(0);
      }
    }
    if (M_PRIO) __builtin_amdgcn_s_setprio(0);
    STAMP(20);
    auto gelu_half = [&](UOut& u, int half, const f32x16& acc) {
#pragma unroll
      for (int p = 0; p < 8; p += 2) {
        if (p + 2 < 8) u_cs(u, (p + 2) >> 1);
        constexpr int nr = (8 - UE_P) / 2;
        if (half == 1) {
#pragma unroll
          for (int n = 20 - A4_LATE + p / 2 * A4_LATE / 4; n < 20 - A4_LATE + (p / 2 + 1) * A4_LATE / 4; ++n) a4_load(n);
        }
        if (half == 1 && p >= UE_P) {
#pragma unroll
          for (int i = (p - UE_P) / 2 * U_INIT / (nr ? nr : 1); i < ((p - UE_P) / 2 + 1) * U_INIT / (nr ? nr : 1); ++i) u_load1(i);
        }
        if (GB_PK) { u_pair(u, half, acc, p); u_pair(u, half, acc, p + 1); u_store(u, half, p + 1); }
        else u_quad(u, half, acc, p);
        __builtin_amdgcn_sched_barrier(0);
      }
      if (half == 1 && UE_P >= 8) {
#pragma unroll
        for (int i = 0; i < U_INIT; ++i) u_load1(i);
      }
    };
    if (PQ == 0 || w < 4) gelu_half(uA, 0, accA);
    asm volatile("s_waitcnt vmcnt(%0)" :: "n"(A4_N0 + (PQ ? 0 : 2)) : "memory");
    x1_finish(1);
    BAR();
    STAMP(21);
    if (!M_PRIO) prio_late(Q_PRIO);
    if (PQ && w >= 4) gelu_half(uA, 0, accA);
    if (M_PRIO) __builtin_amdgcn_s_setprio(1);
    {
#pragma unroll
      for (int t = 0; t < 4; ++t) bq[t] = ldsr<bf16x8>(xb + 32768 + t * 1024);
#pragma unroll
      for (int t = 0; t < 32; ++t) {
        if (W3B_TOG && (t % (W3B_TOG ? W3B_TOG : 1)) == 0) prio_late(((t / (W3B_TOG ? W3B_TOG : 1)) & 1) ? 0 : 1);
        accB = mfma32(a3[t], bq[t & 3], accB);
        if (t + 4 < 32) bq[t & 3] = ldsr<bf16x8>(xb + 32768 + (t + 4) * 1024);
        if (t == 0) u_stat0(uB, 1);
        if (t == 3) u_stat1(uB);
        if (t == 29) u_cs(uB, 0);
        if (A4_SPREAD == 0) { const int n = A4_N0 + t - 1; if (t >= 1 && n < 20 - A4_LATE) a4_load(n); }
        else if (A4_SPREAD == 1) { const int n = A4_N0 + (t - 1) / 2; if ((t & 1) && n < 20 - A4_LATE) a4_load(n); }
        else { const int n = A4_N0 + (t - 1) / 4; if ((t & 3) == 1 && n < 20 - A4_LATE) a4_load(n); }
        __builtin_amdgcn_sched_barrier(0);
      }
    }
    if (M_PRIO) __builtin_amdgcn_s_setprio(0); else prio_late(GB_PRIO);
    if (!MID_BAR && lane == 0) asm volatile("ds_add_u32 %0, %1" :: "v"(lb + L_CNT + 8), "v"(1u) : "memory");
    STAMP(22);
    gelu_half(uB, 1, accB);
    STAMP(23);
  }
  {
    if (M_PRIO) { __builtin_amdgcn_s_setprio(1); if (W4_PRIO) prio_late(2); } else prio_late(W4_PRIO);
#pragma unroll
    for (int half = 0; half < 2; ++half) {
      const u32 sa = L_A4 + w * 8192 + opaque<u32>(l16);
      f32x16 acc0 = {}, acc1 = {};
      bf16x8 sq[4][2];
      auto step = [&](int s_, u32x4 u) {
        if (s_ + 2 < 14 && ord[s_ + 2] >= 10) { const int k = ord[s_ + 2] - 10; sq[k][0] = ldsr<bf16x8>(sa + k * 1024); sq[k][1] = ldsr<bf16x8>(sa + 4096 + k * 1024); }
        const int t = ord[s_];
        acc0 = mfma32(t < 10 ? a4[t < 10 ? t : 0][0] : sq[t < 10 ? 0 : t - 10][0], asbf(u), acc0);
        acc1 = mfma32(t < 10 ? a4[t < 10 ? t : 0][1] : sq[t < 10 ? 0 : t - 10][1], asbf(u), acc1);
      };
      u32 m = 0;
#pragma unroll
      for (int s_ = 0; s_ < 14; ++s_) {
        const int k = 14 * half + s_;
        step(s_, uf[k]);
        m = opaque(umax(umax(umax(m, uf[k][0]), uf[k][1]), umax(uf[k][2], uf[k][3])));
        if (k + U_WIN < 28 && k + U_WIN >= U_INIT) u_load1(k + U_WIN);
        __builtin_amdgcn_sched_barrier(0);
      }
      if (__builtin_expect(__any(m == SENT), 0)) {
        u32x4 ug[14];
        bool bad;
        do {
          __builtin_amdgcn_s_sleep(1);
          asm volatile("s_nop 7" ::: "memory");
          const u32 so = WS_UX + (blk_of(half) * 112 + w * 14) * 1024, l = opaque<u32>(l16);
#pragma unroll
          for (int i = 0; i < 14; ++i) regather1(srd, l, so + ord[i] * 1024, ug[i]);
          asm volatile("s_waitcnt vmcnt(0)" : "+v"(ug[0]), "+v"(ug[1]), "+v"(ug[2]), "+v"(ug[3]), "+v"(ug[4]), "+v"(ug[5]), "+v"(ug[6]), "+v"(ug[7]), "+v"(ug[8]), "+v"(ug[9]),
                       "+v"(ug[10]), "+v"(ug[11]), "+v"(ug[12]), "+v"(ug[13]) :: "memory");
          u32 mm = 0;
#pragma unroll
          for (int i = 0; i < 14; ++i) mm = umax(mm, max4(ug[i]));
          bad = __any(mm == SENT);
        } while (bad && !timed_out(t0));
        acc0 = (f32x16){}; acc1 = (f32x16){};
#pragma unroll
        for (int s_ = 0; s_ < 14; ++s_) step(s_, ug[s_]);
      }
      STAMP(24 + half);
      if (!MID_BAR && half == 1) {
        while (__builtin_amdgcn_readfirstlane(*(volatile LAS u32*)((LAS char*)smem + L_CNT + 8)) != 8) {
          if (timed_out(t0)) break;
          __builtin_amdgcn_s_sleep(1);
        }
        FENCE();
      }
      if (OUT_ROWS) {
        const u32 l = opaque<u32>(lane), r = l & 31;
        const u32 pf = L_P + half * 32768 + (r >> 2) * 4096 + (w >> 1) * 1024 + (r & 3) * 256 + ((w ^ (l >> 3)) & 1) * 8;
#pragma unroll
        for (int a = 0; a < 8; ++a) {
          const f32x16& ac = a < 4 ? acc0 : acc1;
          const int e = 4 * (a & 3);
          ldsw<u32x2>(pf + (((2 * a + (l >> 5)) ^ r) & 15) * 16, (u32x2){cvtpk(ac[e], ac[e + 1]), cvtpk(ac[e + 2], ac[e + 3])});
        }
      } else partials(half, acc0, acc1, pw_of(opaque<u32>(lane)));
      if (MID_BAR ? half == 0 || !OUT_EARLY : half == 1) make_sel();
      if (half == 1) STAMP(26);
      constexpr bool FIN2 = !MID_BAR && OUT_ROWS;
      f32x4 fin[2];
      u32 fo = 0;
      if (FIN2 && half == 1) {
        const u32 l = opaque<u32>(lane), r = 4 * w + (l >> 4), g = (l ^ r) & 15, ra = L_RES + (g >> 1) * 1024 + (2 * r + (g & 1)) * 16;
        fin[0] = ldsr<f32x4>(ra); fin[1] = ldsr<f32x4>(ra + 8192);
        fo = r * 1024 + 128 * j + 8 * g;
      }
      if (half == 1 || (MID_BAR && OUT_EARLY)) BAR();
      if (FIN2 && half == 1) {
        const u32 pa = opaque<u32>(L_P + w * 4096 + lane * 16);
        bf16x8 p[2][4];
#pragma unroll
        for (int q = 0; q < 4; ++q) { p[0][q] = ldsr<bf16x8>(pa + q * 1024); p[1][q] = ldsr<bf16x8>(pa + 32768 + q * 1024); }
        __builtin_amdgcn_sched_barrier(0);
#pragma unroll
        for (int q = 0; q < 4; ++q) { fin[0] = mfma16(sel, p[0][q], fin[0]); fin[1] = mfma16(sel, p[1][q], fin[1]); }
#pragma unroll
        for (int hf = 0; hf < 2; ++hf)
          __builtin_amdgcn_raw_buffer_store_b64((u32x2){cvtpk(fin[hf][0], fin[hf][1]), cvtpk(fin[hf][2], fin[hf][3])}, mkrsrc(out, 2048 * 1024), fo + (u32)(blk_of(hf) * 32 * 1024), 0, OUT_AUX < 0 ? 0 : OUT_AUX);
      }
      auto out_half = [&](int hf) {
        const u32 l = opaque<u32>(lane);
        f32x4 v = reduce_at(hf, L_P + w * 4096 + l * 16);
        u32 oo;
        if (OUT_ROWS) {
          const u32 r = 4 * w + (l >> 4), g = (l ^ r) & 15;
          v += ldsr<f32x4>(L_RES + hf * 8192 + (g >> 1) * 1024 + (2 * r + (g & 1)) * 16);
          oo = (u32)((blk_of(hf) * 32 + r) * 1024 + 128 * j + 8 * g);
        } else {
          v += ldsr<f32x4>(L_RES + hf * 8192 + w * 1024 + l * 16);
          oo = (u32)((blk_of(hf) * 32 + (l >> 1)) * 1024 + (64 * j + 8 * w + 4 * (l & 1)) * 2);
        }
        if (OUT_AUX >= 0) __builtin_amdgcn_raw_buffer_store_b64((u32x2){cvtpk(v[0], v[1]), cvtpk(v[2], v[3])}, mkrsrc(out, 2048 * 1024), oo, 0, OUT_AUX < 0 ? 0 : OUT_AUX);
        else *(GAS u32x2*)((char*)out + oo) = (u32x2){cvtpk(v[0], v[1]), cvtpk(v[2], v[3])};
      };
      if (MID_BAR && OUT_EARLY) out_half(half);
      else if (half == 1) { if (!FIN2) { out_half(0); out_half(1); } }
      else if (MID_BAR) BAR();
      FENCE();
    }
  }
  if (MEASP >= 0) {
    const u64 te = __builtin_amdgcn_s_memrealtime();
    u64 d = MEASP == 98 ? te - t0 : tmeas - t0;
    if (MEASP == 0) d = t0 - tA;
    if (MEASP == 99) d = (__builtin_amdgcn_s_memtime() - c0) / 20;
    while (__builtin_amdgcn_s_memrealtime() - te < 4 * d) __builtin_amdgcn_s_sleep(4);
  }
  STAMP(27);
}

extern "C" void kernel_launch(void* const* d_in, const int* in_sizes, int n_in, void* d_out, int out_size, void* d_ws, size_t ws_size, hipStream_t stream) {
  (void)in_sizes; (void)n_in; (void)out_size; (void)ws_size;
  fused_block_kernel<<<256, 512, 0, stream>>>((const u16*)d_in[0], (const u16*)d_in[1], (const u16*)d_in[2], (const u16*)d_in[3], (const u16*)d_in[4],
                                              (const u16*)d_in[6], (const u16*)d_in[7], (const u16*)d_in[8], (const u16*)d_in[9], (u16*)d_out, (char*)d_ws);
}
```
